```python
import torch

def _softmax(x, axis=-1):
    return torch.softmax(x, dim=axis)
def _gelu(x):
    return torch.nn.functional.gelu(x, approximate="tanh")
def _silu(x):
    return torch.nn.functional.silu(x)
def _sigmoid(x):
    return torch.sigmoid(x)
def _layernorm(x, axis=-1, eps=1e-5):
    m = x.mean(dim=axis, keepdim=True)
    v = x.var(dim=axis, keepdim=True, unbiased=False)
    return (x - m) / torch.sqrt(v + eps)
def _scatter_add(tgt, idx, src, axis=0):
    return tgt.clone().index_add_(axis, idx, src)
def _segment_sum(src, seg, n):
    out = torch.zeros((n,) + src.shape[1:], dtype=src.dtype)
    return out.index_add_(0, seg, src)
def _pad(x, pw):
    flat = tuple(v for pair in reversed(pw) for v in pair)
    return torch.nn.functional.pad(x, flat)

kernel_name = 'standalone__paged_block_table_kv_radixattention__sampling'

def setup_inputs():

    _w = (lambda a: (lambda t: t.to(torch.bfloat16) if t.is_floating_point() else t)(torch.as_tensor(a)))
    B, H, Hkv, Dh = 16, 32, 16, 128
    Lk = 4096
    bs = 16
    nb = max(2, Lk // bs)
    P = max(1, nb // 2)
    n_phys = P + B * (nb - P)
    shared = torch.arange(P, dtype=torch.int64)
    priv = P + torch.arange(B * (nb - P), dtype=torch.int64).reshape(B, nb - P)
    bt = torch.cat([torch.tile(shared[None, :], (B, 1)), priv], dim=1)
    ctx = nb * bs - (torch.arange(1, B + 1, dtype=torch.int64) * 2654435761) % max(1, (nb * bs - P * bs) // 2)
    return {
        "q": _w((lambda *s: torch.randn(*s, dtype=torch.bfloat16) * 0.8)(B, H, Dh)),
        "k_cache": _w((lambda *s: torch.randn(*s, dtype=torch.bfloat16) * 0.8)(n_phys, bs, Hkv, Dh)),
        "v_cache": _w((lambda *s: torch.randn(*s, dtype=torch.bfloat16) * 0.05)(n_phys, bs, Hkv, Dh)),
        "block_table": _w(bt.to(torch.int64)),
        "ctx_lens": _w(torch.as_tensor(ctx).to(torch.int64)),
    }

def reference(q, k_cache, v_cache, block_table, ctx_lens):
    B, H, Dh = q.shape
    n_phys, bs, Hkv, _ = k_cache.shape
    nb = block_table.shape[1]
    Lk = nb * bs
    rep = H // Hkv
    k = k_cache[block_table].reshape(B, Lk, Hkv, Dh)
    v = v_cache[block_table].reshape(B, Lk, Hkv, Dh)
    k = (lambda x, n, axis: torch.repeat_interleave(x, n, dim=axis))(k, rep, axis=2)
    v = (lambda x, n, axis: torch.repeat_interleave(x, n, dim=axis))(v, rep, axis=2)
    scores = torch.einsum("bhd,bkhd->bhk", q, k) * (Dh ** -0.5)
    pos = torch.arange(Lk)
    valid = pos[None, :] < ctx_lens[:, None]
    scores = torch.where(valid[:, None, :], scores, -1e9)
    p = _softmax(scores, axis=-1)
    return torch.einsum("bhk,bkhd->bhd", p, v)
```

```cpp
#include <hip/hip_runtime.h>
#include <hip/hip_bf16.h>
#include <cstdio>
#include <cstdint>

using bf16 = __hip_bfloat16;
constexpr int D = 128, PAGE = 16, WAVES = 4, NTHR = WAVES * 64;
constexpr float SCALE = 0.088388347648318440f;
#define ATTN_B 16
#define ATTN_HQ 32
#define ATTN_HKV 16
#define ATTN_CTX 4096
#define ATTN_NSPLIT 0
#define ATTN_COMBINE 0
#define ATTN_KV_NT 1
#define ATTN_PAGED 1
#define ATTN_SQ 1
constexpr int B = ATTN_B, HQ = ATTN_HQ, HKV = ATTN_HKV, CTX = ATTN_CTX, G = HQ / HKV, SQ = ATTN_SQ;
constexpr int GE = G * SQ;
constexpr int GEP = GE <= 4 ? 4 : 8;
constexpr int NBPS = CTX / PAGE;
constexpr int NUM_PHYS = ATTN_PAGED ? B * NBPS + 4 : B * NBPS;
constexpr int nsplit_rule() { int n = 1; while (B * HKV * n < 512 && NBPS / (2 * n) >= 16) n *= 2; return n; }
constexpr int NSPLIT = ATTN_NSPLIT ? ATTN_NSPLIT : nsplit_rule();
constexpr int PPS = NBPS / NSPLIT, PPW = PPS / WAVES;
constexpr int COMBINE = ATTN_COMBINE, KV_NT = ATTN_KV_NT, PAGED = ATTN_PAGED;
constexpr int HPL = GEP / 4;
constexpr int TOK_STRIDE = PAGED ? HKV * D : D;
static_assert(HQ % HKV == 0 && GE >= 1 && GE <= 8, "GE = (HQ/HKV)*SQ in 1..8 (padded to 4 or 8 lane-layout rows); 16 would need acc[128] + x[64] per lane: > 256 VGPRs in this layout");
static_assert(CTX % (PAGE * NSPLIT * WAVES) == 0 && PPW >= 1 && PPW <= 64, "pages split evenly over splits and waves; <= 64 pages per wave (block_table slice in one VGPR)");
constexpr size_t WS_CNT = 0, CNT_BYTES = (size_t)B * HKV * 4, WS_PML = 65536;
constexpr size_t WS_PO = WS_PML + (size_t)B * HQ * SQ * NSPLIT * 2 * 4, WS_END = WS_PO + (size_t)B * HQ * SQ * NSPLIT * D * 4;
constexpr int LDS_P = 0, LDS_MRG = WAVES * 64 * HPL, MRG_REC = 4 + D, LDS_FLAG = LDS_MRG + WAVES * GEP * MRG_REC, LDS_FLOATS = LDS_FLAG + 4;

using bf16x2 = __attribute__((ext_vector_type(2))) __bf16;
using u32x4  = __attribute__((ext_vector_type(4))) unsigned;
using f32x4  = __attribute__((ext_vector_type(4))) float;
using f32x8  = __attribute__((ext_vector_type(8))) float;
__device__ __forceinline__ float dot2(unsigned k, unsigned q, float acc) {
  return __builtin_amdgcn_fdot2_f32_bf16(__builtin_bit_cast(bf16x2, k), __builtin_bit_cast(bf16x2, q), acc, false);
}
__device__ __forceinline__ unsigned cvtpk(float lo, float hi) {
  unsigned r; asm volatile("v_cvt_pk_bf16_f32 %0, %1, %2" : "=v"(r) : "v"(lo), "v"(hi)); return r;
}
template <int CTRL> __device__ __forceinline__ float dpp(float x) {
  return __builtin_bit_cast(float, __builtin_amdgcn_mov_dpp(__builtin_bit_cast(int, x), CTRL, 0xf, 0xf, true));
}
constexpr int XOR1 = 0xB1, XOR2 = 0x4E, XOR7 = 0x141, XOR8 = 0x128, ROR4 = 0x124;
__device__ __forceinline__ float xrow16_max(float x) {
  auto s = __builtin_amdgcn_permlane16_swap(__float_as_uint(x), __float_as_uint(x), false, false);
  x = fmaxf(__uint_as_float(s[0]), __uint_as_float(s[1]));
  auto t = __builtin_amdgcn_permlane32_swap(__float_as_uint(x), __float_as_uint(x), false, false);
  return fmaxf(__uint_as_float(t[0]), __uint_as_float(t[1]));
}
__device__ __forceinline__ float xrow16_sum(float x) {
  auto s = __builtin_amdgcn_permlane16_swap(__float_as_uint(x), __float_as_uint(x), false, false);
  x = __uint_as_float(s[0]) + __uint_as_float(s[1]);
  auto t = __builtin_amdgcn_permlane32_swap(__float_as_uint(x), __float_as_uint(x), false, false);
  return __uint_as_float(t[0]) + __uint_as_float(t[1]);
}
template <typename TIn> struct Stage;
template <> struct Stage<bf16> { using T = u32x4;
  __device__ static __forceinline__ T ld8(const bf16* p) { return KV_NT ? __builtin_nontemporal_load(reinterpret_cast<const u32x4*>(p)) : *reinterpret_cast<const u32x4*>(p); }
  __device__ static __forceinline__ T ld8q(const bf16* p) { return *reinterpret_cast<const u32x4*>(p); }
  __device__ static __forceinline__ u32x4 tobf(T x) { return x; } };
template <> struct Stage<float> { using T = f32x8;
  __device__ static __forceinline__ T ld8(const float* p) { return KV_NT ? __builtin_nontemporal_load(reinterpret_cast<const f32x8*>(p)) : *reinterpret_cast<const f32x8*>(p); }
  __device__ static __forceinline__ T ld8q(const float* p) { return *reinterpret_cast<const f32x8*>(p); }
  __device__ static __forceinline__ u32x4 tobf(T x) { u32x4 w = {cvtpk(x[0], x[1]), cvtpk(x[2], x[3]), cvtpk(x[4], x[5]), cvtpk(x[6], x[7])}; return w; } };

template <typename TIn> struct DecArgs { const TIn* q; const TIn* k; const TIn* v; const int* bt; const int* lens; bf16* out; unsigned char* ws; };

__device__ __forceinline__ void st_partial(float2* p, float2 v) {
  if constexpr (COMBINE == 1) __hip_atomic_store(reinterpret_cast<unsigned long long*>(p), __builtin_bit_cast(unsigned long long, v), __ATOMIC_RELAXED, __HIP_MEMORY_SCOPE_AGENT);
  else *p = v;
}
__device__ __forceinline__ void merge_partials(const float* pml, const float* po, int n, int d0, bf16* o) {
  float M = -INFINITY;
  for (int s = 0; s < n; ++s) M = fmaxf(M, pml[2 * s]);
  float L = 0.f, o0 = 0.f, o1 = 0.f;
  for (int s = 0; s < n; ++s) {
    const float ls = pml[2 * s + 1], w = (ls > 0.f) ? __builtin_amdgcn_exp2f(pml[2 * s] - M) : 0.f;
    const float2 a = *reinterpret_cast<const float2*>(po + (size_t)s * D + d0);
    L = fmaf(w, ls, L); o0 = fmaf(w, a.x, o0); o1 = fmaf(w, a.y, o1);
  }
  const float inv = (L > 0.f) ? 1.f / L : 0.f;
  *reinterpret_cast<unsigned*>(o + d0) = cvtpk(o0 * inv, o1 * inv);
}

template <typename TIn>
__global__ void __launch_bounds__(NTHR) attn_decode_split(DecArgs<TIn> a) {
  __shared__ __attribute__((aligned(16))) float lds[LDS_FLOATS];
  const int s = blockIdx.x, kvh = blockIdx.y, b = blockIdx.z;
  const int tid = threadIdx.x, lane = tid & 63, wave = __builtin_amdgcn_readfirstlane(tid >> 6), r = lane >> 4, c = lane & 15;
  constexpr float C2 = SCALE * 1.4426950408889634f;
  const int len = PAGED ? a.lens[b] : CTX;
  const int npages = (len + PAGE - 1) / PAGE;
  const int pps = (npages + NSPLIT - 1) / NSPLIT;
  const int p0 = s * pps, p1 = min(p0 + pps, npages);
  const int nwp = (p1 > p0 + wave) ? min(PPW, (p1 - p0 - wave + WAVES - 1) / WAVES) : 0;
  int btv = 0;
  if (lane < PPW) { const int pg = min(p0 + wave + 4 * lane, NBPS - 1); btv = PAGED ? a.bt[(size_t)b * NBPS + pg] : pg; }
  u32x4 qpk[GEP];
#pragma unroll
  for (int g = 0; g < GEP; ++g) qpk[g] = g < GE ? Stage<TIn>::tobf(Stage<TIn>::ld8q(a.q + ((size_t)b * HQ * SQ + kvh * GE + g) * D + 8 * c)) : u32x4{0u, 0u, 0u, 0u};
  const size_t loff = (size_t)r * TOK_STRIDE + 8 * c;
#define PAGE_BASE(phys) (PAGED ? (size_t)(phys) * (PAGE * HKV * D) + (size_t)kvh * D : ((size_t)(b * HKV + kvh) * CTX + (size_t)(phys) * PAGE) * D)
#define ISSUE(i_) do { const int phys_ = __builtin_amdgcn_readlane(btv, (i_)); const TIn* kb_ = a.k + PAGE_BASE(phys_) + loff; const TIn* vb_ = a.v + PAGE_BASE(phys_) + loff; \
    _Pragma("unroll") for (int t_ = 0; t_ < 4; ++t_) { Kn[t_] = Stage<TIn>::ld8(kb_ + (size_t)t_ * 4 * TOK_STRIDE); Vn[t_] = Stage<TIn>::ld8(vb_ + (size_t)t_ * 4 * TOK_STRIDE); } } while (0)
  typename Stage<TIn>::T Kn[4], Vn[4];
  float m[HPL], l[HPL], acc[GEP * 8];
#pragma unroll
  for (int j = 0; j < HPL; ++j) { m[j] = -INFINITY; l[j] = 0.f; }
#pragma unroll
  for (int k = 0; k < GEP * 8; ++k) acc[k] = 0.f;
  if (nwp > 0) ISSUE(0);
  for (int i = 0; i < nwp; ++i) {
    u32x4 Kc[4], Vc[4];
#pragma unroll
    for (int t = 0; t < 4; ++t) { Kc[t] = Stage<TIn>::tobf(Kn[t]); Vc[t] = Stage<TIn>::tobf(Vn[t]); }
    const int pg = p0 + wave + 4 * i, rem = len - pg * PAGE;
    if (i + 1 < nwp) ISSUE(i + 1);
    float x[4 * GEP];
#pragma unroll
    for (int t = 0; t < 4; ++t)
#pragma unroll
      for (int g = 0; g < GEP; ++g) { float d = 0.f;
        if (g < GE) {
#pragma unroll
          for (int k = 0; k < 4; ++k) d = dot2(Kc[t][k], qpk[g][k], d); }
        x[t * GEP + g] = d; }
#define TR_STEP(HALF, CTRL, BIT) _Pragma("unroll") for (int h = 0; h < (HALF); ++h) _Pragma("unroll") for (int j = 0; j < HPL; ++j) { \
      const float lo_ = x[h * HPL + j], hi_ = x[(h + (HALF)) * HPL + j]; const float t1_ = lo_ + dpp<CTRL>(lo_), t2_ = hi_ + dpp<CTRL>(hi_); \
      x[h * HPL + j] = (c & (BIT)) ? t2_ : t1_; }
    TR_STEP(8, XOR8, 8) TR_STEP(4, XOR7, 4) TR_STEP(2, XOR2, 2) TR_STEP(1, XOR1, 1)
#undef TR_STEP
    const bool tail = rem < PAGE;
    const bool live = !tail || (r + 4 * (c >> 2)) < rem;
    float p[HPL], alpha[HPL];
#pragma unroll
    for (int j = 0; j < HPL; ++j) {
      const float sc = live ? x[j] * C2 : -INFINITY;
      float pm = sc; pm = fmaxf(pm, dpp<ROR4>(pm)); pm = fmaxf(pm, dpp<XOR8>(pm)); pm = xrow16_max(pm);
      const float mn = fmaxf(m[j], pm), msub = (mn == -INFINITY) ? 0.f : mn;
      alpha[j] = __builtin_amdgcn_exp2f(m[j] - msub); p[j] = __builtin_amdgcn_exp2f(sc - msub);
      l[j] = fmaf(l[j], alpha[j], p[j]); m[j] = mn;
    }
#pragma unroll
    for (int q4 = 0; q4 < 4; ++q4)
#pragma unroll
      for (int j = 0; j < HPL; ++j) { if (q4 * HPL + j >= GE) continue;
        const float al = q4 == 0 ? dpp<0x00>(alpha[j]) : q4 == 1 ? dpp<0x55>(alpha[j]) : q4 == 2 ? dpp<0xAA>(alpha[j]) : dpp<0xFF>(alpha[j]);
#pragma unroll
        for (int k = 0; k < 8; ++k) acc[(q4 * HPL + j) * 8 + k] *= al; }
    float* prow = lds + LDS_P + (wave * 4 + r) * 16 * HPL;
#pragma unroll
    for (int j = 0; j < HPL; ++j) prow[c * HPL + j] = p[j];
    asm volatile("s_waitcnt lgkmcnt(0)" ::: "memory");
    f32x4 pr[GEP];
#pragma unroll
    for (int u = 0; u < GEP; ++u) pr[u] = *reinterpret_cast<const f32x4*>(prow + 4 * u);
    if (tail) {
#pragma unroll
      for (int t = 0; t < 4; ++t) { const bool lv = (r + 4 * t) < rem;
#pragma unroll
        for (int k = 0; k < 4; ++k) Vc[t][k] = lv ? Vc[t][k] : 0u; } }
#pragma unroll
    for (int t = 0; t < 4; ++t) {
      float vf[8];
#pragma unroll
      for (int k = 0; k < 4; ++k) { vf[2 * k] = __builtin_bit_cast(float, Vc[t][k] << 16); vf[2 * k + 1] = __builtin_bit_cast(float, Vc[t][k] & 0xffff0000u); }
#pragma unroll
      for (int g = 0; g < GE; ++g) { const float pg_ = pr[(t * GEP + g) >> 2][(t * GEP + g) & 3];
#pragma unroll
        for (int k = 0; k < 8; ++k) acc[g * 8 + k] = fmaf(pg_, vf[k], acc[g * 8 + k]); } }
  }
#undef ISSUE
#pragma unroll
  for (int k = 0; k < GE * 8; ++k) acc[k] = xrow16_sum(acc[k]);
#pragma unroll
  for (int j = 0; j < HPL; ++j) { float t = l[j]; t += dpp<ROR4>(t); t += dpp<XOR8>(t); l[j] = xrow16_sum(t); }
  float* mrg = lds + LDS_MRG;
  if (r == 0) {
#pragma unroll
    for (int g = 0; g < GE; ++g) {
      float* rec = mrg + (wave * GEP + g) * MRG_REC;
#pragma unroll
      for (int k = 0; k < 8; k += 4) *reinterpret_cast<f32x4*>(rec + 4 + 8 * c + k) = f32x4{acc[g * 8 + k], acc[g * 8 + k + 1], acc[g * 8 + k + 2], acc[g * 8 + k + 3]};
      if (c == g / HPL) { rec[0] = m[g % HPL]; rec[1] = l[g % HPL]; } } }
  __syncthreads();
  float* pml = reinterpret_cast<float*>(a.ws + WS_PML);
  float* po = reinterpret_cast<float*>(a.ws + WS_PO);
#pragma unroll
  for (int jj = 0; jj < HPL; ++jj) {
    const int g = wave + 4 * jj, d0 = 2 * lane;
    if (g >= GE) continue;
    const size_t row = (size_t)b * HQ * SQ + kvh * GE + g;
    float M = -INFINITY;
#pragma unroll
    for (int w = 0; w < WAVES; ++w) M = fmaxf(M, mrg[(w * GEP + g) * MRG_REC]);
    float L = 0.f, o0 = 0.f, o1 = 0.f;
#pragma unroll
    for (int w = 0; w < WAVES; ++w) { const float* rec = mrg + (w * GEP + g) * MRG_REC; const float lw = rec[1];
      const float wt = (lw > 0.f) ? __builtin_amdgcn_exp2f(rec[0] - M) : 0.f;
      L = fmaf(wt, lw, L); o0 = fmaf(wt, rec[4 + d0], o0); o1 = fmaf(wt, rec[4 + d0 + 1], o1); }
    if constexpr (NSPLIT == 1) {
      const float inv = (L > 0.f) ? 1.f / L : 0.f;
      *reinterpret_cast<unsigned*>(a.out + row * D + d0) = cvtpk(o0 * inv, o1 * inv);
    } else {
      const size_t slot = row * NSPLIT + s;
      st_partial(reinterpret_cast<float2*>(po + slot * D + d0), make_float2(o0, o1));
      if (lane == 0) st_partial(reinterpret_cast<float2*>(pml + 2 * slot), make_float2(M, L));
    }
  }
  if constexpr (COMBINE == 1 && NSPLIT > 1) {
    asm volatile("s_waitcnt vmcnt(0)" ::: "memory");
    __syncthreads();
    if (tid == 0) {
      unsigned* cnt = reinterpret_cast<unsigned*>(a.ws + WS_CNT) + b * HKV + kvh;
      const unsigned old = __hip_atomic_fetch_add(cnt, 1u, __ATOMIC_RELAXED, __HIP_MEMORY_SCOPE_AGENT);
      const unsigned last = (old == (unsigned)(NSPLIT - 1)) ? 1u : 0u;
      if (last) __builtin_amdgcn_fence(__ATOMIC_ACQUIRE, "agent");
      reinterpret_cast<unsigned*>(lds + LDS_FLAG)[0] = last;
    }
    __syncthreads();
    if (reinterpret_cast<const unsigned*>(lds + LDS_FLAG)[0] == 0u) return;
#pragma unroll
    for (int jj = 0; jj < HPL; ++jj) { if (wave + 4 * jj >= GE) continue;
      const size_t row = (size_t)b * HQ * SQ + kvh * GE + wave + 4 * jj;
      merge_partials(pml + row * NSPLIT * 2, po + row * NSPLIT * D, NSPLIT, 2 * lane, a.out + row * D);
    }
  }
}

__global__ void __launch_bounds__(64) attn_decode_combine(const float* pml, const float* po, bf16* out) {
  const size_t row = (size_t)blockIdx.y * HQ * SQ + blockIdx.x;
  merge_partials(pml + row * NSPLIT * 2, po + row * NSPLIT * D, NSPLIT, 2 * (threadIdx.x & 63), out + row * D);
}

template <typename TIn>
static void attn_decode_launch(const TIn* q, const TIn* k, const TIn* v, const int* bt, const int* lens, bf16* out, void* ws, hipStream_t stream) {
  DecArgs<TIn> a{q, k, v, bt, lens, out, (unsigned char*)ws};
  if (COMBINE == 1 && NSPLIT > 1 && hipMemsetAsync((char*)ws + WS_CNT, 0, CNT_BYTES, stream) != hipSuccess) { fprintf(stderr, "kernel_launch: memset failed\n"); return; }
  hipLaunchKernelGGL(attn_decode_split<TIn>, dim3(NSPLIT, HKV, B), dim3(NTHR), 0, stream, a);
  if (COMBINE == 0 && NSPLIT > 1) hipLaunchKernelGGL(attn_decode_combine, dim3(HQ * SQ, B), dim3(64), 0, stream, (const float*)((char*)ws + WS_PML), (const float*)((char*)ws + WS_PO), out);
  const hipError_t le = hipPeekAtLastError();
  if (le != hipSuccess) fprintf(stderr, "kernel_launch: launch failed: %s\n", hipGetErrorName(le));
}

extern "C" void kernel_launch(void* const* d_in, const int* in_sizes, int n_in, void* d_out, int out_size, void* d_ws, size_t ws_size, hipStream_t stream) {
  attn_decode_launch<bf16>((const bf16*)d_in[0], (const bf16*)d_in[1], (const bf16*)d_in[2], PAGED ? (const int*)d_in[3] : nullptr, PAGED ? (const int*)d_in[4] : nullptr,
                            (bf16*)d_out, d_ws, stream);
}
```

```cpp
#include <hip/hip_runtime.h>
#include <hip/hip_bf16.h>
#include <stdint.h>

#define NSLOT 4
#define KV_NT 1
#define DIAG 0

constexpr int B = 16, HQ = 32, HKV = 16, D = 128, PAGE = 16, NB = 256, LK = NB * PAGE;
constexpr int NSW = 4, NTHR = (NSW + 1) * 64;
constexpr float C2 = 0.088388347648318440f * 1.4426950408889634f;
constexpr unsigned PAGE_BYTES = PAGE * HKV * D * 2;
constexpr unsigned TOK_BYTES = HKV * D * 2;

constexpr int ROW = 528;
constexpr int L_RING = 0, RING_WAVE = NSLOT * 8192;
constexpr int L_MRG = NSW * RING_WAVE;
constexpr int L_RECX = L_MRG + 32 * ROW;
constexpr int L_RECF = L_RECX + 4 * 2 * ROW;
constexpr int L_MISC = L_RECF + 5 * 2 * ROW;
constexpr int LDS_BYTES = L_MISC + 64;
static_assert(LDS_BYTES <= 160 * 1024, "LDS");
static_assert(32 * ROW >= B * NB * 4, "table image fits the merge buffer");

constexpr size_t W_EPOCH = 0, W_FLAG = 256, FLAG_STRIDE = 128;
constexpr size_t W_PART = 65536;
constexpr unsigned PS_BYTES = 32 * ROW, PX_BYTES = 2 * ROW;
constexpr unsigned W_PX = 256 * PS_BYTES;
constexpr unsigned PART_BYTES = W_PX + 256 * 16 * PX_BYTES;

typedef __bf16 bf16x8 __attribute__((ext_vector_type(8)));
typedef __bf16 bf16x2 __attribute__((ext_vector_type(2)));
typedef short s16x4 __attribute__((ext_vector_type(4)));
typedef float f32x4 __attribute__((ext_vector_type(4)));
typedef float f32x2 __attribute__((ext_vector_type(2)));
typedef unsigned u32x4 __attribute__((ext_vector_type(4)));
typedef unsigned u32x2 __attribute__((ext_vector_type(2)));
typedef int i32x4 __attribute__((ext_vector_type(4)));
#define LAS __attribute__((address_space(3)))
#define GAS __attribute__((address_space(1)))
#define DI __device__ __forceinline__
typedef GAS unsigned long long gu64;
#define RLX_AGENT __ATOMIC_RELAXED, __HIP_MEMORY_SCOPE_AGENT

DI int rfl(int x) { return __builtin_amdgcn_readfirstlane(x); }
DI int rdl(int x, int l) { return __builtin_amdgcn_readlane(x, l); }
template <int CTRL> DI int dpp_i(int oldv, int x) { return __builtin_amdgcn_update_dpp(oldv, x, CTRL, 0xf, 0xf, false); }
DI int row_scan_add(int x) {
  x += dpp_i<0x111>(0, x); x += dpp_i<0x112>(0, x); x += dpp_i<0x114>(0, x); x += dpp_i<0x118>(0, x); return x;
}
DI int row_scan_max(int x) {
  x = max(x, dpp_i<0x111>(0, x)); x = max(x, dpp_i<0x112>(0, x)); x = max(x, dpp_i<0x114>(0, x)); x = max(x, dpp_i<0x118>(0, x)); return x;
}
DI float xrow16_max(float x) {
  auto s = __builtin_amdgcn_permlane16_swap(__float_as_uint(x), __float_as_uint(x), false, false);
  x = fmaxf(__uint_as_float(s[0]), __uint_as_float(s[1]));
  auto t = __builtin_amdgcn_permlane32_swap(__float_as_uint(x), __float_as_uint(x), false, false);
  return fmaxf(__uint_as_float(t[0]), __uint_as_float(t[1]));
}
DI float xrow16_sum(float x) {
  auto s = __builtin_amdgcn_permlane16_swap(__float_as_uint(x), __float_as_uint(x), false, false);
  x = __uint_as_float(s[0]) + __uint_as_float(s[1]);
  auto t = __builtin_amdgcn_permlane32_swap(__float_as_uint(x), __float_as_uint(x), false, false);
  return __uint_as_float(t[0]) + __uint_as_float(t[1]);
}
DI unsigned cvtpk(float lo, float hi) { f32x2 v = {lo, hi}; bf16x2 b = __builtin_convertvector(v, bf16x2); return __builtin_bit_cast(unsigned, b); }
DI float ex2(float x) { return __builtin_amdgcn_exp2f(x); }
DI void wg_barrier() { asm volatile("s_waitcnt lgkmcnt(0)\n\ts_barrier" ::: "memory"); }
template <int N> DI void wait_vm() { asm volatile("s_waitcnt vmcnt(%0)" :: "i"(N) : "memory"); }

#define NTS " nt"
DI void dma4(unsigned m0v, __amdgpu_buffer_rsrc_t rsrc, unsigned soff, unsigned v0, unsigned v1, unsigned v2, unsigned v3) {
  asm volatile("s_waitcnt lgkmcnt(0)\n\t"
               "s_mov_b32 m0, %0\n\t"
               "s_nop 3\n\t"
               "buffer_load_dwordx4 %1, %5, %6 offen" NTS " lds\n\t"
               "buffer_load_dwordx4 %2, %5, %6 offen offset:1024" NTS " lds\n\t"
               "buffer_load_dwordx4 %3, %5, %6 offen offset:2048" NTS " lds\n\t"
               "buffer_load_dwordx4 %4, %5, %6 offen offset:3072" NTS " lds"
               :: "s"(m0v), "v"(v0), "v"(v1), "v"(v2), "v"(v3), "s"(rsrc), "s"(soff) : "memory");
}

struct Args { const bf16x8* q; const void* k; const void* v; const int* bt; const int* lens; unsigned* out; unsigned char* ws; unsigned kv_bytes; unsigned pad; };

DI float mfac(float m, float M) { return (m == -INFINITY) ? 0.f : ex2((m - M) * C2); }

struct Soft { float m, l; f32x4 o[8]; };
DI void soft_reset(Soft& s) { s.m = -INFINITY; s.l = 0.f;
#pragma unroll
  for (int i = 0; i < 8; ++i) s.o[i] = f32x4{0.f, 0.f, 0.f, 0.f}; }

DI s16x4 soft_step(Soft& s, f32x4 x, bool mask, int tok0, int len) {
  if (mask) {
#pragma unroll
    for (int r = 0; r < 4; ++r) x[r] = (tok0 + r < len) ? x[r] : -INFINITY;
  }
  float pm = fmaxf(fmaxf(x[0], x[1]), fmaxf(x[2], x[3]));
  pm = xrow16_max(pm);
  const float mn = fmaxf(s.m, pm);
  const float ms = (mn == -INFINITY) ? 0.f : mn * C2;
  const float alpha = ex2(s.m * C2 - ms);
  float p[4];
#pragma unroll
  for (int r = 0; r < 4; ++r) p[r] = ex2(fmaf(x[r], C2, -ms));
  s.l = fmaf(s.l, alpha, (p[0] + p[1]) + (p[2] + p[3]));
  s.m = mn;
#pragma unroll
  for (int i = 0; i < 8; ++i) s.o[i] *= alpha;
  u32x2 pk = {cvtpk(p[0], p[1]), cvtpk(p[2], p[3])};
  return __builtin_bit_cast(s16x4, pk);
}

__global__ void __launch_bounds__(NTHR, 2) radix_attn(Args a) {
  extern __shared__ __attribute__((aligned(16))) unsigned char lds_raw[];
  LAS unsigned char* lds = (LAS unsigned char*)lds_raw;
  const int tid = threadIdx.x, lane = tid & 63, wave = rfl(tid >> 6);
  const int h = blockIdx.x & 15, w = blockIdx.x >> 4;
  const int c = lane & 15, g = lane >> 4;

  bf16x8 qf[2][4];
  unsigned long long tag = 0;
  int len_raw;
  if (wave < NSW) {
    const i32x4* bt4 = (const i32x4*)a.bt;
    i32x4 rk[4];
#pragma unroll
    for (int k = 0; k < 4; ++k) rk[k] = bt4[(4 * wave + k) * (NB / 4) + lane];
    const i32x4 r0 = bt4[lane];
    len_raw = a.lens[c];
#pragma unroll
    for (int cg = 0; cg < 2; ++cg)
#pragma unroll
      for (int s = 0; s < 4; ++s) { const int r = 16 * cg + c; qf[cg][s] = a.q[(((r >> 1) * HQ + 2 * h + (r & 1)) * D + 32 * s + 8 * g) / 8]; }
    int first = NB;
#pragma unroll
    for (int e = 0; e < 4; ++e) {
      const bool mm = (rk[0][e] != r0[e]) | (rk[1][e] != r0[e]) | (rk[2][e] != r0[e]) | (rk[3][e] != r0[e]);
      const unsigned long long bal = __ballot(mm);
      const int f = bal ? 4 * (int)__builtin_ctzll(bal) + e : NB;
      first = min(first, f);
    }
#pragma unroll
    for (int k = 0; k < 4; ++k) *(LAS i32x4*)(lds + L_MRG + ((4 * wave + k) * NB + 4 * lane) * 4) = rk[k];
    if (lane == 0) *(LAS int*)(lds + L_MISC + 4 * wave) = first;
  } else {
    len_raw = a.lens[c];
    tag = __hip_atomic_load((gu64*)(a.ws + W_EPOCH), RLX_AGENT) + 1ull;
#pragma unroll
    for (int cg = 0; cg < 2; ++cg)
#pragma unroll
      for (int s = 0; s < 4; ++s) qf[cg][s] = bf16x8{};
  }
  wg_barrier();
  const i32x4 pw4 = *(LAS i32x4*)(lds + L_MISC);
  const int P = rfl(min(min(pw4[0], pw4[1]), min(pw4[2], pw4[3])));

  const bool all_masked = len_raw <= 0;
  const int len = all_masked ? LK : min(len_raw, LK);
  const int np = (len + PAGE - 1) >> 4;
  const int Pe = min(P, rdl(row_scan_max(np), 15));
  const int n = max(np - Pe, 0);
  const int W = Pe + rdl(row_scan_add(n), 15);
  const int T = (W >> 4) + (c < (W & 15) ? 1 : 0);
  const int own = min(n, T), exc = n - own, spare = T - own;
  const int S = row_scan_add(spare) - spare;
  const int E = Pe + row_scan_add(exc) - exc;
  const int minlen = LK - rdl(row_scan_max(LK - len), 15);
  const int S_w = rdl(S, w), spare_w = rdl(spare, w), T_w = rdl(T, w);
  const int ns = max(0, min(S_w + spare_w, Pe) - S_w);
  const int lo = max(S_w, E), hi = min(S_w + spare_w, E + exc);
  const unsigned xmask = (unsigned)(__ballot(hi > lo) & 0xFFFFull);

  const __amdgpu_buffer_rsrc_t prsrc = __builtin_amdgcn_make_buffer_rsrc(a.ws + W_PART, 0, PART_BYTES, 0x00020000);

  if (wave < NSW) {
    unsigned vpage;
    {
      const int i = 4 * lane + wave, x = S_w + i;
      int row = w, lp = Pe + (i - spare_w);
      if (i < spare_w) { row = 0; lp = x; }
      for (unsigned m = xmask; m; m &= m - 1) {
        const int b = __builtin_ctz(m);
        const int lo_b = rdl(lo, b), hi_b = rdl(hi, b), off_b = rdl(Pe + own - E, b);
        if (i < spare_w && x >= lo_b && x < hi_b) { row = b; lp = x + off_b; }
      }
      const bool valid = i < T_w;
      const int phys = *(LAS int*)(lds + L_MRG + (valid ? (row * NB + lp) * 4 : 0));
      vpage = (unsigned)phys * PAGE_BYTES + (unsigned)h * (D * 2);
    }
    const int nw = (T_w - wave + 3) >> 2;
    {
      const bool z0 = __shfl((int)all_masked, c >> 1) != 0, z1 = __shfl((int)all_masked, 8 + (c >> 1)) != 0;
#pragma unroll
      for (int s = 0; s < 4; ++s) {
        u32x4 t0 = __builtin_bit_cast(u32x4, qf[0][s]), t1 = __builtin_bit_cast(u32x4, qf[1][s]);
#pragma unroll
        for (int e = 0; e < 4; ++e) { t0[e] = z0 ? 0u : t0[e]; t1[e] = z1 ? 0u : t1[e]; }
        asm volatile("" : "+v"(t0), "+v"(t1));
        qf[0][s] = __builtin_bit_cast(bf16x8, t0); qf[1][s] = __builtin_bit_cast(bf16x8, t1);
      }
    }
    const int len0 = __shfl(len, c >> 1), len1 = __shfl(len, 8 + (c >> 1));
    unsigned vk[4], vv[4];
#pragma unroll
    for (int i = 0; i < 4; ++i) { const int t = 4 * i + g;
      vk[i] = t * TOK_BYTES + ((c ^ t) << 4) - 1024 * i;
      vv[i] = t * TOK_BYTES + ((c ^ ((t & 7) << 1)) << 4) - 1024 * i; }
    const unsigned ring = (unsigned)(uintptr_t)(lds + L_RING + wave * RING_WAVE);
    const unsigned ka0 = c * 256 + ((g ^ c) << 4);
    const int tv = 4 * g + (c >> 2);
    const unsigned va0 = 4096 + tv * 256 + ((tv & 7) << 5) + (c & 3) * 8;
    auto issue_k = [&](int j, int slot) {
      const __amdgpu_buffer_rsrc_t r = __builtin_amdgcn_make_buffer_rsrc((void*)a.k, 0, j < nw ? a.kv_bytes : 0u, 0x00020000);
      dma4(ring + slot * 8192, r, (unsigned)rdl((int)vpage, j & 63), vk[0], vk[1], vk[2], vk[3]); };
    auto issue_v = [&](int j, int slot) {
      const __amdgpu_buffer_rsrc_t r = __builtin_amdgcn_make_buffer_rsrc((void*)a.v, 0, j < nw ? a.kv_bytes : 0u, 0x00020000);
      dma4(ring + slot * 8192 + 4096, r, (unsigned)rdl((int)vpage, j & 63), vv[0], vv[1], vv[2], vv[3]); };
    wg_barrier();
#pragma unroll
    for (int s = 0; s < NSLOT; ++s) { issue_k(s, s); issue_v(s, s); }

    int j = 0, slot = 0;
    Soft s0, s1;
    auto page = [&](auto SHc, const bf16x8 (&qa)[4], const bf16x8 (&qb)[4], int la, int lb, int t0, bool mask) {
      constexpr bool SH = decltype(SHc)::value;
      LAS unsigned char* sl = (LAS unsigned char*)(uintptr_t)(ring + slot * 8192);
      wait_vm<8 * NSLOT - 4>();
      bf16x8 kf[4];
#pragma unroll
      for (int s = 0; s < 4; ++s) kf[s] = *(LAS bf16x8*)(sl + (ka0 ^ (s << 6)));
      f32x4 xa = {0.f, 0.f, 0.f, 0.f}, xb = {0.f, 0.f, 0.f, 0.f};
#pragma unroll
      for (int s = 0; s < 4; ++s) { xa = __builtin_amdgcn_mfma_f32_16x16x32_bf16(kf[s], qa[s], xa, 0, 0, 0);
        if (SH) xb = __builtin_amdgcn_mfma_f32_16x16x32_bf16(kf[s], qb[s], xb, 0, 0, 0); }
      issue_k(j + NSLOT, slot);
      const s16x4 pa = soft_step(s0, xa, mask, t0 + 4 * g, la);
      s16x4 pb; if (SH) pb = soft_step(s1, xb, mask, t0 + 4 * g, lb);
      wait_vm<8 * NSLOT - 4>();
      s16x4 vf[8];
#pragma unroll
      for (int blk = 0; blk < 8; ++blk) vf[blk] = __builtin_amdgcn_ds_read_tr16_b64_v4i16((LAS s16x4*)(sl + (va0 ^ (blk << 5))));
#pragma unroll
      for (int blk = 0; blk < 8; ++blk) { s0.o[blk] = __builtin_amdgcn_mfma_f32_16x16x16bf16_1k(vf[blk], pa, s0.o[blk], 0, 0, 0);
        if (SH) s1.o[blk] = __builtin_amdgcn_mfma_f32_16x16x16bf16_1k(vf[blk], pb, s1.o[blk], 0, 0, 0); }
      issue_v(j + NSLOT, slot);
      ++j; slot = (slot + 1 == NSLOT) ? 0 : slot + 1;
    };
    auto private_piece = [&](int b, int i0, int i1, int lp0, int recbase) {
      bf16x8 qa[4];
      if (b < 8) {
#pragma unroll
        for (int s = 0; s < 4; ++s) qa[s] = qf[0][s];
      } else {
#pragma unroll
        for (int s = 0; s < 4; ++s) qa[s] = qf[1][s];
      }
      const int lb = rdl(len, b);
      soft_reset(s0);
      for (int i = 4 * j + wave; i < i1; i += 4) { const int t0 = (lp0 + i - i0) * PAGE; page(std::false_type{}, qa, qa, lb, lb, t0, t0 + PAGE > lb); }
      return lb;
    };
    auto write_rec = [&](int b, int recbase) {
      const float lt = xrow16_sum(s0.l);
      if ((c >> 1) == (b & 7)) {
        LAS unsigned char* r = lds + recbase + (wave * 2 + (c & 1)) * ROW;
#pragma unroll
        for (int blk = 0; blk < 8; ++blk) *(LAS f32x4*)(r + blk * 64 + g * 16) = s0.o[blk];
        if (g == 0) *(LAS f32x2*)(r + 512) = f32x2{s0.m, lt};
      }
    };
    if (ns > 0) {
      soft_reset(s0); soft_reset(s1);
      for (int i = wave; i < ns; i += 4) { const int t0 = (S_w + i) * PAGE; page(std::true_type{}, qf[0], qf[1], len0, len1, t0, t0 + PAGE > minlen); }
      s0.l = xrow16_sum(s0.l); s1.l = xrow16_sum(s1.l);
      wg_barrier();
      for (int step = 0; step < NSW; ++step) {
        if (step == wave) {
#pragma unroll
          for (int cg = 0; cg < 2; ++cg) { Soft& s = cg ? s1 : s0;
            LAS unsigned char* r = lds + L_MRG + (16 * cg + c) * ROW;
            if (step > 0) {
              const f32x2 ml = *(LAS f32x2*)(r + 512);
              const float M = fmaxf(ml[0], s.m), f0 = mfac(ml[0], M), f1 = mfac(s.m, M);
#pragma unroll
              for (int blk = 0; blk < 8; ++blk) s.o[blk] = *(LAS f32x4*)(r + blk * 64 + g * 16) * f0 + s.o[blk] * f1;
              s.l = ml[1] * f0 + s.l * f1; s.m = M;
            }
#pragma unroll
            for (int blk = 0; blk < 8; ++blk) *(LAS f32x4*)(r + blk * 64 + g * 16) = s.o[blk];
            asm volatile("s_waitcnt lgkmcnt(0)" ::: "memory");
            if (g == 0) *(LAS f32x2*)(r + 512) = f32x2{s.m, s.l};
          }
        }
        wg_barrier();
      }
    }
    for (unsigned m = xmask; m; m &= m - 1) {
      const int b = __builtin_ctz(m);
      const int lo_b = rdl(lo, b), hi_b = rdl(hi, b), off_b = rdl(Pe + own - E, b);
      private_piece(b, lo_b - S_w, hi_b - S_w, lo_b + off_b, L_RECX);
      wg_barrier();
      write_rec(b, L_RECX);
      wg_barrier();
    }
    private_piece(w, spare_w, T_w, Pe, L_RECF);
    write_rec(w, L_RECF);
    wait_vm<0>();
    wg_barrier();
    if (tid < 128) {
      const int r = tid >> 6, d0 = 2 * (tid & 63);
      float M = -INFINITY; f32x2 ml[5];
#pragma unroll
      for (int k = 0; k < 5; ++k) { ml[k] = *(LAS f32x2*)(lds + L_RECF + (k * 2 + r) * ROW + 512); M = fmaxf(M, ml[k][0]); }
      float L = 0.f, o0 = 0.f, o1 = 0.f;
#pragma unroll
      for (int k = 0; k < 5; ++k) { const float f = mfac(ml[k][0], M); const f32x2 o = *(LAS f32x2*)(lds + L_RECF + (k * 2 + r) * ROW + d0 * 4);
        L = fmaf(f, ml[k][1], L); o0 = fmaf(f, o[0], o0); o1 = fmaf(f, o[1], o1); }
      const float inv = 1.f / L;
      a.out[((w * HQ + 2 * h + r) * D + d0) >> 1] = cvtpk(o0 * inv, o1 * inv);
    }
  } else {
    wg_barrier();
    const unsigned blk = blockIdx.x;
    if (ns > 0) {
      wg_barrier();
      for (int step = 0; step < NSW; ++step) wg_barrier();
#pragma unroll 4
      for (int k = 0; k < 16; ++k) { const int off = (2 * k + (lane >> 5)) * ROW + (lane & 31) * 16;
        const u32x4 v = *(LAS u32x4*)(lds + L_MRG + off);
        __builtin_amdgcn_raw_buffer_store_b128(v, prsrc, off, blk * PS_BYTES, 16); }
      if (lane < 32) { const u32x2 v = *(LAS u32x2*)(lds + L_MRG + lane * ROW + 512);
        __builtin_amdgcn_raw_buffer_store_b64(v, prsrc, lane * ROW + 512, blk * PS_BYTES, 16); }
    }
    for (unsigned m = xmask; m; m &= m - 1) {
      const int b = __builtin_ctz(m);
      wg_barrier();
      wg_barrier();
      const int r = lane >> 5, ch = lane & 31;
      float M = -INFINITY; f32x2 ml[4];
#pragma unroll
      for (int k = 0; k < 4; ++k) { ml[k] = *(LAS f32x2*)(lds + L_RECX + (k * 2 + r) * ROW + 512); M = fmaxf(M, ml[k][0]); }
      float L = 0.f; f32x4 o = {0.f, 0.f, 0.f, 0.f};
#pragma unroll
      for (int k = 0; k < 4; ++k) { const float f = mfac(ml[k][0], M); L = fmaf(f, ml[k][1], L); o += *(LAS f32x4*)(lds + L_RECX + (k * 2 + r) * ROW + ch * 16) * f; }
      const unsigned so = W_PX + (blk * 16 + b) * PX_BYTES;
      __builtin_amdgcn_raw_buffer_store_b128(__builtin_bit_cast(u32x4, o), prsrc, r * ROW + ch * 16, so, 16);
      if (ch == 0) __builtin_amdgcn_raw_buffer_store_b64(__builtin_bit_cast(u32x2, f32x2{M, L}), prsrc, r * ROW + 512, so, 16);
    }
    asm volatile("s_waitcnt vmcnt(0)" ::: "memory");
    if (lane == 0) __hip_atomic_store((gu64*)(a.ws + W_FLAG + blk * FLAG_STRIDE), tag, RLX_AGENT);
    const int E_w = rdl(E, w), exc_w = rdl(exc, w);
    const bool sh_ov = spare > 0 && S < Pe;
    const bool ex_ov = min(S + spare, E_w + exc_w) > max(S, E_w);
    unsigned cmask = (unsigned)(__ballot(sh_ov) & 0xFFFFull) | ((unsigned)(__ballot(ex_ov) & 0xFFFFull) << 16);
    {
      const bool need = lane < 16 && (sh_ov || ex_ov);
      gu64* f = (gu64*)(a.ws + W_FLAG + (size_t)(c * 16 + h) * FLAG_STRIDE);
      for (unsigned spins = 0; spins < (1u << 20); ++spins) {
        const bool ok = !need || __hip_atomic_load(f, RLX_AGENT) == tag;
        if (__all(ok)) break;
        __builtin_amdgcn_s_sleep(8);
      }
      asm volatile("" ::: "memory");
    }
    {
      const int r = lane >> 5, ch = lane & 31;
      float M = -INFINITY, L = 0.f; f32x4 o = {0.f, 0.f, 0.f, 0.f};
      constexpr int GB = 10;
      while (cmask) {
        u32x4 po[GB]; u32x2 pml[GB]; bool pv[GB];
#pragma unroll
        for (int k = 0; k < GB; ++k) {
          pv[k] = cmask != 0;
          const int id = pv[k] ? __builtin_ctz(cmask) : 0; cmask &= cmask - 1;
          const unsigned so = id < 16 ? ((unsigned)(id * 16 + h) * PS_BYTES + (unsigned)(2 * w) * ROW) : (W_PX + ((unsigned)((id - 16) * 16 + h) * 16 + w) * PX_BYTES);
          const unsigned so2 = pv[k] ? so : PART_BYTES;
          po[k] = __builtin_amdgcn_raw_buffer_load_b128(prsrc, r * ROW + ch * 16 + so2, 0, 16);
          pml[k] = __builtin_amdgcn_raw_buffer_load_b64(prsrc, r * ROW + 512 + so2, 0, 16);
        }
#pragma unroll
        for (int k = 0; k < GB; ++k) {
          const float mk = pv[k] ? __uint_as_float(pml[k][0]) : -INFINITY, lk = __uint_as_float(pml[k][1]);
          const float Mn = fmaxf(M, mk), f0 = mfac(M, Mn), f1 = mfac(mk, Mn);
          o = o * f0 + __builtin_bit_cast(f32x4, po[k]) * f1; L = L * f0 + lk * f1; M = Mn;
        }
      }
      LAS unsigned char* rec = lds + L_RECF + (4 * 2 + r) * ROW;
      *(LAS f32x4*)(rec + ch * 16) = o;
      if (ch == 0) *(LAS f32x2*)(rec + 512) = f32x2{M, L};
    }
    wg_barrier();
    if (blk == 0) {
      for (unsigned spins = 0; spins < (1u << 20); ++spins) {
        bool ok = true;
#pragma unroll
        for (int k = 0; k < 4; ++k) ok &= __hip_atomic_load((gu64*)(a.ws + W_FLAG + (size_t)(k * 64 + lane) * FLAG_STRIDE), RLX_AGENT) == tag;
        if (__all(ok)) break;
        __builtin_amdgcn_s_sleep(8);
      }
      if (lane == 0) __hip_atomic_store((gu64*)(a.ws + W_EPOCH), tag, RLX_AGENT);
    }
  }
}

extern "C" void kernel_launch(void* const* d_in, const int* in_sizes, int n_in, void* d_out, int out_size, void* d_ws, size_t ws_size, hipStream_t stream) {
  Args a;
  a.q = (const bf16x8*)d_in[0]; a.k = d_in[1]; a.v = d_in[2]; a.bt = (const int*)d_in[3]; a.lens = (const int*)d_in[4];
  a.out = (unsigned*)d_out; a.ws = (unsigned char*)d_ws; a.kv_bytes = (unsigned)in_sizes[1] * 2u; a.pad = 0;
  hipFuncSetAttribute((const void*)radix_attn, hipFuncAttributeMaxDynamicSharedMemorySize, LDS_BYTES);
  hipLaunchKernelGGL(radix_attn, dim3(256), dim3(NTHR), LDS_BYTES, stream, a);
}
```

```cpp
#define MAPSWAP 1
#define INFL 20
#include <hip/hip_runtime.h>
#include <hip/hip_bf16.h>
#include <stdint.h>

#define NSLOT 16
#define KV_NT 1
#define WARM 1
#define DIAG 0

constexpr int B = 16, HQ = 32, HKV = 16, D = 128, PAGE = 16, NB = 256, LK = NB * PAGE;
constexpr int NSW = 4, NTHR = (NSW + 2) * 64;
constexpr float C2 = 0.088388347648318440f * 1.4426950408889634f;
constexpr unsigned PAGE_BYTES = PAGE * HKV * D * 2;
constexpr unsigned TOK_BYTES = HKV * D * 2;

constexpr int ROW = 528;
constexpr int L_RING = 0;
constexpr int L_MRG = NSLOT * 8192;
constexpr int L_RECX = L_MRG + 32 * ROW;
constexpr int L_RECF = L_RECX + 4 * 2 * ROW;
constexpr int L_MISC = L_RECF + 5 * 2 * ROW;
constexpr int C_LANDED = 0, C_REL = 4, C_CSTEP = 20, C_RCNT = 24, C_RFREE = 28, C_P = 32;
constexpr int LDS_BYTES = L_MISC + 64;
static_assert(NSLOT % 4 == 0 && INFL % 4 == 0 && (NSLOT & (NSLOT - 1)) == 0, "ring geometry");
static_assert(LDS_BYTES <= 160 * 1024, "LDS");

constexpr size_t W_EPOCH = 0, W_FLAG = 256, FLAG_STRIDE = 128;
constexpr size_t W_PART = 65536;
constexpr unsigned PS_BYTES = 32 * ROW, PX_BYTES = 2 * ROW;
constexpr unsigned W_PX = 256 * PS_BYTES;
constexpr unsigned PART_BYTES = W_PX + 256 * 16 * PX_BYTES;
#define STAMP(k) do {} while (0)

typedef __bf16 bf16x8 __attribute__((ext_vector_type(8)));
typedef __bf16 bf16x2 __attribute__((ext_vector_type(2)));
typedef short s16x4 __attribute__((ext_vector_type(4)));
typedef float f32x4 __attribute__((ext_vector_type(4)));
typedef float f32x2 __attribute__((ext_vector_type(2)));
typedef unsigned u32x4 __attribute__((ext_vector_type(4)));
typedef unsigned u32x2 __attribute__((ext_vector_type(2)));
typedef int i32x4 __attribute__((ext_vector_type(4)));
#define LAS __attribute__((address_space(3)))
#define GAS __attribute__((address_space(1)))
#define DI __device__ __forceinline__
typedef GAS unsigned long long gu64;
#define RLX_AGENT __ATOMIC_RELAXED, __HIP_MEMORY_SCOPE_AGENT

DI int rfl(int x) { return __builtin_amdgcn_readfirstlane(x); }
DI int rdl(int x, int l) { return __builtin_amdgcn_readlane(x, l); }
template <int CTRL> DI int dpp_i(int oldv, int x) { return __builtin_amdgcn_update_dpp(oldv, x, CTRL, 0xf, 0xf, false); }
DI int row_scan_add(int x) {
  x += dpp_i<0x111>(0, x); x += dpp_i<0x112>(0, x); x += dpp_i<0x114>(0, x); x += dpp_i<0x118>(0, x); return x;
}
DI int row_scan_max(int x) {
  x = max(x, dpp_i<0x111>(0, x)); x = max(x, dpp_i<0x112>(0, x)); x = max(x, dpp_i<0x114>(0, x)); x = max(x, dpp_i<0x118>(0, x)); return x;
}
DI float xrow16_max(float x) {
  auto s = __builtin_amdgcn_permlane16_swap(__float_as_uint(x), __float_as_uint(x), false, false);
  x = fmaxf(__uint_as_float(s[0]), __uint_as_float(s[1]));
  auto t = __builtin_amdgcn_permlane32_swap(__float_as_uint(x), __float_as_uint(x), false, false);
  return fmaxf(__uint_as_float(t[0]), __uint_as_float(t[1]));
}
DI float xrow16_sum(float x) {
  auto s = __builtin_amdgcn_permlane16_swap(__float_as_uint(x), __float_as_uint(x), false, false);
  x = __uint_as_float(s[0]) + __uint_as_float(s[1]);
  auto t = __builtin_amdgcn_permlane32_swap(__float_as_uint(x), __float_as_uint(x), false, false);
  return __uint_as_float(t[0]) + __uint_as_float(t[1]);
}
DI unsigned cvtpk(float lo, float hi) { f32x2 v = {lo, hi}; bf16x2 b = __builtin_convertvector(v, bf16x2); return __builtin_bit_cast(unsigned, b); }
DI float ex2(float x) { return __builtin_amdgcn_exp2f(x); }
DI void wg_barrier() { asm volatile("s_waitcnt lgkmcnt(0)\n\ts_barrier" ::: "memory"); }
template <int N> DI void wait_vm() { asm volatile("s_waitcnt vmcnt(%0)" :: "i"(N) : "memory"); }

#define NTS " nt"
DI void dma4(unsigned m0v, __amdgpu_buffer_rsrc_t rsrc, unsigned soff, unsigned v0, unsigned v1, unsigned v2, unsigned v3) {
  asm volatile("s_mov_b32 m0, %0\n\t"
               "s_nop 4\n\t"
               "buffer_load_dwordx4 %1, %5, %6 offen" NTS " lds\n\t"
               "buffer_load_dwordx4 %2, %5, %6 offen offset:1024" NTS " lds\n\t"
               "buffer_load_dwordx4 %3, %5, %6 offen offset:2048" NTS " lds\n\t"
               "buffer_load_dwordx4 %4, %5, %6 offen offset:3072" NTS " lds"
               :: "s"(m0v), "v"(v0), "v"(v1), "v"(v2), "v"(v3), "s"(rsrc), "s"(soff) : "memory");
}

struct Args { const bf16x8* q; const void* k; const void* v; const int* bt; const int* lens; unsigned* out; unsigned char* ws; unsigned kv_bytes; unsigned pad; };

DI float mfac(float m, float M) { return (m == -INFINITY) ? 0.f : ex2((m - M) * C2); }

struct Soft { float m, l; f32x4 o[8]; };
DI void soft_reset(Soft& s) { s.m = -INFINITY; s.l = 0.f;
#pragma unroll
  for (int i = 0; i < 8; ++i) s.o[i] = f32x4{0.f, 0.f, 0.f, 0.f}; }

DI s16x4 soft_step(Soft& s, f32x4 x, bool mask, int tok0, int len) {
  if (mask) {
#pragma unroll
    for (int r = 0; r < 4; ++r) x[r] = (tok0 + r < len) ? x[r] : -INFINITY;
  }
  float pm = fmaxf(fmaxf(x[0], x[1]), fmaxf(x[2], x[3]));
  pm = xrow16_max(pm);
  const float mn = fmaxf(s.m, pm);
  const float ms = (mn == -INFINITY) ? 0.f : mn * C2;
  const float alpha = ex2(s.m * C2 - ms);
  float p[4];
#pragma unroll
  for (int r = 0; r < 4; ++r) p[r] = ex2(fmaf(x[r], C2, -ms));
  s.l = fmaf(s.l, alpha, (p[0] + p[1]) + (p[2] + p[3]));
  s.m = mn;
#pragma unroll
  for (int i = 0; i < 8; ++i) s.o[i] *= alpha;
  u32x2 pk = {cvtpk(p[0], p[1]), cvtpk(p[2], p[3])};
  return __builtin_bit_cast(s16x4, pk);
}

DI int lds_ld(LAS unsigned char* p) { return __hip_atomic_load((LAS int*)p, __ATOMIC_RELAXED, __HIP_MEMORY_SCOPE_WORKGROUP); }
DI void lds_st(LAS unsigned char* p, int v) { __hip_atomic_store((LAS int*)p, v, __ATOMIC_RELAXED, __HIP_MEMORY_SCOPE_WORKGROUP); }
DI int lds_wait_ge(LAS unsigned char* p, int need) {
  int v;
  for (unsigned spins = 0;; ++spins) { v = rfl(lds_ld(p)); if (v >= need || spins > (1u << 24)) break; __builtin_amdgcn_s_sleep(1); }
  asm volatile("" ::: "memory");
  return v;
}
DI void lds_drain() { asm volatile("s_waitcnt lgkmcnt(0)" ::: "memory"); }

__global__ void __launch_bounds__(NTHR, 2) radix_attn(Args a) {
  extern __shared__ __attribute__((aligned(16))) unsigned char lds_raw[];
  LAS unsigned char* lds = (LAS unsigned char*)lds_raw;
  LAS unsigned char* ctl = lds + L_MISC;
  const int tid = threadIdx.x, lane = tid & 63, wave = rfl(tid >> 6);
  const int w = blockIdx.x & 15, h = blockIdx.x >> 4;
  const int c = lane & 15, g = lane >> 4;
  const int len_raw = a.lens[c];
  int P;
  bf16x8 qf[2][4];
  unsigned long long tag = 0;
  if (wave == 0) {
    const i32x4* bt4 = (const i32x4*)a.bt;
    i32x4 rk[16];
#pragma unroll
    for (int k = 0; k < 16; ++k) rk[k] = bt4[k * (NB / 4) + lane];
    i32x4 acc = rk[1] ^ rk[0];
#pragma unroll
    for (int k = 2; k < 16; ++k) acc |= rk[k] ^ rk[0];
    P = NB;
#pragma unroll
    for (int e = 0; e < 4; ++e) {
      const unsigned long long bal = __ballot(acc[e] != 0);
      P = min(P, bal ? 4 * (int)__builtin_ctzll(bal) + e : NB);
    }
    if (lane == 0) *(LAS int*)(ctl + C_P) = P;
    wg_barrier();
  } else {
    if (wave <= NSW) {
      __builtin_amdgcn_s_sleep(6);
#pragma unroll
      for (int cg = 0; cg < 2; ++cg)
#pragma unroll
        for (int s = 0; s < 4; ++s) { const int r = 16 * cg + c; qf[cg][s] = a.q[(((r >> 1) * HQ + 2 * h + (r & 1)) * D + 32 * s + 8 * g) / 8]; }
    } else {
      tag = __hip_atomic_load((gu64*)(a.ws + W_EPOCH), RLX_AGENT) + 1ull;
      if (lane < 8) *(LAS int*)(ctl + 4 * lane) = 0;
#pragma unroll
      for (int cg = 0; cg < 2; ++cg)
#pragma unroll
        for (int s = 0; s < 4; ++s) qf[cg][s] = bf16x8{};
    }
    wg_barrier();
    P = rfl(*(LAS int*)(ctl + C_P));
  }
  STAMP(2);

  const bool all_masked = len_raw <= 0;
  const int len = all_masked ? LK : min(len_raw, LK);
  const int np = (len + PAGE - 1) >> 4;
  const int Pe = min(P, rdl(row_scan_max(np), 15));
  const int n = max(np - Pe, 0);
  const int W = Pe + rdl(row_scan_add(n), 15);
  const int T = (W >> 4) + (c < (W & 15) ? 1 : 0);
  const int own = min(n, T), exc = n - own, spare = T - own;
  const int S = row_scan_add(spare) - spare;
  const int E = Pe + row_scan_add(exc) - exc;
  const int S_w = rdl(S, w), spare_w = rdl(spare, w), T_w = rdl(T, w);
  const int ns = max(0, min(S_w + spare_w, Pe) - S_w);
  const int lo = max(S_w, E), hi = min(S_w + spare_w, E + exc);
  const unsigned xmask = (unsigned)(__ballot(hi > lo) & 0xFFFFull);
  const int xoff = Pe + own - E;

  if (wave == 0) {
    __builtin_amdgcn_s_setprio(3);
    unsigned vp[4];
    {
      int idx[4];
#pragma unroll
      for (int r = 0; r < 4; ++r) { const int i = 64 * r + lane; idx[r] = (i < spare_w) ? S_w + i : w * NB + Pe + (i - spare_w); }
      for (unsigned m = xmask; m; m &= m - 1) {
        const int b = __builtin_ctz(m);
        const int lo_b = rdl(lo, b) - S_w, hi_b = rdl(hi, b) - S_w, off_b = rdl(xoff, b) + b * NB + S_w;
#pragma unroll
        for (int r = 0; r < 4; ++r) { const int i = 64 * r + lane; if (i >= lo_b && i < hi_b) idx[r] = i + off_b; }
      }
#pragma unroll
      for (int r = 0; r < 4; ++r) { const int i = 64 * r + lane; vp[r] = (unsigned)a.bt[i < T_w ? idx[r] : 0] * PAGE_BYTES + (unsigned)h * (D * 2); }
    }
    unsigned vk[4], vv[4];
#pragma unroll
    for (int i = 0; i < 4; ++i) { const int t = 4 * i + g;
      vk[i] = t * TOK_BYTES + ((c ^ t) << 4) - 1024 * i;
      vv[i] = t * TOK_BYTES + ((c ^ ((t & 7) << 1)) << 4) - 1024 * i; }
    const unsigned ring = (unsigned)(uintptr_t)(lds + L_RING);
    const __amdgpu_buffer_rsrc_t rk = __builtin_amdgcn_make_buffer_rsrc((void*)a.k, 0, a.kv_bytes, 0x00020000);
    const __amdgpu_buffer_rsrc_t rv = __builtin_amdgcn_make_buffer_rsrc((void*)a.v, 0, a.kv_bytes, 0x00020000);
    asm volatile("" : "+v"(vp[0]), "+v"(vp[1]), "+v"(vp[2]), "+v"(vp[3]));
    STAMP(3);
    int pub = 0, relnext = 0;
    auto publish = [&](int nl) { if (nl > pub) { pub = nl; lds_st(ctl + C_LANDED, nl); } };
    for (int i = 0; i < T_w; ++i) {
      if (i >= NSLOT) {
        const int need = (i >> 2) - NSLOT / 4 + 1;
        if (rfl(relnext) < need) {
          LAS unsigned char* rel = ctl + C_REL + 4 * (i & 3);
          if (rfl(lds_ld(rel)) < need) {
            wait_vm<0>(); publish(2 * i);
            lds_wait_ge(rel, need);
          }
        }
        asm volatile("" ::: "memory");
      }
      const unsigned so = (unsigned)rdl((int)vp[0], i & 63);
      const unsigned m0v = ring + (i & (NSLOT - 1)) * 8192;
      dma4(m0v, rk, so, vk[0], vk[1], vk[2], vk[3]);
      publish(2 * i - INFL / 4);
      relnext = lds_ld(ctl + C_REL + 4 * ((i + 1) & 3));
      if ((i & 63) == 63) { vp[0] = vp[1]; vp[1] = vp[2]; vp[2] = vp[3]; }
      wait_vm<INFL>();
      dma4(m0v + 4096, rv, so, vv[0], vv[1], vv[2], vv[3]);
      publish(2 * i + 1 - INFL / 4);
      wait_vm<INFL>();
    }
    STAMP(5);
    publish(2 * T_w - INFL / 4);
    if (INFL > 16) { wait_vm<16>(); publish(2 * T_w - 4); }
    if (INFL > 12) { wait_vm<12>(); publish(2 * T_w - 3); }
    if (INFL > 8) { wait_vm<8>(); publish(2 * T_w - 2); }
    wait_vm<4>(); publish(2 * T_w - 1);
    wait_vm<0>(); publish(2 * T_w);
    STAMP(6);
  } else if (wave <= NSW) {
    const int cw = wave - 1;
    const int minlen = LK - rdl(row_scan_max(LK - len), 15);
    {
      const bool z0 = __shfl((int)all_masked, c >> 1) != 0, z1 = __shfl((int)all_masked, 8 + (c >> 1)) != 0;
#pragma unroll
      for (int s = 0; s < 4; ++s) {
        u32x4 t0 = __builtin_bit_cast(u32x4, qf[0][s]), t1 = __builtin_bit_cast(u32x4, qf[1][s]);
#pragma unroll
        for (int e = 0; e < 4; ++e) { t0[e] = z0 ? 0u : t0[e]; t1[e] = z1 ? 0u : t1[e]; }
        qf[0][s] = __builtin_bit_cast(bf16x8, t0); qf[1][s] = __builtin_bit_cast(bf16x8, t1);
      }
    }
    const int len0 = __shfl(len, c >> 1), len1 = __shfl(len, 8 + (c >> 1));
    const unsigned ka0 = c * 256 + ((g ^ c) << 4);
    const int tv = 4 * g + (c >> 2);
    const unsigned va0 = 4096 + tv * 256 + ((tv & 7) << 5) + (c & 3) * 8;
    int j = 0, seen = 0;
    Soft s0, s1;
    auto page = [&](auto SHc, const bf16x8 (&qa)[4], const bf16x8 (&qb)[4], int la, int lb, int t0, bool mask) {
      constexpr bool SH = decltype(SHc)::value;
      const int i = 4 * j + cw;
      LAS unsigned char* sl = lds + L_RING + (i & (NSLOT - 1)) * 8192;
      if (seen < 2 * i + 1) seen = lds_wait_ge(ctl + C_LANDED, 2 * i + 1);
      bf16x8 kf[4];
#pragma unroll
      for (int s = 0; s < 4; ++s) kf[s] = *(LAS bf16x8*)(sl + (ka0 ^ (s << 6)));
      f32x4 xa = {0.f, 0.f, 0.f, 0.f}, xb = {0.f, 0.f, 0.f, 0.f};
#pragma unroll
      for (int s = 0; s < 4; ++s) { xa = __builtin_amdgcn_mfma_f32_16x16x32_bf16(kf[s], qa[s], xa, 0, 0, 0);
        if (SH) xb = __builtin_amdgcn_mfma_f32_16x16x32_bf16(kf[s], qb[s], xb, 0, 0, 0); }
      const s16x4 pa = soft_step(s0, xa, mask, t0 + 4 * g, la);
      s16x4 pb; if (SH) pb = soft_step(s1, xb, mask, t0 + 4 * g, lb);
      if (seen < 2 * i + 2) seen = lds_wait_ge(ctl + C_LANDED, 2 * i + 2);
      s16x4 vf[8];
#pragma unroll
      for (int blk = 0; blk < 8; ++blk) vf[blk] = __builtin_amdgcn_ds_read_tr16_b64_v4i16((LAS s16x4*)(sl + (va0 ^ (blk << 5))));
      ++j;
      asm volatile("s_waitcnt lgkmcnt(0)" : "+v"(vf[0]), "+v"(vf[1]), "+v"(vf[2]), "+v"(vf[3]), "+v"(vf[4]), "+v"(vf[5]), "+v"(vf[6]), "+v"(vf[7]) :: "memory");
      lds_st(ctl + C_REL + 4 * cw, j);
#pragma unroll
      for (int blk = 0; blk < 8; ++blk) { s0.o[blk] = __builtin_amdgcn_mfma_f32_16x16x16bf16_1k(vf[blk], pa, s0.o[blk], 0, 0, 0);
        if (SH) s1.o[blk] = __builtin_amdgcn_mfma_f32_16x16x16bf16_1k(vf[blk], pb, s1.o[blk], 0, 0, 0); }
    };
    int npass = WARM ? 2 : 1;
    asm volatile("" : "+s"(npass));
#pragma nounroll
    for (int pass = 2 - npass; pass < 2; ++pass) {
      const bool real = pass != 0;
      if (real && ns > 0) {
        soft_reset(s0); soft_reset(s1);
        for (int i = cw; i < ns; i += 4) { const int t0 = (S_w + i) * PAGE; page(std::true_type{}, qf[0], qf[1], len0, len1, t0, t0 + PAGE > minlen); }
        s0.l = xrow16_sum(s0.l); s1.l = xrow16_sum(s1.l);
        if (cw > 0) lds_wait_ge(ctl + C_CSTEP, cw);
#pragma unroll
        for (int cg = 0; cg < 2; ++cg) { Soft& s = cg ? s1 : s0;
          LAS unsigned char* r = lds + L_MRG + (16 * cg + c) * ROW;
          if (cw > 0) {
            const f32x2 ml = *(LAS f32x2*)(r + 512);
            const float M = fmaxf(ml[0], s.m), f0 = mfac(ml[0], M), f1 = mfac(s.m, M);
#pragma unroll
            for (int blk = 0; blk < 8; ++blk) s.o[blk] = *(LAS f32x4*)(r + blk * 64 + g * 16) * f0 + s.o[blk] * f1;
            s.l = ml[1] * f0 + s.l * f1; s.m = M;
          }
#pragma unroll
          for (int blk = 0; blk < 8; ++blk) *(LAS f32x4*)(r + blk * 64 + g * 16) = s.o[blk];
          lds_drain();
          if (g == 0) *(LAS f32x2*)(r + 512) = f32x2{s.m, s.l};
        }
        lds_drain();
        lds_st(ctl + C_CSTEP, cw + 1);
      }
      unsigned m = real ? xmask : 0u;
      int kx = 0;
      for (;;) {
        const bool last = m == 0;
        const int b = last ? w : __builtin_ctz(m);
        const int i0 = last ? spare_w : rdl(lo, b) - S_w;
        const int i1 = last ? (real ? T_w : 0) : rdl(hi, b) - S_w;
        const int lp0 = last ? Pe : rdl(lo, b) + rdl(xoff, b);
        bf16x8 qa[4];
        if (b < 8) {
#pragma unroll
          for (int s = 0; s < 4; ++s) qa[s] = qf[0][s];
        } else {
#pragma unroll
          for (int s = 0; s < 4; ++s) qa[s] = qf[1][s];
        }
        const int lb = rdl(len, b);
        soft_reset(s0);
        for (int i = 4 * j + cw; i < i1; i += 4) { const int t0 = (lp0 + i - i0) * PAGE; page(std::false_type{}, qa, qa, lb, lb, t0, t0 + PAGE > lb); }
        if (!last && kx > 0) lds_wait_ge(ctl + C_RFREE, kx);
        const float lt = xrow16_sum(s0.l);
        if ((c >> 1) == (b & 7)) {
          LAS unsigned char* r = lds + (last ? L_RECF : L_RECX) + (cw * 2 + (c & 1)) * ROW;
#pragma unroll
          for (int blk = 0; blk < 8; ++blk) *(LAS f32x4*)(r + blk * 64 + g * 16) = s0.o[blk];
          if (g == 0) *(LAS f32x2*)(r + 512) = f32x2{s0.m, lt};
        }
        if (last) break;
        lds_drain();
        if (lane == 0) __hip_atomic_fetch_add((LAS int*)(ctl + C_RCNT), 1, __ATOMIC_RELAXED, __HIP_MEMORY_SCOPE_WORKGROUP);
        m &= m - 1; ++kx;
      }
      if (real) wg_barrier();
      if (cw < 2) {
        const int r = cw, d0 = 2 * lane;
        float M = -INFINITY; f32x2 ml[5], o[5];
#pragma unroll
        for (int k = 0; k < 5; ++k) { ml[k] = *(LAS f32x2*)(lds + L_RECF + (k * 2 + r) * ROW + 512); o[k] = *(LAS f32x2*)(lds + L_RECF + (k * 2 + r) * ROW + d0 * 4); }
#pragma unroll
        for (int k = 0; k < 5; ++k) M = fmaxf(M, ml[k][0]);
        float L = 0.f, o0 = 0.f, o1 = 0.f;
#pragma unroll
        for (int k = 0; k < 5; ++k) { const float f = mfac(ml[k][0], M); L = fmaf(f, ml[k][1], L); o0 = fmaf(f, o[k][0], o0); o1 = fmaf(f, o[k][1], o1); }
        const float inv = __builtin_amdgcn_rcpf(L);
        unsigned res = cvtpk(o0 * inv, o1 * inv);
        asm volatile("" : "+v"(res));
        if (real) a.out[((w * HQ + 2 * h + r) * D + d0) >> 1] = res;
      }
    }
  } else {
    const __amdgpu_buffer_rsrc_t prsrc = __builtin_amdgcn_make_buffer_rsrc(a.ws + W_PART, 0, PART_BYTES, 0x00020000);
    const unsigned blk = w * 16 + h;
    const int r = lane >> 5, ch = lane & 31;
    if (ns > 0) {
      lds_wait_ge(ctl + C_CSTEP, NSW);
#pragma unroll 4
      for (int k = 0; k < 16; ++k) { const int off = (2 * k + r) * ROW + ch * 16;
        const u32x4 v = *(LAS u32x4*)(lds + L_MRG + off);
        __builtin_amdgcn_raw_buffer_store_b128(v, prsrc, off, blk * PS_BYTES, 16); }
      if (lane < 32) { const u32x2 v = *(LAS u32x2*)(lds + L_MRG + lane * ROW + 512);
        __builtin_amdgcn_raw_buffer_store_b64(v, prsrc, lane * ROW + 512, blk * PS_BYTES, 16); }
    }
    int kx = 0;
    for (unsigned m = xmask; m; m &= m - 1, ++kx) {
      const int b = __builtin_ctz(m);
      lds_wait_ge(ctl + C_RCNT, NSW * (kx + 1));
      float M = -INFINITY; f32x2 ml[4]; f32x4 ok[4];
#pragma unroll
      for (int k = 0; k < 4; ++k) { ml[k] = *(LAS f32x2*)(lds + L_RECX + (k * 2 + r) * ROW + 512); ok[k] = *(LAS f32x4*)(lds + L_RECX + (k * 2 + r) * ROW + ch * 16); M = fmaxf(M, ml[k][0]); }
      float L = 0.f; f32x4 o = {0.f, 0.f, 0.f, 0.f};
#pragma unroll
      for (int k = 0; k < 4; ++k) { const float f = mfac(ml[k][0], M); L = fmaf(f, ml[k][1], L); o += ok[k] * f; }
      asm volatile("s_waitcnt lgkmcnt(0)" : "+v"(o) :: "memory");
      lds_st(ctl + C_RFREE, kx + 1);
      const unsigned so = W_PX + (blk * 16 + b) * PX_BYTES;
      __builtin_amdgcn_raw_buffer_store_b128(__builtin_bit_cast(u32x4, o), prsrc, r * ROW + ch * 16, so, 16);
      if (ch == 0) __builtin_amdgcn_raw_buffer_store_b64(__builtin_bit_cast(u32x2, f32x2{M, L}), prsrc, r * ROW + 512, so, 16);
    }
    asm volatile("s_waitcnt vmcnt(0)" ::: "memory");
    if (lane == 0) __hip_atomic_store((gu64*)(a.ws + W_FLAG + blk * FLAG_STRIDE), tag, RLX_AGENT);
    STAMP(3);
    const int E_w = rdl(E, w), exc_w = rdl(exc, w);
    const bool sh_ov = spare > 0 && S < Pe;
    const bool ex_ov = min(S + spare, E_w + exc_w) > max(S, E_w);
    unsigned cmask = (unsigned)(__ballot(sh_ov) & 0xFFFFull) | ((unsigned)(__ballot(ex_ov) & 0xFFFFull) << 16);
    {
      const bool need = lane < 16 && (sh_ov || ex_ov);
      gu64* f = (gu64*)(a.ws + W_FLAG + (size_t)(c * 16 + h) * FLAG_STRIDE);
      for (unsigned spins = 0; spins < (1u << 20); ++spins) {
        const bool ok = !need || __hip_atomic_load(f, RLX_AGENT) == tag;
        if (__all(ok)) break;
        __builtin_amdgcn_s_sleep(32);
      }
      asm volatile("" ::: "memory");
    }
    STAMP(4);
    {
      const unsigned vso = lane < 16 ? (unsigned)(c * 16 + h) * PS_BYTES + (unsigned)(2 * w) * ROW : W_PX + ((unsigned)(c * 16 + h) * 16 + w) * PX_BYTES;
      float M = -INFINITY, L = 0.f; f32x4 o = {0.f, 0.f, 0.f, 0.f};
      constexpr int GB = 10;
      while (cmask) {
        u32x4 po[GB]; u32x2 pml[GB]; bool pv[GB];
#pragma unroll
        for (int k = 0; k < GB; ++k) {
          pv[k] = cmask != 0;
          const int id = pv[k] ? __builtin_ctz(cmask) : 0; cmask &= cmask - 1;
          const unsigned so = pv[k] ? (unsigned)rdl((int)vso, id) : PART_BYTES;
          po[k] = __builtin_amdgcn_raw_buffer_load_b128(prsrc, r * ROW + ch * 16 + so, 0, 16);
          pml[k] = __builtin_amdgcn_raw_buffer_load_b64(prsrc, r * ROW + 512 + so, 0, 16);
        }
#pragma unroll
        for (int k = 0; k < GB; ++k) {
          const float mk = pv[k] ? __uint_as_float(pml[k][0]) : -INFINITY, lk = __uint_as_float(pml[k][1]);
          const float Mn = fmaxf(M, mk), f0 = mfac(M, Mn), f1 = mfac(mk, Mn);
          o = o * f0 + __builtin_bit_cast(f32x4, po[k]) * f1; L = L * f0 + lk * f1; M = Mn;
        }
      }
      LAS unsigned char* rec = lds + L_RECF + (4 * 2 + r) * ROW;
      *(LAS f32x4*)(rec + ch * 16) = o;
      if (ch == 0) *(LAS f32x2*)(rec + 512) = f32x2{M, L};
    }
    STAMP(5);
    wg_barrier();
    STAMP(6);
    if (blk == 0) {
      for (unsigned spins = 0; spins < (1u << 20); ++spins) {
        bool ok = true;
#pragma unroll
        for (int k = 0; k < 4; ++k) ok &= __hip_atomic_load((gu64*)(a.ws + W_FLAG + (size_t)(k * 64 + lane) * FLAG_STRIDE), RLX_AGENT) == tag;
        if (__all(ok)) break;
        __builtin_amdgcn_s_sleep(8);
      }
      if (lane == 0) __hip_atomic_store((gu64*)(a.ws + W_EPOCH), tag, RLX_AGENT);
    }
  }
}

extern "C" void kernel_launch(void* const* d_in, const int* in_sizes, int n_in, void* d_out, int out_size, void* d_ws, size_t ws_size, hipStream_t stream) {
  Args a;
  a.q = (const bf16x8*)d_in[0]; a.k = d_in[1]; a.v = d_in[2]; a.bt = (const int*)d_in[3]; a.lens = (const int*)d_in[4];
  a.out = (unsigned*)d_out; a.ws = (unsigned char*)d_ws; a.kv_bytes = (unsigned)in_sizes[1] * 2u; a.pad = 0;
  hipFuncSetAttribute((const void*)radix_attn, hipFuncAttributeMaxDynamicSharedMemorySize, LDS_BYTES);
  hipLaunchKernelGGL(radix_attn, dim3(256), dim3(NTHR), LDS_BYTES, stream, a);
}
```

```cpp
#define MAPSWAP 1
#define INFL 20
#include <hip/hip_runtime.h>
#include <hip/hip_bf16.h>
#include <stdint.h>
#include <stdio.h>

#define NSLOT 16
#define KV_NT 1
#define WARM 1
#define DIAG 0

constexpr int B = 16, HQ = 32, HKV = 16, D = 128, PAGE = 16, NB = 256, LK = NB * PAGE;
constexpr int NSW = 4, NTHR = (NSW + 2) * 64;
constexpr float C2 = 0.088388347648318440f * 1.4426950408889634f;
constexpr unsigned PAGE_BYTES = PAGE * HKV * D * 2;
constexpr unsigned TOK_BYTES = HKV * D * 2;

constexpr int ROW = 528;
constexpr int L_RING = 0;
constexpr int L_MRG = NSLOT * 8192;
constexpr int L_RECX = L_MRG + 32 * ROW;
constexpr int L_RECF = L_RECX + 4 * 2 * ROW;
constexpr int L_MISC = L_RECF + 5 * 2 * ROW;
constexpr int C_LANDED = 0, C_REL = 4, C_CSTEP = 20, C_RCNT = 24, C_RFREE = 28, C_P = 32;
constexpr int LDS_BYTES = L_MISC + 64;
static_assert(NSLOT % 4 == 0 && INFL % 4 == 0 && (NSLOT & (NSLOT - 1)) == 0, "ring geometry");
static_assert(LDS_BYTES <= 160 * 1024, "LDS");

constexpr size_t W_EPOCH = 0, W_FLAG = 256, FLAG_STRIDE = 128;
constexpr size_t W_PART = 65536;
constexpr unsigned PS_BYTES = 32 * ROW, PX_BYTES = 2 * ROW;
constexpr unsigned W_PX = 256 * PS_BYTES;
constexpr unsigned PART_BYTES = W_PX + 256 * 16 * PX_BYTES;
#define STAMP(k) do {} while (0)

typedef __bf16 bf16x8 __attribute__((ext_vector_type(8)));
typedef __bf16 bf16x2 __attribute__((ext_vector_type(2)));
typedef short s16x4 __attribute__((ext_vector_type(4)));
typedef float f32x4 __attribute__((ext_vector_type(4)));
typedef float f32x2 __attribute__((ext_vector_type(2)));
typedef unsigned u32x4 __attribute__((ext_vector_type(4)));
typedef unsigned u32x2 __attribute__((ext_vector_type(2)));
typedef int i32x4 __attribute__((ext_vector_type(4)));
#define LAS __attribute__((address_space(3)))
#define GAS __attribute__((address_space(1)))
#define DI __device__ __forceinline__
typedef GAS unsigned long long gu64;
#define RLX_AGENT __ATOMIC_RELAXED, __HIP_MEMORY_SCOPE_AGENT

DI int rfl(int x) { return __builtin_amdgcn_readfirstlane(x); }
DI int rdl(int x, int l) { return __builtin_amdgcn_readlane(x, l); }
template <int CTRL> DI int dpp_i(int oldv, int x) { return __builtin_amdgcn_update_dpp(oldv, x, CTRL, 0xf, 0xf, false); }
DI int row_scan_add(int x) {
  x += dpp_i<0x111>(0, x); x += dpp_i<0x112>(0, x); x += dpp_i<0x114>(0, x); x += dpp_i<0x118>(0, x); return x;
}
DI int row_scan_max(int x) {
  x = max(x, dpp_i<0x111>(0, x)); x = max(x, dpp_i<0x112>(0, x)); x = max(x, dpp_i<0x114>(0, x)); x = max(x, dpp_i<0x118>(0, x)); return x;
}
DI float xrow16_max(float x) {
  auto s = __builtin_amdgcn_permlane16_swap(__float_as_uint(x), __float_as_uint(x), false, false);
  x = fmaxf(__uint_as_float(s[0]), __uint_as_float(s[1]));
  auto t = __builtin_amdgcn_permlane32_swap(__float_as_uint(x), __float_as_uint(x), false, false);
  return fmaxf(__uint_as_float(t[0]), __uint_as_float(t[1]));
}
DI float xrow16_sum(float x) {
  auto s = __builtin_amdgcn_permlane16_swap(__float_as_uint(x), __float_as_uint(x), false, false);
  x = __uint_as_float(s[0]) + __uint_as_float(s[1]);
  auto t = __builtin_amdgcn_permlane32_swap(__float_as_uint(x), __float_as_uint(x), false, false);
  return __uint_as_float(t[0]) + __uint_as_float(t[1]);
}
DI unsigned cvtpk(float lo, float hi) { f32x2 v = {lo, hi}; bf16x2 b = __builtin_convertvector(v, bf16x2); return __builtin_bit_cast(unsigned, b); }
DI float ex2(float x) { return __builtin_amdgcn_exp2f(x); }
DI void wg_barrier() { asm volatile("s_waitcnt lgkmcnt(0)\n\ts_barrier" ::: "memory"); }
template <int N> DI void wait_vm() { asm volatile("s_waitcnt vmcnt(%0)" :: "i"(N) : "memory"); }

#define NTS " nt"
DI void dma4(unsigned m0v, __amdgpu_buffer_rsrc_t rsrc, unsigned soff, unsigned v0, unsigned v1, unsigned v2, unsigned v3) {
  asm volatile("s_mov_b32 m0, %0\n\t"
               "s_nop 4\n\t"
               "buffer_load_dwordx4 %1, %5, %6 offen" NTS " lds\n\t"
               "buffer_load_dwordx4 %2, %5, %6 offen offset:1024" NTS " lds\n\t"
               "buffer_load_dwordx4 %3, %5, %6 offen offset:2048" NTS " lds\n\t"
               "buffer_load_dwordx4 %4, %5, %6 offen offset:3072" NTS " lds"
               :: "s"(m0v), "v"(v0), "v"(v1), "v"(v2), "v"(v3), "s"(rsrc), "s"(soff) : "memory");
}

struct Args { const bf16x8* q; const void* k; const void* v; const int* bt; const int* lens; unsigned* out; unsigned char* ws; unsigned kv_bytes; unsigned pad; };

DI float mfac(float m, float M) { return (m == -INFINITY) ? 0.f : ex2((m - M) * C2); }

struct Soft { float m, l; f32x4 o[8]; };
DI void soft_reset(Soft& s) { s.m = -INFINITY; s.l = 0.f;
#pragma unroll
  for (int i = 0; i < 8; ++i) s.o[i] = f32x4{0.f, 0.f, 0.f, 0.f}; }

DI s16x4 soft_step(Soft& s, f32x4 x, bool mask, int tok0, int len) {
  if (mask) {
#pragma unroll
    for (int r = 0; r < 4; ++r) x[r] = (tok0 + r < len) ? x[r] : -INFINITY;
  }
  float pm = fmaxf(fmaxf(x[0], x[1]), fmaxf(x[2], x[3]));
  pm = xrow16_max(pm);
  const float mn = fmaxf(s.m, pm);
  const float ms = (mn == -INFINITY) ? 0.f : mn * C2;
  const float alpha = ex2(s.m * C2 - ms);
  float p[4];
#pragma unroll
  for (int r = 0; r < 4; ++r) p[r] = ex2(fmaf(x[r], C2, -ms));
  s.l = fmaf(s.l, alpha, (p[0] + p[1]) + (p[2] + p[3]));
  s.m = mn;
#pragma unroll
  for (int i = 0; i < 8; ++i) s.o[i] *= alpha;
  u32x2 pk = {cvtpk(p[0], p[1]), cvtpk(p[2], p[3])};
  return __builtin_bit_cast(s16x4, pk);
}

DI int lds_ld(LAS unsigned char* p) { return __hip_atomic_load((LAS int*)p, __ATOMIC_RELAXED, __HIP_MEMORY_SCOPE_WORKGROUP); }
DI void lds_st(LAS unsigned char* p, int v) { __hip_atomic_store((LAS int*)p, v, __ATOMIC_RELAXED, __HIP_MEMORY_SCOPE_WORKGROUP); }
DI int lds_wait_ge(LAS unsigned char* p, int need) {
  int v;
  for (unsigned spins = 0;; ++spins) { v = rfl(lds_ld(p)); if (v >= need || spins > (1u << 24)) break; __builtin_amdgcn_s_sleep(1); }
  asm volatile("" ::: "memory");
  return v;
}
DI void lds_drain() { asm volatile("s_waitcnt lgkmcnt(0)" ::: "memory"); }

__global__ void __launch_bounds__(NTHR, 2) radix_attn(Args a) {
  extern __shared__ __attribute__((aligned(16))) unsigned char lds_raw[];
  LAS unsigned char* lds = (LAS unsigned char*)lds_raw;
  LAS unsigned char* ctl = lds + L_MISC;
  const int tid = threadIdx.x, lane = tid & 63, wave = rfl(tid >> 6);
  const int w = blockIdx.x & 15, h = blockIdx.x >> 4;
  const int c = lane & 15, g = lane >> 4;
  const int len_raw = a.lens[c];
  int P;
  bf16x8 qf[2][4];
  unsigned long long tag = 0;
  if (wave == 0) {
    const i32x4* bt4 = (const i32x4*)a.bt;
    i32x4 rk[16];
#pragma unroll
    for (int k = 0; k < 16; ++k) rk[k] = bt4[k * (NB / 4) + lane];
    i32x4 acc = rk[1] ^ rk[0];
#pragma unroll
    for (int k = 2; k < 16; ++k) acc |= rk[k] ^ rk[0];
    P = NB;
#pragma unroll
    for (int e = 0; e < 4; ++e) {
      const unsigned long long bal = __ballot(acc[e] != 0);
      P = min(P, bal ? 4 * (int)__builtin_ctzll(bal) + e : NB);
    }
    if (lane == 0) *(LAS int*)(ctl + C_P) = P;
    wg_barrier();
  } else {
    if (wave <= NSW) {
      __builtin_amdgcn_s_sleep(6);
#pragma unroll
      for (int cg = 0; cg < 2; ++cg)
#pragma unroll
        for (int s = 0; s < 4; ++s) { const int r = 16 * cg + c; qf[cg][s] = a.q[(((r >> 1) * HQ + 2 * h + (r & 1)) * D + 32 * s + 8 * g) / 8]; }
    } else {
      tag = __hip_atomic_load((gu64*)(a.ws + W_EPOCH), RLX_AGENT) + 1ull;
      if (lane < 8) *(LAS int*)(ctl + 4 * lane) = 0;
#pragma unroll
      for (int cg = 0; cg < 2; ++cg)
#pragma unroll
        for (int s = 0; s < 4; ++s) qf[cg][s] = bf16x8{};
    }
    wg_barrier();
    P = rfl(*(LAS int*)(ctl + C_P));
  }
  STAMP(2);

  const bool all_masked = len_raw <= 0;
  const int len = all_masked ? LK : min(len_raw, LK);
  const int np = (len + PAGE - 1) >> 4;
  const int Pe = min(P, rdl(row_scan_max(np), 15));
  const int n = max(np - Pe, 0);
  const int W = Pe + rdl(row_scan_add(n), 15);
  const int T = (W >> 4) + (c < (W & 15) ? 1 : 0);
  const int own = min(n, T), exc = n - own, spare = T - own;
  const int S = row_scan_add(spare) - spare;
  const int E = Pe + row_scan_add(exc) - exc;
  const int S_w = rdl(S, w), spare_w = rdl(spare, w), T_w = rdl(T, w);
  const int ns = max(0, min(S_w + spare_w, Pe) - S_w);
  const int lo = max(S_w, E), hi = min(S_w + spare_w, E + exc);
  const unsigned xmask = (unsigned)(__ballot(hi > lo) & 0xFFFFull);
  const int xoff = Pe + own - E;

  if (wave == 0) {
    __builtin_amdgcn_s_setprio(3);
    unsigned vp[4];
    {
      int idx[4];
#pragma unroll
      for (int r = 0; r < 4; ++r) { const int i = 64 * r + lane; idx[r] = (i < spare_w) ? S_w + i : w * NB + Pe + (i - spare_w); }
      for (unsigned m = xmask; m; m &= m - 1) {
        const int b = __builtin_ctz(m);
        const int lo_b = rdl(lo, b) - S_w, hi_b = rdl(hi, b) - S_w, off_b = rdl(xoff, b) + b * NB + S_w;
#pragma unroll
        for (int r = 0; r < 4; ++r) { const int i = 64 * r + lane; if (i >= lo_b && i < hi_b) idx[r] = i + off_b; }
      }
#pragma unroll
      for (int r = 0; r < 4; ++r) { const int i = 64 * r + lane; vp[r] = (unsigned)a.bt[i < T_w ? idx[r] : 0] * PAGE_BYTES + (unsigned)h * (D * 2); }
    }
    unsigned vk[4], vv[4];
#pragma unroll
    for (int i = 0; i < 4; ++i) { const int t = 4 * i + g;
      vk[i] = t * TOK_BYTES + ((c ^ t) << 4) - 1024 * i;
      vv[i] = t * TOK_BYTES + ((c ^ ((t & 7) << 1)) << 4) - 1024 * i; }
    const unsigned ring = (unsigned)(uintptr_t)(lds + L_RING);
    const __amdgpu_buffer_rsrc_t rk = __builtin_amdgcn_make_buffer_rsrc((void*)a.k, 0, a.kv_bytes, 0x00020000);
    const __amdgpu_buffer_rsrc_t rv = __builtin_amdgcn_make_buffer_rsrc((void*)a.v, 0, a.kv_bytes, 0x00020000);
    asm volatile("" : "+v"(vp[0]), "+v"(vp[1]), "+v"(vp[2]), "+v"(vp[3]));
    STAMP(3);
    int pub = 0, relnext = 0;
    auto publish = [&](int nl) { if (nl > pub) { pub = nl; lds_st(ctl + C_LANDED, nl); } };
    for (int i = 0; i < T_w; ++i) {
      if (i >= NSLOT) {
        const int need = (i >> 2) - NSLOT / 4 + 1;
        if (rfl(relnext) < need) {
          LAS unsigned char* rel = ctl + C_REL + 4 * (i & 3);
          if (rfl(lds_ld(rel)) < need) {
            wait_vm<0>(); publish(2 * i);
            lds_wait_ge(rel, need);
          }
        }
        asm volatile("" ::: "memory");
      }
      const unsigned so = (unsigned)rdl((int)vp[0], i & 63);
      const unsigned m0v = ring + (i & (NSLOT - 1)) * 8192;
      dma4(m0v, rk, so, vk[0], vk[1], vk[2], vk[3]);
      publish(2 * i - INFL / 4);
      relnext = lds_ld(ctl + C_REL + 4 * ((i + 1) & 3));
      if ((i & 63) == 63) { vp[0] = vp[1]; vp[1] = vp[2]; vp[2] = vp[3]; }
      wait_vm<INFL>();
      dma4(m0v + 4096, rv, so, vv[0], vv[1], vv[2], vv[3]);
      publish(2 * i + 1 - INFL / 4);
      wait_vm<INFL>();
    }
    STAMP(5);
    publish(2 * T_w - INFL / 4);
    if (INFL > 16) { wait_vm<16>(); publish(2 * T_w - 4); }
    if (INFL > 12) { wait_vm<12>(); publish(2 * T_w - 3); }
    if (INFL > 8) { wait_vm<8>(); publish(2 * T_w - 2); }
    wait_vm<4>(); publish(2 * T_w - 1);
    wait_vm<0>(); publish(2 * T_w);
    STAMP(6);
  } else if (wave <= NSW) {
    const int cw = wave - 1;
    const int minlen = LK - rdl(row_scan_max(LK - len), 15);
    {
      const bool z0 = __shfl((int)all_masked, c >> 1) != 0, z1 = __shfl((int)all_masked, 8 + (c >> 1)) != 0;
#pragma unroll
      for (int s = 0; s < 4; ++s) {
        u32x4 t0 = __builtin_bit_cast(u32x4, qf[0][s]), t1 = __builtin_bit_cast(u32x4, qf[1][s]);
#pragma unroll
        for (int e = 0; e < 4; ++e) { t0[e] = z0 ? 0u : t0[e]; t1[e] = z1 ? 0u : t1[e]; }
        qf[0][s] = __builtin_bit_cast(bf16x8, t0); qf[1][s] = __builtin_bit_cast(bf16x8, t1);
      }
    }
    const int len0 = __shfl(len, c >> 1), len1 = __shfl(len, 8 + (c >> 1));
    const unsigned ka0 = c * 256 + ((g ^ c) << 4);
    const int tv = 4 * g + (c >> 2);
    const unsigned va0 = 4096 + tv * 256 + ((tv & 7) << 5) + (c & 3) * 8;
    int j = 0, seen = 0;
    Soft s0, s1;
    auto page = [&](auto SHc, const bf16x8 (&qa)[4], const bf16x8 (&qb)[4], int la, int lb, int t0, bool mask) {
      constexpr bool SH = decltype(SHc)::value;
      const int i = 4 * j + cw;
      LAS unsigned char* sl = lds + L_RING + (i & (NSLOT - 1)) * 8192;
      if (seen < 2 * i + 1) seen = lds_wait_ge(ctl + C_LANDED, 2 * i + 1);
      bf16x8 kf[4];
#pragma unroll
      for (int s = 0; s < 4; ++s) kf[s] = *(LAS bf16x8*)(sl + (ka0 ^ (s << 6)));
      f32x4 xa = {0.f, 0.f, 0.f, 0.f}, xb = {0.f, 0.f, 0.f, 0.f};
#pragma unroll
      for (int s = 0; s < 4; ++s) { xa = __builtin_amdgcn_mfma_f32_16x16x32_bf16(kf[s], qa[s], xa, 0, 0, 0);
        if (SH) xb = __builtin_amdgcn_mfma_f32_16x16x32_bf16(kf[s], qb[s], xb, 0, 0, 0); }
      const s16x4 pa = soft_step(s0, xa, mask, t0 + 4 * g, la);
      s16x4 pb; if (SH) pb = soft_step(s1, xb, mask, t0 + 4 * g, lb);
      if (seen < 2 * i + 2) seen = lds_wait_ge(ctl + C_LANDED, 2 * i + 2);
      s16x4 vf[8];
#pragma unroll
      for (int blk = 0; blk < 8; ++blk) vf[blk] = __builtin_amdgcn_ds_read_tr16_b64_v4i16((LAS s16x4*)(sl + (va0 ^ (blk << 5))));
      ++j;
      asm volatile("s_waitcnt lgkmcnt(0)" : "+v"(vf[0]), "+v"(vf[1]), "+v"(vf[2]), "+v"(vf[3]), "+v"(vf[4]), "+v"(vf[5]), "+v"(vf[6]), "+v"(vf[7]) :: "memory");
      lds_st(ctl + C_REL + 4 * cw, j);
#pragma unroll
      for (int blk = 0; blk < 8; ++blk) { s0.o[blk] = __builtin_amdgcn_mfma_f32_16x16x16bf16_1k(vf[blk], pa, s0.o[blk], 0, 0, 0);
        if (SH) s1.o[blk] = __builtin_amdgcn_mfma_f32_16x16x16bf16_1k(vf[blk], pb, s1.o[blk], 0, 0, 0); }
    };
    int npass = WARM ? 2 : 1;
    asm volatile("" : "+s"(npass));
#pragma nounroll
    for (int pass = 2 - npass; pass < 2; ++pass) {
      const bool real = pass != 0;
      if (real && ns > 0) {
        soft_reset(s0); soft_reset(s1);
        for (int i = cw; i < ns; i += 4) { const int t0 = (S_w + i) * PAGE; page(std::true_type{}, qf[0], qf[1], len0, len1, t0, t0 + PAGE > minlen); }
        s0.l = xrow16_sum(s0.l); s1.l = xrow16_sum(s1.l);
        if (cw > 0) lds_wait_ge(ctl + C_CSTEP, cw);
#pragma unroll
        for (int cg = 0; cg < 2; ++cg) { Soft& s = cg ? s1 : s0;
          LAS unsigned char* r = lds + L_MRG + (16 * cg + c) * ROW;
          if (cw > 0) {
            const f32x2 ml = *(LAS f32x2*)(r + 512);
            const float M = fmaxf(ml[0], s.m), f0 = mfac(ml[0], M), f1 = mfac(s.m, M);
#pragma unroll
            for (int blk = 0; blk < 8; ++blk) s.o[blk] = *(LAS f32x4*)(r + blk * 64 + g * 16) * f0 + s.o[blk] * f1;
            s.l = ml[1] * f0 + s.l * f1; s.m = M;
          }
#pragma unroll
          for (int blk = 0; blk < 8; ++blk) *(LAS f32x4*)(r + blk * 64 + g * 16) = s.o[blk];
          lds_drain();
          if (g == 0) *(LAS f32x2*)(r + 512) = f32x2{s.m, s.l};
        }
        lds_drain();
        lds_st(ctl + C_CSTEP, cw + 1);
      }
      unsigned m = real ? xmask : 0u;
      int kx = 0;
      for (;;) {
        const bool last = m == 0;
        const int b = last ? w : __builtin_ctz(m);
        const int i0 = last ? spare_w : rdl(lo, b) - S_w;
        const int i1 = last ? (real ? T_w : 0) : rdl(hi, b) - S_w;
        const int lp0 = last ? Pe : rdl(lo, b) + rdl(xoff, b);
        bf16x8 qa[4];
        if (b < 8) {
#pragma unroll
          for (int s = 0; s < 4; ++s) qa[s] = qf[0][s];
        } else {
#pragma unroll
          for (int s = 0; s < 4; ++s) qa[s] = qf[1][s];
        }
        const int lb = rdl(len, b);
        soft_reset(s0);
        for (int i = 4 * j + cw; i < i1; i += 4) { const int t0 = (lp0 + i - i0) * PAGE; page(std::false_type{}, qa, qa, lb, lb, t0, t0 + PAGE > lb); }
        if (!last && kx > 0) lds_wait_ge(ctl + C_RFREE, kx);
        const float lt = xrow16_sum(s0.l);
        if ((c >> 1) == (b & 7)) {
          LAS unsigned char* r = lds + (last ? L_RECF : L_RECX) + (cw * 2 + (c & 1)) * ROW;
#pragma unroll
          for (int blk = 0; blk < 8; ++blk) *(LAS f32x4*)(r + blk * 64 + g * 16) = s0.o[blk];
          if (g == 0) *(LAS f32x2*)(r + 512) = f32x2{s0.m, lt};
        }
        if (last) break;
        lds_drain();
        if (lane == 0) __hip_atomic_fetch_add((LAS int*)(ctl + C_RCNT), 1, __ATOMIC_RELAXED, __HIP_MEMORY_SCOPE_WORKGROUP);
        m &= m - 1; ++kx;
      }
      if (real) wg_barrier();
      if (cw < 2) {
        const int r = cw, d0 = 2 * lane;
        float M = -INFINITY; f32x2 ml[5], o[5];
#pragma unroll
        for (int k = 0; k < 5; ++k) { ml[k] = *(LAS f32x2*)(lds + L_RECF + (k * 2 + r) * ROW + 512); o[k] = *(LAS f32x2*)(lds + L_RECF + (k * 2 + r) * ROW + d0 * 4); }
#pragma unroll
        for (int k = 0; k < 5; ++k) M = fmaxf(M, ml[k][0]);
        float L = 0.f, o0 = 0.f, o1 = 0.f;
#pragma unroll
        for (int k = 0; k < 5; ++k) { const float f = mfac(ml[k][0], M); L = fmaf(f, ml[k][1], L); o0 = fmaf(f, o[k][0], o0); o1 = fmaf(f, o[k][1], o1); }
        const float inv = __builtin_amdgcn_rcpf(L);
        unsigned res = cvtpk(o0 * inv, o1 * inv);
        asm volatile("" : "+v"(res));
        if (real) a.out[((w * HQ + 2 * h + r) * D + d0) >> 1] = res;
      }
    }
  } else {
    const __amdgpu_buffer_rsrc_t prsrc = __builtin_amdgcn_make_buffer_rsrc(a.ws + W_PART, 0, PART_BYTES, 0x00020000);
    const unsigned blk = w * 16 + h;
    const int r = lane >> 5, ch = lane & 31;
    if (ns > 0) {
      lds_wait_ge(ctl + C_CSTEP, NSW);
#pragma unroll 4
      for (int k = 0; k < 16; ++k) { const int off = (2 * k + r) * ROW + ch * 16;
        const u32x4 v = *(LAS u32x4*)(lds + L_MRG + off);
        __builtin_amdgcn_raw_buffer_store_b128(v, prsrc, off, blk * PS_BYTES, 16); }
      if (lane < 32) { const u32x2 v = *(LAS u32x2*)(lds + L_MRG + lane * ROW + 512);
        __builtin_amdgcn_raw_buffer_store_b64(v, prsrc, lane * ROW + 512, blk * PS_BYTES, 16); }
    }
    int kx = 0;
    for (unsigned m = xmask; m; m &= m - 1, ++kx) {
      const int b = __builtin_ctz(m);
      lds_wait_ge(ctl + C_RCNT, NSW * (kx + 1));
      float M = -INFINITY; f32x2 ml[4]; f32x4 ok[4];
#pragma unroll
      for (int k = 0; k < 4; ++k) { ml[k] = *(LAS f32x2*)(lds + L_RECX + (k * 2 + r) * ROW + 512); ok[k] = *(LAS f32x4*)(lds + L_RECX + (k * 2 + r) * ROW + ch * 16); M = fmaxf(M, ml[k][0]); }
      float L = 0.f; f32x4 o = {0.f, 0.f, 0.f, 0.f};
#pragma unroll
      for (int k = 0; k < 4; ++k) { const float f = mfac(ml[k][0], M); L = fmaf(f, ml[k][1], L); o += ok[k] * f; }
      asm volatile("s_waitcnt lgkmcnt(0)" : "+v"(o) :: "memory");
      lds_st(ctl + C_RFREE, kx + 1);
      const unsigned so = W_PX + (blk * 16 + b) * PX_BYTES;
      __builtin_amdgcn_raw_buffer_store_b128(__builtin_bit_cast(u32x4, o), prsrc, r * ROW + ch * 16, so, 16);
      if (ch == 0) __builtin_amdgcn_raw_buffer_store_b64(__builtin_bit_cast(u32x2, f32x2{M, L}), prsrc, r * ROW + 512, so, 16);
    }
    asm volatile("s_waitcnt vmcnt(0)" ::: "memory");
    if (lane == 0) __hip_atomic_store((gu64*)(a.ws + W_FLAG + blk * FLAG_STRIDE), tag, RLX_AGENT);
    STAMP(3);
    const int E_w = rdl(E, w), exc_w = rdl(exc, w);
    const bool sh_ov = spare > 0 && S < Pe;
    const bool ex_ov = min(S + spare, E_w + exc_w) > max(S, E_w);
    unsigned cmask = (unsigned)(__ballot(sh_ov) & 0xFFFFull) | ((unsigned)(__ballot(ex_ov) & 0xFFFFull) << 16);
    {
      const bool need = lane < 16 && (sh_ov || ex_ov);
      gu64* f = (gu64*)(a.ws + W_FLAG + (size_t)(c * 16 + h) * FLAG_STRIDE);
      for (unsigned spins = 0; spins < (1u << 20); ++spins) {
        const bool ok = !need || __hip_atomic_load(f, RLX_AGENT) == tag;
        if (__all(ok)) break;
        __builtin_amdgcn_s_sleep(32);
      }
      asm volatile("" ::: "memory");
    }
    STAMP(4);
    {
      const unsigned vso = lane < 16 ? (unsigned)(c * 16 + h) * PS_BYTES + (unsigned)(2 * w) * ROW : W_PX + ((unsigned)(c * 16 + h) * 16 + w) * PX_BYTES;
      float M = -INFINITY, L = 0.f; f32x4 o = {0.f, 0.f, 0.f, 0.f};
      constexpr int GB = 10;
      while (cmask) {
        u32x4 po[GB]; u32x2 pml[GB]; bool pv[GB];
#pragma unroll
        for (int k = 0; k < GB; ++k) {
          pv[k] = cmask != 0;
          const int id = pv[k] ? __builtin_ctz(cmask) : 0; cmask &= cmask - 1;
          const unsigned so = pv[k] ? (unsigned)rdl((int)vso, id) : PART_BYTES;
          po[k] = __builtin_amdgcn_raw_buffer_load_b128(prsrc, r * ROW + ch * 16 + so, 0, 16);
          pml[k] = __builtin_amdgcn_raw_buffer_load_b64(prsrc, r * ROW + 512 + so, 0, 16);
        }
#pragma unroll
        for (int k = 0; k < GB; ++k) {
          const float mk = pv[k] ? __uint_as_float(pml[k][0]) : -INFINITY, lk = __uint_as_float(pml[k][1]);
          const float Mn = fmaxf(M, mk), f0 = mfac(M, Mn), f1 = mfac(mk, Mn);
          o = o * f0 + __builtin_bit_cast(f32x4, po[k]) * f1; L = L * f0 + lk * f1; M = Mn;
        }
      }
      LAS unsigned char* rec = lds + L_RECF + (4 * 2 + r) * ROW;
      *(LAS f32x4*)(rec + ch * 16) = o;
      if (ch == 0) *(LAS f32x2*)(rec + 512) = f32x2{M, L};
    }
    STAMP(5);
    wg_barrier();
    STAMP(6);
    if (blk == 0) {
      for (unsigned spins = 0; spins < (1u << 20); ++spins) {
        bool ok = true;
#pragma unroll
        for (int k = 0; k < 4; ++k) ok &= __hip_atomic_load((gu64*)(a.ws + W_FLAG + (size_t)(k * 64 + lane) * FLAG_STRIDE), RLX_AGENT) == tag;
        if (__all(ok)) break;
        __builtin_amdgcn_s_sleep(8);
      }
      if (lane == 0) __hip_atomic_store((gu64*)(a.ws + W_EPOCH), tag, RLX_AGENT);
    }
  }
}

__global__ void probe(const unsigned* k, const unsigned* v, unsigned* ws) {
  if (threadIdx.x != 0) return;
  const unsigned strides[5] = {4096u, 16384u, 65536u, 524288u, 2097152u};
  for (int t = 0; t < 2; ++t) {
    const unsigned* p = t ? v : k;
    for (int s = 0; s < 5; ++s) {
      unsigned long long tot = 0, mx = 0; unsigned idx = 0; int slow = 0;
      const unsigned base = 7u * 2097152u * (unsigned)(s + 1) + 128u * 37u;
      for (int i = 1; i <= 32; ++i) {
        const unsigned off = (base + (unsigned)i * strides[s]) % 140000000u;
        const unsigned long long t0 = __builtin_amdgcn_s_memtime();
        unsigned x = __builtin_nontemporal_load(p + (off >> 2) + idx);
        asm volatile("s_waitcnt vmcnt(0)" : "+v"(x) :: "memory");
        const unsigned long long t1 = __builtin_amdgcn_s_memtime();
        idx = x & 0; asm volatile("" : "+v"(idx));
        tot += t1 - t0; mx = (t1 - t0) > mx ? (t1 - t0) : mx; slow += (t1 - t0) > 1500;
      }
      printf("probe %c stride %7u: mean %llu max %llu slow(>1500) %d\n", t ? 'V' : 'K', strides[s], tot / 32, mx, slow);
    }
  }
}

extern "C" void kernel_launch(void* const* d_in, const int* in_sizes, int n_in, void* d_out, int out_size, void* d_ws, size_t ws_size, hipStream_t stream) {
  Args a;
  a.q = (const bf16x8*)d_in[0]; a.k = d_in[1]; a.v = d_in[2]; a.bt = (const int*)d_in[3]; a.lens = (const int*)d_in[4];
  a.out = (unsigned*)d_out; a.ws = (unsigned char*)d_ws; a.kv_bytes = (unsigned)in_sizes[1] * 2u; a.pad = 0;
  fprintf(stderr, "PTRS q %p k %p v %p bt %p lens %p out %p ws %p\n", d_in[0], d_in[1], d_in[2], d_in[3], d_in[4], d_out, d_ws);
  hipLaunchKernelGGL(probe, dim3(1), dim3(64), 0, stream, (const unsigned*)d_in[1], (const unsigned*)d_in[2], (unsigned*)d_ws);
  hipFuncSetAttribute((const void*)radix_attn, hipFuncAttributeMaxDynamicSharedMemorySize, LDS_BYTES);
  hipLaunchKernelGGL(radix_attn, dim3(256), dim3(NTHR), LDS_BYTES, stream, a);
}
```

```cpp
#define TLBPF 0
#include <hip/hip_runtime.h>
#include <hip/hip_bf16.h>
#include <stdint.h>

#define NSLOT 16
#define INFL 20
#define KV_NT 1
#define WARM 1
#define DIAG 0
#define MAPSWAP 1

constexpr int B = 16, HQ = 32, HKV = 16, D = 128, PAGE = 16, NB = 256, LK = NB * PAGE;
constexpr int NSW = 4, NTHR = (NSW + 2) * 64;
constexpr float C2 = 0.088388347648318440f * 1.4426950408889634f;
constexpr unsigned PAGE_BYTES = PAGE * HKV * D * 2;
constexpr unsigned TOK_BYTES = HKV * D * 2;

constexpr int ROW = 528;
constexpr int L_RING = 0;
constexpr int L_MRG = NSLOT * 8192;
constexpr int L_RECX = L_MRG + 32 * ROW;
constexpr int L_RECF = L_RECX + 4 * 2 * ROW;
constexpr int L_MISC = L_RECF + 5 * 2 * ROW;
constexpr int C_LANDED = 0, C_REL = 4, C_CSTEP = 20, C_RCNT = 24, C_RFREE = 28, C_P = 32;
constexpr int LDS_BYTES = L_MISC + 64;
static_assert(NSLOT % 4 == 0 && INFL % 4 == 0 && (NSLOT & (NSLOT - 1)) == 0, "ring geometry");
static_assert(LDS_BYTES <= 160 * 1024, "LDS");

constexpr size_t W_EPOCH = 0, W_FLAG = 256, FLAG_STRIDE = 128;
constexpr size_t W_PART = 65536;
constexpr unsigned PS_BYTES = 32 * ROW, PX_BYTES = 2 * ROW;
constexpr unsigned W_PX = 256 * PS_BYTES;
constexpr unsigned PART_BYTES = W_PX + 256 * 16 * PX_BYTES;
#define STAMP(k) do {} while (0)

typedef __bf16 bf16x8 __attribute__((ext_vector_type(8)));
typedef __bf16 bf16x2 __attribute__((ext_vector_type(2)));
typedef short s16x4 __attribute__((ext_vector_type(4)));
typedef float f32x4 __attribute__((ext_vector_type(4)));
typedef float f32x2 __attribute__((ext_vector_type(2)));
typedef unsigned u32x4 __attribute__((ext_vector_type(4)));
typedef unsigned u32x2 __attribute__((ext_vector_type(2)));
typedef int i32x4 __attribute__((ext_vector_type(4)));
#define LAS __attribute__((address_space(3)))
#define GAS __attribute__((address_space(1)))
#define DI __device__ __forceinline__
typedef GAS unsigned long long gu64;
#define RLX_AGENT __ATOMIC_RELAXED, __HIP_MEMORY_SCOPE_AGENT

DI int rfl(int x) { return __builtin_amdgcn_readfirstlane(x); }
DI int rdl(int x, int l) { return __builtin_amdgcn_readlane(x, l); }
template <int CTRL> DI int dpp_i(int oldv, int x) { return __builtin_amdgcn_update_dpp(oldv, x, CTRL, 0xf, 0xf, false); }
DI int row_scan_add(int x) {
  x += dpp_i<0x111>(0, x); x += dpp_i<0x112>(0, x); x += dpp_i<0x114>(0, x); x += dpp_i<0x118>(0, x); return x;
}
DI int row_scan_max(int x) {
  x = max(x, dpp_i<0x111>(0, x)); x = max(x, dpp_i<0x112>(0, x)); x = max(x, dpp_i<0x114>(0, x)); x = max(x, dpp_i<0x118>(0, x)); return x;
}
DI float xrow16_max(float x) {
  auto s = __builtin_amdgcn_permlane16_swap(__float_as_uint(x), __float_as_uint(x), false, false);
  x = fmaxf(__uint_as_float(s[0]), __uint_as_float(s[1]));
  auto t = __builtin_amdgcn_permlane32_swap(__float_as_uint(x), __float_as_uint(x), false, false);
  return fmaxf(__uint_as_float(t[0]), __uint_as_float(t[1]));
}
DI float xrow16_sum(float x) {
  auto s = __builtin_amdgcn_permlane16_swap(__float_as_uint(x), __float_as_uint(x), false, false);
  x = __uint_as_float(s[0]) + __uint_as_float(s[1]);
  auto t = __builtin_amdgcn_permlane32_swap(__float_as_uint(x), __float_as_uint(x), false, false);
  return __uint_as_float(t[0]) + __uint_as_float(t[1]);
}
DI unsigned cvtpk(float lo, float hi) { f32x2 v = {lo, hi}; bf16x2 b = __builtin_convertvector(v, bf16x2); return __builtin_bit_cast(unsigned, b); }
DI float ex2(float x) { return __builtin_amdgcn_exp2f(x); }
DI void wg_barrier() { asm volatile("s_waitcnt lgkmcnt(0)\n\ts_barrier" ::: "memory"); }
template <int N> DI void wait_vm() { asm volatile("s_waitcnt vmcnt(%0)" :: "i"(N) : "memory"); }

#define NTS " nt"
DI void dma4(unsigned m0v, __amdgpu_buffer_rsrc_t rsrc, unsigned soff, unsigned v0, unsigned v1, unsigned v2, unsigned v3) {
  asm volatile("s_mov_b32 m0, %0\n\t"
               "s_nop 4\n\t"
               "buffer_load_dwordx4 %1, %5, %6 offen" NTS " lds\n\t"
               "buffer_load_dwordx4 %2, %5, %6 offen offset:1024" NTS " lds\n\t"
               "buffer_load_dwordx4 %3, %5, %6 offen offset:2048" NTS " lds\n\t"
               "buffer_load_dwordx4 %4, %5, %6 offen offset:3072" NTS " lds"
               :: "s"(m0v), "v"(v0), "v"(v1), "v"(v2), "v"(v3), "s"(rsrc), "s"(soff) : "memory");
}

struct Args { const bf16x8* q; const void* k; const void* v; const int* bt; const int* lens; unsigned* out; unsigned char* ws; unsigned kv_bytes; unsigned pad; };

DI float mfac(float m, float M) { return (m == -INFINITY) ? 0.f : ex2((m - M) * C2); }

struct Soft { float m, l; f32x4 o[8]; };
DI void soft_reset(Soft& s) { s.m = -INFINITY; s.l = 0.f;
#pragma unroll
  for (int i = 0; i < 8; ++i) s.o[i] = f32x4{0.f, 0.f, 0.f, 0.f}; }

DI s16x4 soft_step(Soft& s, f32x4 x, bool mask, int tok0, int len) {
  if (mask) {
#pragma unroll
    for (int r = 0; r < 4; ++r) x[r] = (tok0 + r < len) ? x[r] : -INFINITY;
  }
  float pm = fmaxf(fmaxf(x[0], x[1]), fmaxf(x[2], x[3]));
  pm = xrow16_max(pm);
  const float mn = fmaxf(s.m, pm);
  const float ms = (mn == -INFINITY) ? 0.f : mn * C2;
  const float alpha = ex2(s.m * C2 - ms);
  float p[4];
#pragma unroll
  for (int r = 0; r < 4; ++r) p[r] = ex2(fmaf(x[r], C2, -ms));
  s.l = fmaf(s.l, alpha, (p[0] + p[1]) + (p[2] + p[3]));
  s.m = mn;
#pragma unroll
  for (int i = 0; i < 8; ++i) s.o[i] *= alpha;
  u32x2 pk = {cvtpk(p[0], p[1]), cvtpk(p[2], p[3])};
  return __builtin_bit_cast(s16x4, pk);
}

DI int lds_ld(LAS unsigned char* p) { return __hip_atomic_load((LAS int*)p, __ATOMIC_RELAXED, __HIP_MEMORY_SCOPE_WORKGROUP); }
DI void lds_st(LAS unsigned char* p, int v) { __hip_atomic_store((LAS int*)p, v, __ATOMIC_RELAXED, __HIP_MEMORY_SCOPE_WORKGROUP); }
DI int lds_wait_ge(LAS unsigned char* p, int need) {
  int v;
#pragma nounroll
  for (unsigned spins = 0;; ++spins) { v = rfl(lds_ld(p)); if (v >= need || spins > (1u << 24)) break; __builtin_amdgcn_s_sleep(1); }
  asm volatile("" ::: "memory");
  return v;
}
DI void lds_drain() { asm volatile("s_waitcnt lgkmcnt(0)" ::: "memory"); }

__global__ void __launch_bounds__(NTHR, 2) radix_attn(Args a) {
  extern __shared__ __attribute__((aligned(16))) unsigned char lds_raw[];
  LAS unsigned char* lds = (LAS unsigned char*)lds_raw;
  LAS unsigned char* ctl = lds + L_MISC;
  const int tid = threadIdx.x, lane = tid & 63, wave = rfl(tid >> 6);
  const int w = blockIdx.x & 15, h = blockIdx.x >> 4;
  const int c = lane & 15, g = lane >> 4;
  asm volatile("" :: "s"(a.q), "s"(a.k), "s"(a.v), "s"(a.bt), "s"(a.lens), "s"(a.out), "s"(a.ws), "s"(a.kv_bytes));
  const int len_raw = a.lens[c];

  struct Part { int len, Pe, S, E, spare, exc, lo, hi, xoff, S_w, spare_w, T_w, ns; unsigned xmask; bool all_masked; };
  auto partition = [&](int P) {
    Part p;
    p.all_masked = len_raw <= 0;
    p.len = p.all_masked ? LK : min(len_raw, LK);
    const int np = (p.len + PAGE - 1) >> 4;
    p.Pe = min(P, rdl(row_scan_max(np), 15));
    const int n = max(np - p.Pe, 0);
    const int W = p.Pe + rdl(row_scan_add(n), 15);
    const int T = (W >> 4) + (c < (W & 15) ? 1 : 0);
    const int own = min(n, T);
    p.exc = n - own; p.spare = T - own;
    p.S = row_scan_add(p.spare) - p.spare;
    p.E = p.Pe + row_scan_add(p.exc) - p.exc;
    p.S_w = rdl(p.S, w); p.spare_w = rdl(p.spare, w); p.T_w = rdl(T, w);
    p.ns = max(0, min(p.S_w + p.spare_w, p.Pe) - p.S_w);
    p.lo = max(p.S_w, p.E); p.hi = min(p.S_w + p.spare_w, p.E + p.exc);
    p.xmask = (unsigned)(__ballot(p.hi > p.lo) & 0xFFFFull);
    p.xoff = p.Pe + own - p.E;
    return p;
  };

  if (__builtin_expect(wave == 0, 1)) {
    __builtin_amdgcn_s_setprio(3);
    const i32x4* bt4 = (const i32x4*)a.bt;
    i32x4 rk16[16];
#pragma unroll
    for (int k = 0; k < 16; ++k) rk16[k] = bt4[k * (NB / 4) + lane];
    STAMP(8);
    i32x4 acc = rk16[1] ^ rk16[0];
#pragma unroll
    for (int k = 2; k < 16; ++k) acc |= rk16[k] ^ rk16[0];
#pragma unroll
    for (int k = 0; k < 16; ++k) *(LAS i32x4*)(lds + L_MRG + (k * NB + 4 * lane) * 4) = rk16[k];
    int P = NB;
#pragma unroll
    for (int e = 0; e < 4; ++e) {
      const unsigned long long bal = __ballot(acc[e] != 0);
      P = min(P, bal ? 4 * (int)__builtin_ctzll(bal) + e : NB);
    }
    if (lane == 0) *(LAS int*)(ctl + C_P) = P;
    wg_barrier();
    STAMP(2);
    const Part p = partition(P);
    const int Pe = p.Pe, S_w = p.S_w, spare_w = p.spare_w, T_w = p.T_w, lo = p.lo, hi = p.hi, xoff = p.xoff; const unsigned xmask = p.xmask;
    STAMP(7);
    unsigned vp[4];
    {
      int idx[4];
#pragma unroll
      for (int r = 0; r < 4; ++r) { const int i = 64 * r + lane; idx[r] = (i < spare_w) ? S_w + i : w * NB + Pe + (i - spare_w); }
      for (unsigned m = xmask; m; m &= m - 1) {
        const int b = __builtin_ctz(m);
        const int lo_b = rdl(lo, b) - S_w, hi_b = rdl(hi, b) - S_w, off_b = rdl(xoff, b) + b * NB + S_w;
#pragma unroll
        for (int r = 0; r < 4; ++r) { const int i = 64 * r + lane; if (i >= lo_b && i < hi_b) idx[r] = i + off_b; }
      }
#pragma unroll
      for (int r = 0; r < 4; ++r) { const int i = 64 * r + lane; vp[r] = (unsigned)*(LAS int*)(lds + L_MRG + 4 * (i < T_w ? idx[r] : 0)) * PAGE_BYTES + (unsigned)h * (D * 2); }
    }
    unsigned vk[4], vv[4];
#pragma unroll
    for (int i = 0; i < 4; ++i) { const int t = 4 * i + g;
      vk[i] = t * TOK_BYTES + ((c ^ t) << 4) - 1024 * i;
      vv[i] = t * TOK_BYTES + ((c ^ ((t & 7) << 1)) << 4) - 1024 * i; }
    const unsigned ring = (unsigned)(uintptr_t)(lds + L_RING);
    const __amdgpu_buffer_rsrc_t rk = __builtin_amdgcn_make_buffer_rsrc((void*)a.k, 0, a.kv_bytes, 0x00020000);
    const __amdgpu_buffer_rsrc_t rv = __builtin_amdgcn_make_buffer_rsrc((void*)a.v, 0, a.kv_bytes, 0x00020000);
    asm volatile("" : "+v"(vp[0]), "+v"(vp[1]), "+v"(vp[2]), "+v"(vp[3]));
    STAMP(3);
    int pub = 0, relnext = 0;
    auto publish = [&](int nl) { if (nl > pub) { pub = nl; lds_st(ctl + C_LANDED, nl); } };
    for (int i = 0; i < T_w; ++i) {
      if (i >= NSLOT) {
        const int need = (i >> 2) - NSLOT / 4 + 1;
        if (rfl(relnext) < need) {
          LAS unsigned char* rel = ctl + C_REL + 4 * (i & 3);
          if (rfl(lds_ld(rel)) < need) {
            wait_vm<0>(); publish(2 * i);
            lds_wait_ge(rel, need);
          }
        }
        asm volatile("" ::: "memory");
      }
      const unsigned so = (unsigned)rdl((int)vp[0], i & 63);
      const unsigned m0v = ring + (i & (NSLOT - 1)) * 8192;
      dma4(m0v, rk, so, vk[0], vk[1], vk[2], vk[3]);
      publish(2 * i - INFL / 4);
      relnext = lds_ld(ctl + C_REL + 4 * ((i + 1) & 3));
      if ((i & 63) == 63) { vp[0] = vp[1]; vp[1] = vp[2]; vp[2] = vp[3]; }
      wait_vm<INFL>();
      dma4(m0v + 4096, rv, so, vv[0], vv[1], vv[2], vv[3]);
      publish(2 * i + 1 - INFL / 4);
      wait_vm<INFL>();
    }
    STAMP(5);
    publish(2 * T_w - INFL / 4);
    if (INFL > 16) { wait_vm<16>(); publish(2 * T_w - 4); }
    if (INFL > 12) { wait_vm<12>(); publish(2 * T_w - 3); }
    if (INFL > 8) { wait_vm<8>(); publish(2 * T_w - 2); }
    wait_vm<4>(); publish(2 * T_w - 1);
    wait_vm<0>(); publish(2 * T_w);
    STAMP(6);
  } else if (wave <= NSW) {
    const int cw = wave - 1;
    bf16x8 qf[2][4];
    __builtin_amdgcn_s_sleep(6);
#pragma unroll
    for (int cg = 0; cg < 2; ++cg)
#pragma unroll
      for (int s = 0; s < 4; ++s) { const int r = 16 * cg + c; qf[cg][s] = a.q[(((r >> 1) * HQ + 2 * h + (r & 1)) * D + 32 * s + 8 * g) / 8]; }
    wg_barrier();
    STAMP(2);
    const Part p = partition(rfl(*(LAS int*)(ctl + C_P)));
    const int len = p.len, Pe = p.Pe, S_w = p.S_w, spare_w = p.spare_w, T_w = p.T_w, lo = p.lo, hi = p.hi, xoff = p.xoff, ns = p.ns; const unsigned xmask = p.xmask; const bool all_masked = p.all_masked;
    const int minlen = LK - rdl(row_scan_max(LK - len), 15);
    {
      const bool z0 = __shfl((int)all_masked, c >> 1) != 0, z1 = __shfl((int)all_masked, 8 + (c >> 1)) != 0;
#pragma unroll
      for (int s = 0; s < 4; ++s) {
        u32x4 t0 = __builtin_bit_cast(u32x4, qf[0][s]), t1 = __builtin_bit_cast(u32x4, qf[1][s]);
#pragma unroll
        for (int e = 0; e < 4; ++e) { t0[e] = z0 ? 0u : t0[e]; t1[e] = z1 ? 0u : t1[e]; }
        qf[0][s] = __builtin_bit_cast(bf16x8, t0); qf[1][s] = __builtin_bit_cast(bf16x8, t1);
      }
    }
    const int len0 = __shfl(len, c >> 1), len1 = __shfl(len, 8 + (c >> 1));
    const unsigned ka0 = c * 256 + ((g ^ c) << 4);
    const int tv = 4 * g + (c >> 2);
    const unsigned va0 = 4096 + tv * 256 + ((tv & 7) << 5) + (c & 3) * 8;
    int j = 0, seen = 0;
    Soft s0, s1;
    auto page = [&](auto SHc, const bf16x8 (&qa)[4], const bf16x8 (&qb)[4], int la, int lb, int t0, bool mask) {
      constexpr bool SH = decltype(SHc)::value;
      const int i = 4 * j + cw;
      LAS unsigned char* sl = lds + L_RING + (i & (NSLOT - 1)) * 8192;
      if (seen < 2 * i + 1) seen = lds_wait_ge(ctl + C_LANDED, 2 * i + 1);
      bf16x8 kf[4];
#pragma unroll
      for (int s = 0; s < 4; ++s) kf[s] = *(LAS bf16x8*)(sl + (ka0 ^ (s << 6)));
      f32x4 xa = {0.f, 0.f, 0.f, 0.f}, xb = {0.f, 0.f, 0.f, 0.f};
#pragma unroll
      for (int s = 0; s < 4; ++s) { xa = __builtin_amdgcn_mfma_f32_16x16x32_bf16(kf[s], qa[s], xa, 0, 0, 0);
        if (SH) xb = __builtin_amdgcn_mfma_f32_16x16x32_bf16(kf[s], qb[s], xb, 0, 0, 0); }
      const s16x4 pa = soft_step(s0, xa, mask, t0 + 4 * g, la);
      s16x4 pb; if (SH) pb = soft_step(s1, xb, mask, t0 + 4 * g, lb);
      if (seen < 2 * i + 2) seen = lds_wait_ge(ctl + C_LANDED, 2 * i + 2);
      s16x4 vf[8];
#pragma unroll
      for (int blk = 0; blk < 8; ++blk) vf[blk] = __builtin_amdgcn_ds_read_tr16_b64_v4i16((LAS s16x4*)(sl + (va0 ^ (blk << 5))));
      ++j;
      asm volatile("s_waitcnt lgkmcnt(0)" : "+v"(vf[0]), "+v"(vf[1]), "+v"(vf[2]), "+v"(vf[3]), "+v"(vf[4]), "+v"(vf[5]), "+v"(vf[6]), "+v"(vf[7]) :: "memory");
      lds_st(ctl + C_REL + 4 * cw, j);
#pragma unroll
      for (int blk = 0; blk < 8; ++blk) { s0.o[blk] = __builtin_amdgcn_mfma_f32_16x16x16bf16_1k(vf[blk], pa, s0.o[blk], 0, 0, 0);
        if (SH) s1.o[blk] = __builtin_amdgcn_mfma_f32_16x16x16bf16_1k(vf[blk], pb, s1.o[blk], 0, 0, 0); }
    };
    int npass = WARM ? 2 : 1;
    asm volatile("" : "+s"(npass));
#pragma nounroll
    for (int pass = 2 - npass; pass < 2; ++pass) {
      const bool real = pass != 0;
      if (real && ns > 0) {
        soft_reset(s0); soft_reset(s1);
        for (int i = cw; i < ns; i += 4) { const int t0 = (S_w + i) * PAGE; page(std::true_type{}, qf[0], qf[1], len0, len1, t0, t0 + PAGE > minlen); }
        s0.l = xrow16_sum(s0.l); s1.l = xrow16_sum(s1.l);
        if (cw > 0) lds_wait_ge(ctl + C_CSTEP, cw);
#pragma unroll
        for (int cg = 0; cg < 2; ++cg) { Soft& s = cg ? s1 : s0;
          LAS unsigned char* r = lds + L_MRG + (16 * cg + c) * ROW;
          if (cw > 0) {
            const f32x2 ml = *(LAS f32x2*)(r + 512);
            const float M = fmaxf(ml[0], s.m), f0 = mfac(ml[0], M), f1 = mfac(s.m, M);
#pragma unroll
            for (int blk = 0; blk < 8; ++blk) s.o[blk] = *(LAS f32x4*)(r + blk * 64 + g * 16) * f0 + s.o[blk] * f1;
            s.l = ml[1] * f0 + s.l * f1; s.m = M;
          }
#pragma unroll
          for (int blk = 0; blk < 8; ++blk) *(LAS f32x4*)(r + blk * 64 + g * 16) = s.o[blk];
          lds_drain();
          if (g == 0) *(LAS f32x2*)(r + 512) = f32x2{s.m, s.l};
        }
        lds_drain();
        lds_st(ctl + C_CSTEP, cw + 1);
      }
      unsigned m = real ? xmask : 0u;
      int kx = 0;
      for (;;) {
        const bool last = m == 0;
        const int b = last ? w : __builtin_ctz(m);
        const int i0 = last ? spare_w : rdl(lo, b) - S_w;
        const int i1 = last ? (real ? T_w : 0) : rdl(hi, b) - S_w;
        const int lp0 = last ? Pe : rdl(lo, b) + rdl(xoff, b);
        bf16x8 qa[4];
        if (b < 8) {
#pragma unroll
          for (int s = 0; s < 4; ++s) qa[s] = qf[0][s];
        } else {
#pragma unroll
          for (int s = 0; s < 4; ++s) qa[s] = qf[1][s];
        }
        const int lb = rdl(len, b);
        soft_reset(s0);
        for (int i = 4 * j + cw; i < i1; i += 4) { const int t0 = (lp0 + i - i0) * PAGE; page(std::false_type{}, qa, qa, lb, lb, t0, t0 + PAGE > lb); }
        if (!last && kx > 0) lds_wait_ge(ctl + C_RFREE, kx);
        const float lt = xrow16_sum(s0.l);
        if ((c >> 1) == (b & 7)) {
          LAS unsigned char* r = lds + (last ? L_RECF : L_RECX) + (cw * 2 + (c & 1)) * ROW;
#pragma unroll
          for (int blk = 0; blk < 8; ++blk) *(LAS f32x4*)(r + blk * 64 + g * 16) = s0.o[blk];
          if (g == 0) *(LAS f32x2*)(r + 512) = f32x2{s0.m, lt};
        }
        if (last) break;
        lds_drain();
        if (lane == 0) __hip_atomic_fetch_add((LAS int*)(ctl + C_RCNT), 1, __ATOMIC_RELAXED, __HIP_MEMORY_SCOPE_WORKGROUP);
        m &= m - 1; ++kx;
      }
      if (real) wg_barrier();
      if (cw < 2) {
        const int r = cw, d0 = 2 * lane;
        float M = -INFINITY; f32x2 ml[5], o[5];
#pragma unroll
        for (int k = 0; k < 5; ++k) { ml[k] = *(LAS f32x2*)(lds + L_RECF + (k * 2 + r) * ROW + 512); o[k] = *(LAS f32x2*)(lds + L_RECF + (k * 2 + r) * ROW + d0 * 4); }
#pragma unroll
        for (int k = 0; k < 5; ++k) M = fmaxf(M, ml[k][0]);
        float L = 0.f, o0 = 0.f, o1 = 0.f;
#pragma unroll
        for (int k = 0; k < 5; ++k) { const float f = mfac(ml[k][0], M); L = fmaf(f, ml[k][1], L); o0 = fmaf(f, o[k][0], o0); o1 = fmaf(f, o[k][1], o1); }
        const float inv = __builtin_amdgcn_rcpf(L);
        unsigned res = cvtpk(o0 * inv, o1 * inv);
        asm volatile("" : "+v"(res));
        if (real) a.out[((w * HQ + 2 * h + r) * D + d0) >> 1] = res;
      }
    }
  } else {
    const __amdgpu_buffer_rsrc_t prsrc = __builtin_amdgcn_make_buffer_rsrc(a.ws + W_PART, 0, PART_BYTES, 0x00020000);
    if (lane < 8) *(LAS int*)(ctl + 4 * lane) = 0;
    const unsigned long long epoch = __hip_atomic_load((gu64*)(a.ws + W_EPOCH), RLX_AGENT);
    wg_barrier();
    STAMP(2);
    const Part p = partition(rfl(*(LAS int*)(ctl + C_P)));
    const unsigned long long tag = epoch + 1ull;
    const int Pe = p.Pe, S = p.S, E = p.E, spare = p.spare, exc = p.exc, ns = p.ns; const unsigned xmask = p.xmask;
    const unsigned blk = w * 16 + h;
    const int r = lane >> 5, ch = lane & 31;
    if (ns > 0) {
      lds_wait_ge(ctl + C_CSTEP, NSW);
#pragma unroll 4
      for (int k = 0; k < 16; ++k) { const int off = (2 * k + r) * ROW + ch * 16;
        const u32x4 v = *(LAS u32x4*)(lds + L_MRG + off);
        __builtin_amdgcn_raw_buffer_store_b128(v, prsrc, off, blk * PS_BYTES, 16); }
      if (lane < 32) { const u32x2 v = *(LAS u32x2*)(lds + L_MRG + lane * ROW + 512);
        __builtin_amdgcn_raw_buffer_store_b64(v, prsrc, lane * ROW + 512, blk * PS_BYTES, 16); }
    }
    int kx = 0;
    for (unsigned m = xmask; m; m &= m - 1, ++kx) {
      const int b = __builtin_ctz(m);
      lds_wait_ge(ctl + C_RCNT, NSW * (kx + 1));
      float M = -INFINITY; f32x2 ml[4]; f32x4 ok[4];
#pragma unroll
      for (int k = 0; k < 4; ++k) { ml[k] = *(LAS f32x2*)(lds + L_RECX + (k * 2 + r) * ROW + 512); ok[k] = *(LAS f32x4*)(lds + L_RECX + (k * 2 + r) * ROW + ch * 16); M = fmaxf(M, ml[k][0]); }
      float L = 0.f; f32x4 o = {0.f, 0.f, 0.f, 0.f};
#pragma unroll
      for (int k = 0; k < 4; ++k) { const float f = mfac(ml[k][0], M); L = fmaf(f, ml[k][1], L); o += ok[k] * f; }
      asm volatile("s_waitcnt lgkmcnt(0)" : "+v"(o) :: "memory");
      lds_st(ctl + C_RFREE, kx + 1);
      const unsigned so = W_PX + (blk * 16 + b) * PX_BYTES;
      __builtin_amdgcn_raw_buffer_store_b128(__builtin_bit_cast(u32x4, o), prsrc, r * ROW + ch * 16, so, 16);
      if (ch == 0) __builtin_amdgcn_raw_buffer_store_b64(__builtin_bit_cast(u32x2, f32x2{M, L}), prsrc, r * ROW + 512, so, 16);
    }
    asm volatile("s_waitcnt vmcnt(0)" ::: "memory");
    if (lane == 0) __hip_atomic_store((gu64*)(a.ws + W_FLAG + blk * FLAG_STRIDE), tag, RLX_AGENT);
    STAMP(3);
    const int E_w = rdl(E, w), exc_w = rdl(exc, w);
    const bool sh_ov = spare > 0 && S < Pe;
    const bool ex_ov = min(S + spare, E_w + exc_w) > max(S, E_w);
    unsigned cmask = (unsigned)(__ballot(sh_ov) & 0xFFFFull) | ((unsigned)(__ballot(ex_ov) & 0xFFFFull) << 16);
    {
      const bool need = lane < 16 && (sh_ov || ex_ov);
      gu64* f = (gu64*)(a.ws + W_FLAG + (size_t)(c * 16 + h) * FLAG_STRIDE);
#pragma nounroll
      for (unsigned spins = 0; spins < (1u << 20); ++spins) {
        const bool ok = !need || __hip_atomic_load(f, RLX_AGENT) == tag;
        if (__all(ok)) break;
        __builtin_amdgcn_s_sleep(32);
      }
      asm volatile("" ::: "memory");
    }
    STAMP(4);
    {
      const unsigned vso = lane < 16 ? (unsigned)(c * 16 + h) * PS_BYTES + (unsigned)(2 * w) * ROW : W_PX + ((unsigned)(c * 16 + h) * 16 + w) * PX_BYTES;
      float M = -INFINITY, L = 0.f; f32x4 o = {0.f, 0.f, 0.f, 0.f};
      constexpr int GB = 10;
      while (cmask) {
        u32x4 po[GB]; u32x2 pml[GB]; bool pv[GB];
#pragma unroll
        for (int k = 0; k < GB; ++k) {
          pv[k] = cmask != 0;
          const int id = pv[k] ? __builtin_ctz(cmask) : 0; cmask &= cmask - 1;
          const unsigned so = pv[k] ? (unsigned)rdl((int)vso, id) : PART_BYTES;
          po[k] = __builtin_amdgcn_raw_buffer_load_b128(prsrc, r * ROW + ch * 16 + so, 0, 16);
          pml[k] = __builtin_amdgcn_raw_buffer_load_b64(prsrc, r * ROW + 512 + so, 0, 16);
        }
#pragma unroll
        for (int k = 0; k < GB; ++k) {
          const float mk = pv[k] ? __uint_as_float(pml[k][0]) : -INFINITY, lk = __uint_as_float(pml[k][1]);
          const float Mn = fmaxf(M, mk), f0 = mfac(M, Mn), f1 = mfac(mk, Mn);
          o = o * f0 + __builtin_bit_cast(f32x4, po[k]) * f1; L = L * f0 + lk * f1; M = Mn;
        }
      }
      LAS unsigned char* rec = lds + L_RECF + (4 * 2 + r) * ROW;
      *(LAS f32x4*)(rec + ch * 16) = o;
      if (ch == 0) *(LAS f32x2*)(rec + 512) = f32x2{M, L};
    }
    STAMP(5);
    wg_barrier();
    STAMP(6);
    if (blk == 0) {
#pragma nounroll
      for (unsigned spins = 0; spins < (1u << 20); ++spins) {
        bool ok = true;
#pragma unroll
        for (int k = 0; k < 4; ++k) ok &= __hip_atomic_load((gu64*)(a.ws + W_FLAG + (size_t)(k * 64 + lane) * FLAG_STRIDE), RLX_AGENT) == tag;
        if (__all(ok)) break;
        __builtin_amdgcn_s_sleep(8);
      }
      if (lane == 0) __hip_atomic_store((gu64*)(a.ws + W_EPOCH), tag, RLX_AGENT);
    }
  }
}

extern "C" void kernel_launch(void* const* d_in, const int* in_sizes, int n_in, void* d_out, int out_size, void* d_ws, size_t ws_size, hipStream_t stream) {
  Args a;
  a.q = (const bf16x8*)d_in[0]; a.k = d_in[1]; a.v = d_in[2]; a.bt = (const int*)d_in[3]; a.lens = (const int*)d_in[4];
  a.out = (unsigned*)d_out; a.ws = (unsigned char*)d_ws; a.kv_bytes = (unsigned)in_sizes[1] * 2u; a.pad = 0;
  hipFuncSetAttribute((const void*)radix_attn, hipFuncAttributeMaxDynamicSharedMemorySize, LDS_BYTES);
  hipLaunchKernelGGL(radix_attn, dim3(256), dim3(NTHR), LDS_BYTES, stream, a);
}
```

```cpp
#define TLBPF 0
#define PF 1
#include <hip/hip_runtime.h>
#include <hip/hip_bf16.h>
#include <stdint.h>
#include <stdio.h>

#define NSLOT 16
#define INFL 20
#define KV_NT 1
#define WARM 1
#define DIAG 0
#define MAPSWAP 1

constexpr int B = 16, HQ = 32, HKV = 16, D = 128, PAGE = 16, NB = 256, LK = NB * PAGE;
constexpr int NSW = 4, NTHR = (NSW + 2) * 64;
constexpr float C2 = 0.088388347648318440f * 1.4426950408889634f;
constexpr unsigned PAGE_BYTES = PAGE * HKV * D * 2;
constexpr unsigned TOK_BYTES = HKV * D * 2;

constexpr int ROW = 528;
constexpr int L_RING = 0;
constexpr int L_MRG = NSLOT * 8192;
constexpr int L_RECX = L_MRG + 32 * ROW;
constexpr int L_RECF = L_RECX + 4 * 2 * ROW;
constexpr int L_MISC = L_RECF + 5 * 2 * ROW;
constexpr int C_LANDED = 0, C_REL = 4, C_CSTEP = 20, C_RCNT = 24, C_RFREE = 28, C_P = 32;
constexpr int LDS_BYTES = L_MISC + 64;
static_assert(NSLOT % 4 == 0 && INFL % 4 == 0 && (NSLOT & (NSLOT - 1)) == 0, "ring geometry");
static_assert(LDS_BYTES <= 160 * 1024, "LDS");

constexpr size_t W_EPOCH = 0, W_FLAG = 256, FLAG_STRIDE = 128;
constexpr size_t W_PART = 65536;
constexpr unsigned PS_BYTES = 32 * ROW, PX_BYTES = 2 * ROW;
constexpr unsigned W_PX = 256 * PS_BYTES;
constexpr unsigned PART_BYTES = W_PX + 256 * 16 * PX_BYTES;
#define STAMP(k) do {} while (0)

typedef __bf16 bf16x8 __attribute__((ext_vector_type(8)));
typedef __bf16 bf16x2 __attribute__((ext_vector_type(2)));
typedef short s16x4 __attribute__((ext_vector_type(4)));
typedef float f32x4 __attribute__((ext_vector_type(4)));
typedef float f32x2 __attribute__((ext_vector_type(2)));
typedef unsigned u32x4 __attribute__((ext_vector_type(4)));
typedef unsigned u32x2 __attribute__((ext_vector_type(2)));
typedef int i32x4 __attribute__((ext_vector_type(4)));
#define LAS __attribute__((address_space(3)))
#define GAS __attribute__((address_space(1)))
#define DI __device__ __forceinline__
typedef GAS unsigned long long gu64;
#define RLX_AGENT __ATOMIC_RELAXED, __HIP_MEMORY_SCOPE_AGENT

DI int rfl(int x) { return __builtin_amdgcn_readfirstlane(x); }
DI int rdl(int x, int l) { return __builtin_amdgcn_readlane(x, l); }
template <int CTRL> DI int dpp_i(int oldv, int x) { return __builtin_amdgcn_update_dpp(oldv, x, CTRL, 0xf, 0xf, false); }
DI int row_scan_add(int x) {
  x += dpp_i<0x111>(0, x); x += dpp_i<0x112>(0, x); x += dpp_i<0x114>(0, x); x += dpp_i<0x118>(0, x); return x;
}
DI int row_scan_max(int x) {
  x = max(x, dpp_i<0x111>(0, x)); x = max(x, dpp_i<0x112>(0, x)); x = max(x, dpp_i<0x114>(0, x)); x = max(x, dpp_i<0x118>(0, x)); return x;
}
DI float xrow16_max(float x) {
  auto s = __builtin_amdgcn_permlane16_swap(__float_as_uint(x), __float_as_uint(x), false, false);
  x = fmaxf(__uint_as_float(s[0]), __uint_as_float(s[1]));
  auto t = __builtin_amdgcn_permlane32_swap(__float_as_uint(x), __float_as_uint(x), false, false);
  return fmaxf(__uint_as_float(t[0]), __uint_as_float(t[1]));
}
DI float xrow16_sum(float x) {
  auto s = __builtin_amdgcn_permlane16_swap(__float_as_uint(x), __float_as_uint(x), false, false);
  x = __uint_as_float(s[0]) + __uint_as_float(s[1]);
  auto t = __builtin_amdgcn_permlane32_swap(__float_as_uint(x), __float_as_uint(x), false, false);
  return __uint_as_float(t[0]) + __uint_as_float(t[1]);
}
DI unsigned cvtpk(float lo, float hi) { f32x2 v = {lo, hi}; bf16x2 b = __builtin_convertvector(v, bf16x2); return __builtin_bit_cast(unsigned, b); }
DI float ex2(float x) { return __builtin_amdgcn_exp2f(x); }
DI void wg_barrier() { asm volatile("s_waitcnt lgkmcnt(0)\n\ts_barrier" ::: "memory"); }
template <int N> DI void wait_vm() { asm volatile("s_waitcnt vmcnt(%0)" :: "i"(N) : "memory"); }

#define NTS " nt"
DI void dma4(unsigned m0v, __amdgpu_buffer_rsrc_t rsrc, unsigned soff, unsigned v0, unsigned v1, unsigned v2, unsigned v3) {
  asm volatile("s_mov_b32 m0, %0\n\t"
               "s_nop 4\n\t"
               "buffer_load_dwordx4 %1, %5, %6 offen" NTS " lds\n\t"
               "buffer_load_dwordx4 %2, %5, %6 offen offset:1024" NTS " lds\n\t"
               "buffer_load_dwordx4 %3, %5, %6 offen offset:2048" NTS " lds\n\t"
               "buffer_load_dwordx4 %4, %5, %6 offen offset:3072" NTS " lds"
               :: "s"(m0v), "v"(v0), "v"(v1), "v"(v2), "v"(v3), "s"(rsrc), "s"(soff) : "memory");
}

struct Args { const bf16x8* q; const void* k; const void* v; const int* bt; const int* lens; unsigned* out; unsigned char* ws; unsigned kv_bytes; unsigned pad; };

DI float mfac(float m, float M) { return (m == -INFINITY) ? 0.f : ex2((m - M) * C2); }

struct Soft { float m, l; f32x4 o[8]; };
DI void soft_reset(Soft& s) { s.m = -INFINITY; s.l = 0.f;
#pragma unroll
  for (int i = 0; i < 8; ++i) s.o[i] = f32x4{0.f, 0.f, 0.f, 0.f}; }

DI s16x4 soft_step(Soft& s, f32x4 x, bool mask, int tok0, int len) {
  if (mask) {
#pragma unroll
    for (int r = 0; r < 4; ++r) x[r] = (tok0 + r < len) ? x[r] : -INFINITY;
  }
  float pm = fmaxf(fmaxf(x[0], x[1]), fmaxf(x[2], x[3]));
  pm = xrow16_max(pm);
  const float mn = fmaxf(s.m, pm);
  const float ms = (mn == -INFINITY) ? 0.f : mn * C2;
  const float alpha = ex2(s.m * C2 - ms);
  float p[4];
#pragma unroll
  for (int r = 0; r < 4; ++r) p[r] = ex2(fmaf(x[r], C2, -ms));
  s.l = fmaf(s.l, alpha, (p[0] + p[1]) + (p[2] + p[3]));
  s.m = mn;
#pragma unroll
  for (int i = 0; i < 8; ++i) s.o[i] *= alpha;
  u32x2 pk = {cvtpk(p[0], p[1]), cvtpk(p[2], p[3])};
  return __builtin_bit_cast(s16x4, pk);
}

DI int lds_ld(LAS unsigned char* p) { return __hip_atomic_load((LAS int*)p, __ATOMIC_RELAXED, __HIP_MEMORY_SCOPE_WORKGROUP); }
DI void lds_st(LAS unsigned char* p, int v) { __hip_atomic_store((LAS int*)p, v, __ATOMIC_RELAXED, __HIP_MEMORY_SCOPE_WORKGROUP); }
DI int lds_wait_ge(LAS unsigned char* p, int need) {
  int v;
#pragma nounroll
  for (unsigned spins = 0;; ++spins) { v = rfl(lds_ld(p)); if (v >= need || spins > (1u << 24)) break; __builtin_amdgcn_s_sleep(1); }
  asm volatile("" ::: "memory");
  return v;
}
DI void lds_drain() { asm volatile("s_waitcnt lgkmcnt(0)" ::: "memory"); }

__global__ void __launch_bounds__(NTHR, 2) radix_attn(Args a) {
  extern __shared__ __attribute__((aligned(16))) unsigned char lds_raw[];
  LAS unsigned char* lds = (LAS unsigned char*)lds_raw;
  LAS unsigned char* ctl = lds + L_MISC;
  const int tid = threadIdx.x, lane = tid & 63, wave = rfl(tid >> 6);
  const int w = blockIdx.x & 15, h = blockIdx.x >> 4;
  const int c = lane & 15, g = lane >> 4;
  const unsigned long long pf_t0 = __builtin_amdgcn_s_memrealtime();
  asm volatile("" :: "s"(a.q), "s"(a.k), "s"(a.v), "s"(a.bt), "s"(a.lens), "s"(a.out), "s"(a.ws), "s"(a.kv_bytes));
  const int len_raw = a.lens[c];

  struct Part { int len, Pe, S, E, spare, exc, lo, hi, xoff, S_w, spare_w, T_w, ns; unsigned xmask; bool all_masked; };
  auto partition = [&](int P) {
    Part p;
    p.all_masked = len_raw <= 0;
    p.len = p.all_masked ? LK : min(len_raw, LK);
    const int np = (p.len + PAGE - 1) >> 4;
    p.Pe = min(P, rdl(row_scan_max(np), 15));
    const int n = max(np - p.Pe, 0);
    const int W = p.Pe + rdl(row_scan_add(n), 15);
    const int T = (W >> 4) + (c < (W & 15) ? 1 : 0);
    const int own = min(n, T);
    p.exc = n - own; p.spare = T - own;
    p.S = row_scan_add(p.spare) - p.spare;
    p.E = p.Pe + row_scan_add(p.exc) - p.exc;
    p.S_w = rdl(p.S, w); p.spare_w = rdl(p.spare, w); p.T_w = rdl(T, w);
    p.ns = max(0, min(p.S_w + p.spare_w, p.Pe) - p.S_w);
    p.lo = max(p.S_w, p.E); p.hi = min(p.S_w + p.spare_w, p.E + p.exc);
    p.xmask = (unsigned)(__ballot(p.hi > p.lo) & 0xFFFFull);
    p.xoff = p.Pe + own - p.E;
    return p;
  };

  if (__builtin_expect(wave == 0, 1)) {
    __builtin_amdgcn_s_setprio(3);
    const i32x4* bt4 = (const i32x4*)a.bt;
    i32x4 rk16[16];
#pragma unroll
    for (int k = 0; k < 16; ++k) rk16[k] = bt4[k * (NB / 4) + lane];
    STAMP(8);
    i32x4 acc = rk16[1] ^ rk16[0];
#pragma unroll
    for (int k = 2; k < 16; ++k) acc |= rk16[k] ^ rk16[0];
#pragma unroll
    for (int k = 0; k < 16; ++k) *(LAS i32x4*)(lds + L_MRG + (k * NB + 4 * lane) * 4) = rk16[k];
    int P = NB;
#pragma unroll
    for (int e = 0; e < 4; ++e) {
      const unsigned long long bal = __ballot(acc[e] != 0);
      P = min(P, bal ? 4 * (int)__builtin_ctzll(bal) + e : NB);
    }
    if (lane == 0) *(LAS int*)(ctl + C_P) = P;
    wg_barrier();
    STAMP(2);
    const Part p = partition(P);
    const int Pe = p.Pe, S_w = p.S_w, spare_w = p.spare_w, T_w = p.T_w, lo = p.lo, hi = p.hi, xoff = p.xoff; const unsigned xmask = p.xmask;
    STAMP(7);
    unsigned vp[4];
    {
      int idx[4];
#pragma unroll
      for (int r = 0; r < 4; ++r) { const int i = 64 * r + lane; idx[r] = (i < spare_w) ? S_w + i : w * NB + Pe + (i - spare_w); }
      for (unsigned m = xmask; m; m &= m - 1) {
        const int b = __builtin_ctz(m);
        const int lo_b = rdl(lo, b) - S_w, hi_b = rdl(hi, b) - S_w, off_b = rdl(xoff, b) + b * NB + S_w;
#pragma unroll
        for (int r = 0; r < 4; ++r) { const int i = 64 * r + lane; if (i >= lo_b && i < hi_b) idx[r] = i + off_b; }
      }
#pragma unroll
      for (int r = 0; r < 4; ++r) { const int i = 64 * r + lane; vp[r] = (unsigned)*(LAS int*)(lds + L_MRG + 4 * (i < T_w ? idx[r] : 0)) * PAGE_BYTES + (unsigned)h * (D * 2); }
    }
    unsigned vk[4], vv[4];
#pragma unroll
    for (int i = 0; i < 4; ++i) { const int t = 4 * i + g;
      vk[i] = t * TOK_BYTES + ((c ^ t) << 4) - 1024 * i;
      vv[i] = t * TOK_BYTES + ((c ^ ((t & 7) << 1)) << 4) - 1024 * i; }
    const unsigned ring = (unsigned)(uintptr_t)(lds + L_RING);
    const __amdgpu_buffer_rsrc_t rk = __builtin_amdgcn_make_buffer_rsrc((void*)a.k, 0, a.kv_bytes, 0x00020000);
    const __amdgpu_buffer_rsrc_t rv = __builtin_amdgcn_make_buffer_rsrc((void*)a.v, 0, a.kv_bytes, 0x00020000);
    asm volatile("" : "+v"(vp[0]), "+v"(vp[1]), "+v"(vp[2]), "+v"(vp[3]));
    STAMP(3);
    if (lane == 0) *(LAS int*)(ctl + 40) = (int)(__builtin_amdgcn_s_memrealtime() - pf_t0);
    int pub = 0, relnext = 0;
    auto publish = [&](int nl) { if (nl > pub) { pub = nl; lds_st(ctl + C_LANDED, nl); } };
    for (int i = 0; i < T_w; ++i) {
      if (i >= NSLOT) {
        const int need = (i >> 2) - NSLOT / 4 + 1;
        if (rfl(relnext) < need) {
          LAS unsigned char* rel = ctl + C_REL + 4 * (i & 3);
          if (rfl(lds_ld(rel)) < need) {
            wait_vm<0>(); publish(2 * i);
            lds_wait_ge(rel, need);
          }
        }
        asm volatile("" ::: "memory");
      }
      const unsigned so = (unsigned)rdl((int)vp[0], i & 63);
      const unsigned m0v = ring + (i & (NSLOT - 1)) * 8192;
      dma4(m0v, rk, so, vk[0], vk[1], vk[2], vk[3]);
      publish(2 * i - INFL / 4);
      relnext = lds_ld(ctl + C_REL + 4 * ((i + 1) & 3));
      if ((i & 63) == 63) { vp[0] = vp[1]; vp[1] = vp[2]; vp[2] = vp[3]; }
      wait_vm<INFL>();
      dma4(m0v + 4096, rv, so, vv[0], vv[1], vv[2], vv[3]);
      publish(2 * i + 1 - INFL / 4);
      wait_vm<INFL>();
    }
    STAMP(5);
    publish(2 * T_w - INFL / 4);
    if (INFL > 16) { wait_vm<16>(); publish(2 * T_w - 4); }
    if (INFL > 12) { wait_vm<12>(); publish(2 * T_w - 3); }
    if (INFL > 8) { wait_vm<8>(); publish(2 * T_w - 2); }
    wait_vm<4>(); publish(2 * T_w - 1);
    wait_vm<0>(); publish(2 * T_w);
    if (lane == 0) *(LAS int*)(ctl + 44) = (int)(__builtin_amdgcn_s_memrealtime() - pf_t0);
    STAMP(6);
  } else if (wave <= NSW) {
    const int cw = wave - 1;
    bf16x8 qf[2][4];
    __builtin_amdgcn_s_sleep(6);
#pragma unroll
    for (int cg = 0; cg < 2; ++cg)
#pragma unroll
      for (int s = 0; s < 4; ++s) { const int r = 16 * cg + c; qf[cg][s] = a.q[(((r >> 1) * HQ + 2 * h + (r & 1)) * D + 32 * s + 8 * g) / 8]; }
    wg_barrier();
    STAMP(2);
    const Part p = partition(rfl(*(LAS int*)(ctl + C_P)));
    const int len = p.len, Pe = p.Pe, S_w = p.S_w, spare_w = p.spare_w, T_w = p.T_w, lo = p.lo, hi = p.hi, xoff = p.xoff, ns = p.ns; const unsigned xmask = p.xmask; const bool all_masked = p.all_masked;
    const int minlen = LK - rdl(row_scan_max(LK - len), 15);
    {
      const bool z0 = __shfl((int)all_masked, c >> 1) != 0, z1 = __shfl((int)all_masked, 8 + (c >> 1)) != 0;
#pragma unroll
      for (int s = 0; s < 4; ++s) {
        u32x4 t0 = __builtin_bit_cast(u32x4, qf[0][s]), t1 = __builtin_bit_cast(u32x4, qf[1][s]);
#pragma unroll
        for (int e = 0; e < 4; ++e) { t0[e] = z0 ? 0u : t0[e]; t1[e] = z1 ? 0u : t1[e]; }
        qf[0][s] = __builtin_bit_cast(bf16x8, t0); qf[1][s] = __builtin_bit_cast(bf16x8, t1);
      }
    }
    const int len0 = __shfl(len, c >> 1), len1 = __shfl(len, 8 + (c >> 1));
    const unsigned ka0 = c * 256 + ((g ^ c) << 4);
    const int tv = 4 * g + (c >> 2);
    const unsigned va0 = 4096 + tv * 256 + ((tv & 7) << 5) + (c & 3) * 8;
    int j = 0, seen = 0;
    Soft s0, s1;
    auto page = [&](auto SHc, const bf16x8 (&qa)[4], const bf16x8 (&qb)[4], int la, int lb, int t0, bool mask) {
      constexpr bool SH = decltype(SHc)::value;
      const int i = 4 * j + cw;
      LAS unsigned char* sl = lds + L_RING + (i & (NSLOT - 1)) * 8192;
      if (seen < 2 * i + 1) seen = lds_wait_ge(ctl + C_LANDED, 2 * i + 1);
      bf16x8 kf[4];
#pragma unroll
      for (int s = 0; s < 4; ++s) kf[s] = *(LAS bf16x8*)(sl + (ka0 ^ (s << 6)));
      f32x4 xa = {0.f, 0.f, 0.f, 0.f}, xb = {0.f, 0.f, 0.f, 0.f};
#pragma unroll
      for (int s = 0; s < 4; ++s) { xa = __builtin_amdgcn_mfma_f32_16x16x32_bf16(kf[s], qa[s], xa, 0, 0, 0);
        if (SH) xb = __builtin_amdgcn_mfma_f32_16x16x32_bf16(kf[s], qb[s], xb, 0, 0, 0); }
      const s16x4 pa = soft_step(s0, xa, mask, t0 + 4 * g, la);
      s16x4 pb; if (SH) pb = soft_step(s1, xb, mask, t0 + 4 * g, lb);
      if (seen < 2 * i + 2) seen = lds_wait_ge(ctl + C_LANDED, 2 * i + 2);
      s16x4 vf[8];
#pragma unroll
      for (int blk = 0; blk < 8; ++blk) vf[blk] = __builtin_amdgcn_ds_read_tr16_b64_v4i16((LAS s16x4*)(sl + (va0 ^ (blk << 5))));
      ++j;
      asm volatile("s_waitcnt lgkmcnt(0)" : "+v"(vf[0]), "+v"(vf[1]), "+v"(vf[2]), "+v"(vf[3]), "+v"(vf[4]), "+v"(vf[5]), "+v"(vf[6]), "+v"(vf[7]) :: "memory");
      lds_st(ctl + C_REL + 4 * cw, j);
#pragma unroll
      for (int blk = 0; blk < 8; ++blk) { s0.o[blk] = __builtin_amdgcn_mfma_f32_16x16x16bf16_1k(vf[blk], pa, s0.o[blk], 0, 0, 0);
        if (SH) s1.o[blk] = __builtin_amdgcn_mfma_f32_16x16x16bf16_1k(vf[blk], pb, s1.o[blk], 0, 0, 0); }
    };
    int npass = WARM ? 2 : 1;
    asm volatile("" : "+s"(npass));
#pragma nounroll
    for (int pass = 2 - npass; pass < 2; ++pass) {
      const bool real = pass != 0;
      if (real && ns > 0) {
        soft_reset(s0); soft_reset(s1);
        for (int i = cw; i < ns; i += 4) { const int t0 = (S_w + i) * PAGE; page(std::true_type{}, qf[0], qf[1], len0, len1, t0, t0 + PAGE > minlen); }
        s0.l = xrow16_sum(s0.l); s1.l = xrow16_sum(s1.l);
        if (cw > 0) lds_wait_ge(ctl + C_CSTEP, cw);
#pragma unroll
        for (int cg = 0; cg < 2; ++cg) { Soft& s = cg ? s1 : s0;
          LAS unsigned char* r = lds + L_MRG + (16 * cg + c) * ROW;
          if (cw > 0) {
            const f32x2 ml = *(LAS f32x2*)(r + 512);
            const float M = fmaxf(ml[0], s.m), f0 = mfac(ml[0], M), f1 = mfac(s.m, M);
#pragma unroll
            for (int blk = 0; blk < 8; ++blk) s.o[blk] = *(LAS f32x4*)(r + blk * 64 + g * 16) * f0 + s.o[blk] * f1;
            s.l = ml[1] * f0 + s.l * f1; s.m = M;
          }
#pragma unroll
          for (int blk = 0; blk < 8; ++blk) *(LAS f32x4*)(r + blk * 64 + g * 16) = s.o[blk];
          lds_drain();
          if (g == 0) *(LAS f32x2*)(r + 512) = f32x2{s.m, s.l};
        }
        lds_drain();
        lds_st(ctl + C_CSTEP, cw + 1);
      }
      unsigned m = real ? xmask : 0u;
      int kx = 0;
      for (;;) {
        const bool last = m == 0;
        const int b = last ? w : __builtin_ctz(m);
        const int i0 = last ? spare_w : rdl(lo, b) - S_w;
        const int i1 = last ? (real ? T_w : 0) : rdl(hi, b) - S_w;
        const int lp0 = last ? Pe : rdl(lo, b) + rdl(xoff, b);
        bf16x8 qa[4];
        if (b < 8) {
#pragma unroll
          for (int s = 0; s < 4; ++s) qa[s] = qf[0][s];
        } else {
#pragma unroll
          for (int s = 0; s < 4; ++s) qa[s] = qf[1][s];
        }
        const int lb = rdl(len, b);
        soft_reset(s0);
        for (int i = 4 * j + cw; i < i1; i += 4) { const int t0 = (lp0 + i - i0) * PAGE; page(std::false_type{}, qa, qa, lb, lb, t0, t0 + PAGE > lb); }
        if (!last && kx > 0) lds_wait_ge(ctl + C_RFREE, kx);
        const float lt = xrow16_sum(s0.l);
        if ((c >> 1) == (b & 7)) {
          LAS unsigned char* r = lds + (last ? L_RECF : L_RECX) + (cw * 2 + (c & 1)) * ROW;
#pragma unroll
          for (int blk = 0; blk < 8; ++blk) *(LAS f32x4*)(r + blk * 64 + g * 16) = s0.o[blk];
          if (g == 0) *(LAS f32x2*)(r + 512) = f32x2{s0.m, lt};
        }
        if (last) break;
        lds_drain();
        if (lane == 0) __hip_atomic_fetch_add((LAS int*)(ctl + C_RCNT), 1, __ATOMIC_RELAXED, __HIP_MEMORY_SCOPE_WORKGROUP);
        m &= m - 1; ++kx;
      }
      if (real) wg_barrier();
      if (cw < 2) {
        const int r = cw, d0 = 2 * lane;
        float M = -INFINITY; f32x2 ml[5], o[5];
#pragma unroll
        for (int k = 0; k < 5; ++k) { ml[k] = *(LAS f32x2*)(lds + L_RECF + (k * 2 + r) * ROW + 512); o[k] = *(LAS f32x2*)(lds + L_RECF + (k * 2 + r) * ROW + d0 * 4); }
#pragma unroll
        for (int k = 0; k < 5; ++k) M = fmaxf(M, ml[k][0]);
        float L = 0.f, o0 = 0.f, o1 = 0.f;
#pragma unroll
        for (int k = 0; k < 5; ++k) { const float f = mfac(ml[k][0], M); L = fmaf(f, ml[k][1], L); o0 = fmaf(f, o[k][0], o0); o1 = fmaf(f, o[k][1], o1); }
        const float inv = __builtin_amdgcn_rcpf(L);
        unsigned res = cvtpk(o0 * inv, o1 * inv);
        asm volatile("" : "+v"(res));
        if (real) a.out[((w * HQ + 2 * h + r) * D + d0) >> 1] = res;
      }
    }
  } else {
    const __amdgpu_buffer_rsrc_t prsrc = __builtin_amdgcn_make_buffer_rsrc(a.ws + W_PART, 0, PART_BYTES, 0x00020000);
    if (lane < 8) *(LAS int*)(ctl + 4 * lane) = 0;
    const unsigned long long epoch = __hip_atomic_load((gu64*)(a.ws + W_EPOCH), RLX_AGENT);
    wg_barrier();
    STAMP(2);
    const Part p = partition(rfl(*(LAS int*)(ctl + C_P)));
    const unsigned long long tag = epoch + 1ull;
    const int Pe = p.Pe, S = p.S, E = p.E, spare = p.spare, exc = p.exc, ns = p.ns; const unsigned xmask = p.xmask;
    const unsigned blk = w * 16 + h;
    const int r = lane >> 5, ch = lane & 31;
    if (ns > 0) {
      lds_wait_ge(ctl + C_CSTEP, NSW);
#pragma unroll 4
      for (int k = 0; k < 16; ++k) { const int off = (2 * k + r) * ROW + ch * 16;
        const u32x4 v = *(LAS u32x4*)(lds + L_MRG + off);
        __builtin_amdgcn_raw_buffer_store_b128(v, prsrc, off, blk * PS_BYTES, 16); }
      if (lane < 32) { const u32x2 v = *(LAS u32x2*)(lds + L_MRG + lane * ROW + 512);
        __builtin_amdgcn_raw_buffer_store_b64(v, prsrc, lane * ROW + 512, blk * PS_BYTES, 16); }
    }
    int kx = 0;
    for (unsigned m = xmask; m; m &= m - 1, ++kx) {
      const int b = __builtin_ctz(m);
      lds_wait_ge(ctl + C_RCNT, NSW * (kx + 1));
      float M = -INFINITY; f32x2 ml[4]; f32x4 ok[4];
#pragma unroll
      for (int k = 0; k < 4; ++k) { ml[k] = *(LAS f32x2*)(lds + L_RECX + (k * 2 + r) * ROW + 512); ok[k] = *(LAS f32x4*)(lds + L_RECX + (k * 2 + r) * ROW + ch * 16); M = fmaxf(M, ml[k][0]); }
      float L = 0.f; f32x4 o = {0.f, 0.f, 0.f, 0.f};
#pragma unroll
      for (int k = 0; k < 4; ++k) { const float f = mfac(ml[k][0], M); L = fmaf(f, ml[k][1], L); o += ok[k] * f; }
      asm volatile("s_waitcnt lgkmcnt(0)" : "+v"(o) :: "memory");
      lds_st(ctl + C_RFREE, kx + 1);
      const unsigned so = W_PX + (blk * 16 + b) * PX_BYTES;
      __builtin_amdgcn_raw_buffer_store_b128(__builtin_bit_cast(u32x4, o), prsrc, r * ROW + ch * 16, so, 16);
      if (ch == 0) __builtin_amdgcn_raw_buffer_store_b64(__builtin_bit_cast(u32x2, f32x2{M, L}), prsrc, r * ROW + 512, so, 16);
    }
    asm volatile("s_waitcnt vmcnt(0)" ::: "memory");
    if (lane == 0) __hip_atomic_store((gu64*)(a.ws + W_FLAG + blk * FLAG_STRIDE), tag, RLX_AGENT);
    STAMP(3);
    const int E_w = rdl(E, w), exc_w = rdl(exc, w);
    const bool sh_ov = spare > 0 && S < Pe;
    const bool ex_ov = min(S + spare, E_w + exc_w) > max(S, E_w);
    unsigned cmask = (unsigned)(__ballot(sh_ov) & 0xFFFFull) | ((unsigned)(__ballot(ex_ov) & 0xFFFFull) << 16);
    {
      const bool need = lane < 16 && (sh_ov || ex_ov);
      gu64* f = (gu64*)(a.ws + W_FLAG + (size_t)(c * 16 + h) * FLAG_STRIDE);
#pragma nounroll
      for (unsigned spins = 0; spins < (1u << 20); ++spins) {
        const bool ok = !need || __hip_atomic_load(f, RLX_AGENT) == tag;
        if (__all(ok)) break;
        __builtin_amdgcn_s_sleep(32);
      }
      asm volatile("" ::: "memory");
    }
    STAMP(4);
    {
      const unsigned vso = lane < 16 ? (unsigned)(c * 16 + h) * PS_BYTES + (unsigned)(2 * w) * ROW : W_PX + ((unsigned)(c * 16 + h) * 16 + w) * PX_BYTES;
      float M = -INFINITY, L = 0.f; f32x4 o = {0.f, 0.f, 0.f, 0.f};
      constexpr int GB = 10;
      while (cmask) {
        u32x4 po[GB]; u32x2 pml[GB]; bool pv[GB];
#pragma unroll
        for (int k = 0; k < GB; ++k) {
          pv[k] = cmask != 0;
          const int id = pv[k] ? __builtin_ctz(cmask) : 0; cmask &= cmask - 1;
          const unsigned so = pv[k] ? (unsigned)rdl((int)vso, id) : PART_BYTES;
          po[k] = __builtin_amdgcn_raw_buffer_load_b128(prsrc, r * ROW + ch * 16 + so, 0, 16);
          pml[k] = __builtin_amdgcn_raw_buffer_load_b64(prsrc, r * ROW + 512 + so, 0, 16);
        }
#pragma unroll
        for (int k = 0; k < GB; ++k) {
          const float mk = pv[k] ? __uint_as_float(pml[k][0]) : -INFINITY, lk = __uint_as_float(pml[k][1]);
          const float Mn = fmaxf(M, mk), f0 = mfac(M, Mn), f1 = mfac(mk, Mn);
          o = o * f0 + __builtin_bit_cast(f32x4, po[k]) * f1; L = L * f0 + lk * f1; M = Mn;
        }
      }
      LAS unsigned char* rec = lds + L_RECF + (4 * 2 + r) * ROW;
      *(LAS f32x4*)(rec + ch * 16) = o;
      if (ch == 0) *(LAS f32x2*)(rec + 512) = f32x2{M, L};
    }
    STAMP(5);
    wg_barrier();
    STAMP(6);
    if (lane == 0 && (blk == 0 || blk == 100 || blk == 255)) printf("blk %d start %llu firstdma %d streamend %d end %d\n", blk, pf_t0 % 100000000ull, *(LAS int*)(ctl + 40), *(LAS int*)(ctl + 44), (int)(__builtin_amdgcn_s_memrealtime() - pf_t0));
    if (blk == 0) {
#pragma nounroll
      for (unsigned spins = 0; spins < (1u << 20); ++spins) {
        bool ok = true;
#pragma unroll
        for (int k = 0; k < 4; ++k) ok &= __hip_atomic_load((gu64*)(a.ws + W_FLAG + (size_t)(k * 64 + lane) * FLAG_STRIDE), RLX_AGENT) == tag;
        if (__all(ok)) break;
        __builtin_amdgcn_s_sleep(8);
      }
      if (lane == 0) __hip_atomic_store((gu64*)(a.ws + W_EPOCH), tag, RLX_AGENT);
    }
  }
}

extern "C" void kernel_launch(void* const* d_in, const int* in_sizes, int n_in, void* d_out, int out_size, void* d_ws, size_t ws_size, hipStream_t stream) {
  Args a;
  a.q = (const bf16x8*)d_in[0]; a.k = d_in[1]; a.v = d_in[2]; a.bt = (const int*)d_in[3]; a.lens = (const int*)d_in[4];
  a.out = (unsigned*)d_out; a.ws = (unsigned char*)d_ws; a.kv_bytes = (unsigned)in_sizes[1] * 2u; a.pad = 0;
  hipFuncSetAttribute((const void*)radix_attn, hipFuncAttributeMaxDynamicSharedMemorySize, LDS_BYTES);
  hipLaunchKernelGGL(radix_attn, dim3(256), dim3(NTHR), LDS_BYTES, stream, a);
}
```

```cpp
#define TLBPF 0
#include <hip/hip_runtime.h>
#include <hip/hip_bf16.h>
#include <stdint.h>

#define NSLOT 16
#define INFL 20
#define KV_NT 1
#define OUTSC1 0
#define OUT_STORE(p, v) (*(p) = (v))
#define WARM 1
#define DIAG 0
#define MAPSWAP 1

constexpr int B = 16, HQ = 32, HKV = 16, D = 128, PAGE = 16, NB = 256, LK = NB * PAGE;
constexpr int NSW = 4, NTHR = (NSW + 2) * 64;
constexpr float C2 = 0.088388347648318440f * 1.4426950408889634f;
constexpr unsigned PAGE_BYTES = PAGE * HKV * D * 2;
constexpr unsigned TOK_BYTES = HKV * D * 2;

constexpr int ROW = 528;
constexpr int L_RING = 0;
constexpr int L_MRG = NSLOT * 8192;
constexpr int L_RECX = L_MRG + 32 * ROW;
constexpr int L_RECF = L_RECX + 4 * 2 * ROW;
constexpr int L_MISC = L_RECF + 5 * 2 * ROW;
constexpr int C_LANDED = 0, C_REL = 4, C_CSTEP = 20, C_RCNT = 24, C_RFREE = 28, C_P = 32, C_PTR = 48, C_LENS = 80;
constexpr int LDS_BYTES = L_MISC + 160;
static_assert(NSLOT % 4 == 0 && INFL % 4 == 0 && (NSLOT & (NSLOT - 1)) == 0, "ring geometry");
static_assert(LDS_BYTES <= 160 * 1024, "LDS");

constexpr size_t W_EPOCH = 0, W_FLAG = 256, FLAG_STRIDE = 128;
constexpr size_t W_PART = 65536;
constexpr unsigned PS_BYTES = 32 * ROW, PX_BYTES = 2 * ROW;
constexpr unsigned W_PX = 256 * PS_BYTES;
constexpr unsigned PART_BYTES = W_PX + 256 * 16 * PX_BYTES;
#define STAMP(k) do {} while (0)

typedef __bf16 bf16x8 __attribute__((ext_vector_type(8)));
typedef __bf16 bf16x2 __attribute__((ext_vector_type(2)));
typedef short s16x4 __attribute__((ext_vector_type(4)));
typedef float f32x4 __attribute__((ext_vector_type(4)));
typedef float f32x2 __attribute__((ext_vector_type(2)));
typedef unsigned u32x4 __attribute__((ext_vector_type(4)));
typedef unsigned u32x2 __attribute__((ext_vector_type(2)));
typedef int i32x4 __attribute__((ext_vector_type(4)));
#define LAS __attribute__((address_space(3)))
#define GAS __attribute__((address_space(1)))
#define DI __device__ __forceinline__
typedef GAS unsigned long long gu64;
#define RLX_AGENT __ATOMIC_RELAXED, __HIP_MEMORY_SCOPE_AGENT

DI int rfl(int x) { return __builtin_amdgcn_readfirstlane(x); }
DI int rdl(int x, int l) { return __builtin_amdgcn_readlane(x, l); }
template <int CTRL> DI int dpp_i(int oldv, int x) { return __builtin_amdgcn_update_dpp(oldv, x, CTRL, 0xf, 0xf, false); }
DI int row_scan_add(int x) {
  x += dpp_i<0x111>(0, x); x += dpp_i<0x112>(0, x); x += dpp_i<0x114>(0, x); x += dpp_i<0x118>(0, x); return x;
}
DI int row_scan_max(int x) {
  x = max(x, dpp_i<0x111>(0, x)); x = max(x, dpp_i<0x112>(0, x)); x = max(x, dpp_i<0x114>(0, x)); x = max(x, dpp_i<0x118>(0, x)); return x;
}
DI float xrow16_max(float x) {
  auto s = __builtin_amdgcn_permlane16_swap(__float_as_uint(x), __float_as_uint(x), false, false);
  x = fmaxf(__uint_as_float(s[0]), __uint_as_float(s[1]));
  auto t = __builtin_amdgcn_permlane32_swap(__float_as_uint(x), __float_as_uint(x), false, false);
  return fmaxf(__uint_as_float(t[0]), __uint_as_float(t[1]));
}
DI float xrow16_sum(float x) {
  auto s = __builtin_amdgcn_permlane16_swap(__float_as_uint(x), __float_as_uint(x), false, false);
  x = __uint_as_float(s[0]) + __uint_as_float(s[1]);
  auto t = __builtin_amdgcn_permlane32_swap(__float_as_uint(x), __float_as_uint(x), false, false);
  return __uint_as_float(t[0]) + __uint_as_float(t[1]);
}
DI unsigned cvtpk(float lo, float hi) { f32x2 v = {lo, hi}; bf16x2 b = __builtin_convertvector(v, bf16x2); return __builtin_bit_cast(unsigned, b); }
DI float ex2(float x) { return __builtin_amdgcn_exp2f(x); }
DI void wg_barrier() { asm volatile("s_waitcnt lgkmcnt(0)\n\ts_barrier" ::: "memory"); }
template <int N> DI void wait_vm() { asm volatile("s_waitcnt vmcnt(%0)" :: "i"(N) : "memory"); }

#define NTS " nt"
DI void dma4(unsigned m0v, __amdgpu_buffer_rsrc_t rsrc, unsigned soff, unsigned v0, unsigned v1, unsigned v2, unsigned v3) {
  asm volatile("s_mov_b32 m0, %0\n\t"
               "s_nop 4\n\t"
               "buffer_load_dwordx4 %1, %5, %6 offen" NTS " lds\n\t"
               "buffer_load_dwordx4 %2, %5, %6 offen offset:1024" NTS " lds\n\t"
               "buffer_load_dwordx4 %3, %5, %6 offen offset:2048" NTS " lds\n\t"
               "buffer_load_dwordx4 %4, %5, %6 offen offset:3072" NTS " lds"
               :: "s"(m0v), "v"(v0), "v"(v1), "v"(v2), "v"(v3), "s"(rsrc), "s"(soff) : "memory");
}

struct Args { const bf16x8* q; const void* k; const void* v; const int* bt; const int* lens; unsigned* out; unsigned char* ws; unsigned kv_bytes; unsigned pad; };

DI float mfac(float m, float M) { return (m == -INFINITY) ? 0.f : ex2((m - M) * C2); }

struct Soft { float m, l; f32x4 o[8]; };
DI void soft_reset(Soft& s) { s.m = -INFINITY; s.l = 0.f;
#pragma unroll
  for (int i = 0; i < 8; ++i) s.o[i] = f32x4{0.f, 0.f, 0.f, 0.f}; }

DI s16x4 soft_step(Soft& s, f32x4 x, bool mask, int tok0, int len) {
  if (mask) {
#pragma unroll
    for (int r = 0; r < 4; ++r) x[r] = (tok0 + r < len) ? x[r] : -INFINITY;
  }
  float pm = fmaxf(fmaxf(x[0], x[1]), fmaxf(x[2], x[3]));
  pm = xrow16_max(pm);
  const float mn = fmaxf(s.m, pm);
  const float ms = (mn == -INFINITY) ? 0.f : mn * C2;
  const float alpha = ex2(s.m * C2 - ms);
  float p[4];
#pragma unroll
  for (int r = 0; r < 4; ++r) p[r] = ex2(fmaf(x[r], C2, -ms));
  s.l = fmaf(s.l, alpha, (p[0] + p[1]) + (p[2] + p[3]));
  s.m = mn;
#pragma unroll
  for (int i = 0; i < 8; ++i) s.o[i] *= alpha;
  u32x2 pk = {cvtpk(p[0], p[1]), cvtpk(p[2], p[3])};
  return __builtin_bit_cast(s16x4, pk);
}

DI int lds_ld(LAS unsigned char* p) { return __hip_atomic_load((LAS int*)p, __ATOMIC_RELAXED, __HIP_MEMORY_SCOPE_WORKGROUP); }
DI void lds_st(LAS unsigned char* p, int v) { __hip_atomic_store((LAS int*)p, v, __ATOMIC_RELAXED, __HIP_MEMORY_SCOPE_WORKGROUP); }
DI int lds_wait_ge(LAS unsigned char* p, int need) {
  int v;
#pragma nounroll
  for (unsigned spins = 0;; ++spins) { v = rfl(lds_ld(p)); if (v >= need || spins > (1u << 24)) break; __builtin_amdgcn_s_sleep(1); }
  asm volatile("" ::: "memory");
  return v;
}
DI void lds_drain() { asm volatile("s_waitcnt lgkmcnt(0)" ::: "memory"); }

__global__ void __launch_bounds__(NTHR, 2) radix_attn(Args a) {
  extern __shared__ __attribute__((aligned(16))) unsigned char lds_raw[];
  LAS unsigned char* lds = (LAS unsigned char*)lds_raw;
  LAS unsigned char* ctl = lds + L_MISC;
  const int tid = threadIdx.x, lane = tid & 63, wave = rfl(tid >> 6);
  const int w = blockIdx.x & 15, h = blockIdx.x >> 4;
  const int c = lane & 15, g = lane >> 4;
  int len_raw;

  struct Part { int len, Pe, S, E, spare, exc, lo, hi, xoff, S_w, spare_w, T_w, ns; unsigned xmask; bool all_masked; };
  auto partition = [&](int P) {
    Part p;
    p.all_masked = len_raw <= 0;
    p.len = p.all_masked ? LK : min(len_raw, LK);
    const int np = (p.len + PAGE - 1) >> 4;
    p.Pe = min(P, rdl(row_scan_max(np), 15));
    const int n = max(np - p.Pe, 0);
    const int W = p.Pe + rdl(row_scan_add(n), 15);
    const int T = (W >> 4) + (c < (W & 15) ? 1 : 0);
    const int own = min(n, T);
    p.exc = n - own; p.spare = T - own;
    p.S = row_scan_add(p.spare) - p.spare;
    p.E = p.Pe + row_scan_add(p.exc) - p.exc;
    p.S_w = rdl(p.S, w); p.spare_w = rdl(p.spare, w); p.T_w = rdl(T, w);
    p.ns = max(0, min(p.S_w + p.spare_w, p.Pe) - p.S_w);
    p.lo = max(p.S_w, p.E); p.hi = min(p.S_w + p.spare_w, p.E + p.exc);
    p.xmask = (unsigned)(__ballot(p.hi > p.lo) & 0xFFFFull);
    p.xoff = p.Pe + own - p.E;
    return p;
  };

  if (__builtin_expect(wave == 0, 1)) {
    __builtin_amdgcn_s_setprio(3);
    asm volatile("" :: "s"(a.q), "s"(a.k), "s"(a.v), "s"(a.bt), "s"(a.lens), "s"(a.out), "s"(a.ws), "s"(a.kv_bytes));
    len_raw = a.lens[c];
    if (lane == 0) { *(LAS u32x4*)(ctl + C_PTR) = u32x4{(unsigned)(uintptr_t)a.q, (unsigned)((uintptr_t)a.q >> 32), (unsigned)(uintptr_t)a.out, (unsigned)((uintptr_t)a.out >> 32)};
                     *(LAS u32x2*)(ctl + C_PTR + 16) = u32x2{(unsigned)(uintptr_t)a.ws, (unsigned)((uintptr_t)a.ws >> 32)}; }
    const i32x4* bt4 = (const i32x4*)a.bt;
    i32x4 rk16[16];
#pragma unroll
    for (int k = 0; k < 16; ++k) rk16[k] = bt4[k * (NB / 4) + lane];
    STAMP(8);
    i32x4 acc = rk16[1] ^ rk16[0];
#pragma unroll
    for (int k = 2; k < 16; ++k) acc |= rk16[k] ^ rk16[0];
#pragma unroll
    for (int k = 0; k < 16; ++k) *(LAS i32x4*)(lds + L_MRG + (k * NB + 4 * lane) * 4) = rk16[k];
    int P = NB;
#pragma unroll
    for (int e = 0; e < 4; ++e) {
      const unsigned long long bal = __ballot(acc[e] != 0);
      P = min(P, bal ? 4 * (int)__builtin_ctzll(bal) + e : NB);
    }
    if (lane == 0) *(LAS int*)(ctl + C_P) = P;
    if (lane < 16) *(LAS int*)(ctl + C_LENS + 4 * lane) = len_raw;
    wg_barrier();
    STAMP(2);
    const Part p = partition(P);
    const int Pe = p.Pe, S_w = p.S_w, spare_w = p.spare_w, T_w = p.T_w, lo = p.lo, hi = p.hi, xoff = p.xoff; const unsigned xmask = p.xmask;
    STAMP(7);
    unsigned vp[4];
    {
      int idx[4];
#pragma unroll
      for (int r = 0; r < 4; ++r) { const int i = 64 * r + lane; idx[r] = (i < spare_w) ? S_w + i : w * NB + Pe + (i - spare_w); }
      for (unsigned m = xmask; m; m &= m - 1) {
        const int b = __builtin_ctz(m);
        const int lo_b = rdl(lo, b) - S_w, hi_b = rdl(hi, b) - S_w, off_b = rdl(xoff, b) + b * NB + S_w;
#pragma unroll
        for (int r = 0; r < 4; ++r) { const int i = 64 * r + lane; if (i >= lo_b && i < hi_b) idx[r] = i + off_b; }
      }
#pragma unroll
      for (int r = 0; r < 4; ++r) { const int i = 64 * r + lane; vp[r] = (unsigned)*(LAS int*)(lds + L_MRG + 4 * (i < T_w ? idx[r] : 0)) * PAGE_BYTES + (unsigned)h * (D * 2); }
    }
    unsigned vk[4], vv[4];
#pragma unroll
    for (int i = 0; i < 4; ++i) { const int t = 4 * i + g;
      vk[i] = t * TOK_BYTES + ((c ^ t) << 4) - 1024 * i;
      vv[i] = t * TOK_BYTES + ((c ^ ((t & 7) << 1)) << 4) - 1024 * i; }
    const unsigned ring = (unsigned)(uintptr_t)(lds + L_RING);
    const __amdgpu_buffer_rsrc_t rk = __builtin_amdgcn_make_buffer_rsrc((void*)a.k, 0, a.kv_bytes, 0x00020000);
    const __amdgpu_buffer_rsrc_t rv = __builtin_amdgcn_make_buffer_rsrc((void*)a.v, 0, a.kv_bytes, 0x00020000);
    asm volatile("" : "+v"(vp[0]), "+v"(vp[1]), "+v"(vp[2]), "+v"(vp[3]));
    STAMP(3);
    int pub = 0, relnext = 0;
    auto publish = [&](int nl) { if (nl > pub) { pub = nl; lds_st(ctl + C_LANDED, nl); } };
    for (int i = 0; i < T_w; ++i) {
      if (i >= NSLOT) {
        const int need = (i >> 2) - NSLOT / 4 + 1;
        if (rfl(relnext) < need) {
          LAS unsigned char* rel = ctl + C_REL + 4 * (i & 3);
          if (rfl(lds_ld(rel)) < need) {
            wait_vm<0>(); publish(2 * i);
            lds_wait_ge(rel, need);
          }
        }
        asm volatile("" ::: "memory");
      }
      const unsigned so = (unsigned)rdl((int)vp[0], i & 63);
      const unsigned m0v = ring + (i & (NSLOT - 1)) * 8192;
      dma4(m0v, rk, so, vk[0], vk[1], vk[2], vk[3]);
      publish(2 * i - INFL / 4);
      relnext = lds_ld(ctl + C_REL + 4 * ((i + 1) & 3));
      if ((i & 63) == 63) { vp[0] = vp[1]; vp[1] = vp[2]; vp[2] = vp[3]; }
      wait_vm<INFL>();
      dma4(m0v + 4096, rv, so, vv[0], vv[1], vv[2], vv[3]);
      publish(2 * i + 1 - INFL / 4);
      wait_vm<INFL>();
    }
    STAMP(5);
    publish(2 * T_w - INFL / 4);
    if (INFL > 16) { wait_vm<16>(); publish(2 * T_w - 4); }
    if (INFL > 12) { wait_vm<12>(); publish(2 * T_w - 3); }
    if (INFL > 8) { wait_vm<8>(); publish(2 * T_w - 2); }
    wait_vm<4>(); publish(2 * T_w - 1);
    wait_vm<0>(); publish(2 * T_w);
    STAMP(6);
  } else if (wave <= NSW) {
    const int cw = wave - 1;
    bf16x8 qf[2][4];
    wg_barrier();
    STAMP(2);
    const u32x4 pq = *(LAS u32x4*)(ctl + C_PTR);
    const GAS bf16x8* qp = (const GAS bf16x8*)(((uintptr_t)(unsigned)rfl((int)pq[1]) << 32) | (unsigned)rfl((int)pq[0]));
    GAS unsigned* outp = (GAS unsigned*)(((uintptr_t)(unsigned)rfl((int)pq[3]) << 32) | (unsigned)rfl((int)pq[2]));
#pragma unroll
    for (int cg = 0; cg < 2; ++cg)
#pragma unroll
      for (int s = 0; s < 4; ++s) { const int r = 16 * cg + c; qf[cg][s] = qp[(((r >> 1) * HQ + 2 * h + (r & 1)) * D + 32 * s + 8 * g) / 8]; }
    len_raw = *(LAS int*)(ctl + C_LENS + 4 * c);
    const Part p = partition(rfl(*(LAS int*)(ctl + C_P)));
    const int len = p.len, Pe = p.Pe, S_w = p.S_w, spare_w = p.spare_w, T_w = p.T_w, lo = p.lo, hi = p.hi, xoff = p.xoff, ns = p.ns; const unsigned xmask = p.xmask; const bool all_masked = p.all_masked;
    const int minlen = LK - rdl(row_scan_max(LK - len), 15);
    {
      const bool z0 = __shfl((int)all_masked, c >> 1) != 0, z1 = __shfl((int)all_masked, 8 + (c >> 1)) != 0;
#pragma unroll
      for (int s = 0; s < 4; ++s) {
        u32x4 t0 = __builtin_bit_cast(u32x4, qf[0][s]), t1 = __builtin_bit_cast(u32x4, qf[1][s]);
#pragma unroll
        for (int e = 0; e < 4; ++e) { t0[e] = z0 ? 0u : t0[e]; t1[e] = z1 ? 0u : t1[e]; }
        qf[0][s] = __builtin_bit_cast(bf16x8, t0); qf[1][s] = __builtin_bit_cast(bf16x8, t1);
      }
    }
    const int len0 = __shfl(len, c >> 1), len1 = __shfl(len, 8 + (c >> 1));
    const unsigned ka0 = c * 256 + ((g ^ c) << 4);
    const int tv = 4 * g + (c >> 2);
    const unsigned va0 = 4096 + tv * 256 + ((tv & 7) << 5) + (c & 3) * 8;
    int j = 0, seen = 0;
    Soft s0, s1;
    auto page = [&](auto SHc, const bf16x8 (&qa)[4], const bf16x8 (&qb)[4], int la, int lb, int t0, bool mask) {
      constexpr bool SH = decltype(SHc)::value;
      const int i = 4 * j + cw;
      LAS unsigned char* sl = lds + L_RING + (i & (NSLOT - 1)) * 8192;
      if (seen < 2 * i + 1) seen = lds_wait_ge(ctl + C_LANDED, 2 * i + 1);
      bf16x8 kf[4];
#pragma unroll
      for (int s = 0; s < 4; ++s) kf[s] = *(LAS bf16x8*)(sl + (ka0 ^ (s << 6)));
      f32x4 xa = {0.f, 0.f, 0.f, 0.f}, xb = {0.f, 0.f, 0.f, 0.f};
#pragma unroll
      for (int s = 0; s < 4; ++s) { xa = __builtin_amdgcn_mfma_f32_16x16x32_bf16(kf[s], qa[s], xa, 0, 0, 0);
        if (SH) xb = __builtin_amdgcn_mfma_f32_16x16x32_bf16(kf[s], qb[s], xb, 0, 0, 0); }
      const s16x4 pa = soft_step(s0, xa, mask, t0 + 4 * g, la);
      s16x4 pb; if (SH) pb = soft_step(s1, xb, mask, t0 + 4 * g, lb);
      if (seen < 2 * i + 2) seen = lds_wait_ge(ctl + C_LANDED, 2 * i + 2);
      s16x4 vf[8];
#pragma unroll
      for (int blk = 0; blk < 8; ++blk) vf[blk] = __builtin_amdgcn_ds_read_tr16_b64_v4i16((LAS s16x4*)(sl + (va0 ^ (blk << 5))));
      ++j;
      asm volatile("s_waitcnt lgkmcnt(0)" : "+v"(vf[0]), "+v"(vf[1]), "+v"(vf[2]), "+v"(vf[3]), "+v"(vf[4]), "+v"(vf[5]), "+v"(vf[6]), "+v"(vf[7]) :: "memory");
      lds_st(ctl + C_REL + 4 * cw, j);
#pragma unroll
      for (int blk = 0; blk < 8; ++blk) { s0.o[blk] = __builtin_amdgcn_mfma_f32_16x16x16bf16_1k(vf[blk], pa, s0.o[blk], 0, 0, 0);
        if (SH) s1.o[blk] = __builtin_amdgcn_mfma_f32_16x16x16bf16_1k(vf[blk], pb, s1.o[blk], 0, 0, 0); }
    };
    int npass = WARM ? 2 : 1;
    asm volatile("" : "+s"(npass));
#pragma nounroll
    for (int pass = 2 - npass; pass < 2; ++pass) {
      const bool real = pass != 0;
      if (real && ns > 0) {
        soft_reset(s0); soft_reset(s1);
        for (int i = cw; i < ns; i += 4) { const int t0 = (S_w + i) * PAGE; page(std::true_type{}, qf[0], qf[1], len0, len1, t0, t0 + PAGE > minlen); }
        s0.l = xrow16_sum(s0.l); s1.l = xrow16_sum(s1.l);
        if (cw > 0) lds_wait_ge(ctl + C_CSTEP, cw);
#pragma unroll
        for (int cg = 0; cg < 2; ++cg) { Soft& s = cg ? s1 : s0;
          LAS unsigned char* r = lds + L_MRG + (16 * cg + c) * ROW;
          if (cw > 0) {
            const f32x2 ml = *(LAS f32x2*)(r + 512);
            const float M = fmaxf(ml[0], s.m), f0 = mfac(ml[0], M), f1 = mfac(s.m, M);
#pragma unroll
            for (int blk = 0; blk < 8; ++blk) s.o[blk] = *(LAS f32x4*)(r + blk * 64 + g * 16) * f0 + s.o[blk] * f1;
            s.l = ml[1] * f0 + s.l * f1; s.m = M;
          }
#pragma unroll
          for (int blk = 0; blk < 8; ++blk) *(LAS f32x4*)(r + blk * 64 + g * 16) = s.o[blk];
          lds_drain();
          if (g == 0) *(LAS f32x2*)(r + 512) = f32x2{s.m, s.l};
        }
        lds_drain();
        lds_st(ctl + C_CSTEP, cw + 1);
      }
      unsigned m = real ? xmask : 0u;
      int kx = 0;
      for (;;) {
        const bool last = m == 0;
        const int b = last ? w : __builtin_ctz(m);
        const int i0 = last ? spare_w : rdl(lo, b) - S_w;
        const int i1 = last ? (real ? T_w : 0) : rdl(hi, b) - S_w;
        const int lp0 = last ? Pe : rdl(lo, b) + rdl(xoff, b);
        bf16x8 qa[4];
        if (b < 8) {
#pragma unroll
          for (int s = 0; s < 4; ++s) qa[s] = qf[0][s];
        } else {
#pragma unroll
          for (int s = 0; s < 4; ++s) qa[s] = qf[1][s];
        }
        const int lb = rdl(len, b);
        soft_reset(s0);
        for (int i = 4 * j + cw; i < i1; i += 4) { const int t0 = (lp0 + i - i0) * PAGE; page(std::false_type{}, qa, qa, lb, lb, t0, t0 + PAGE > lb); }
        if (!last && kx > 0) lds_wait_ge(ctl + C_RFREE, kx);
        const float lt = xrow16_sum(s0.l);
        if ((c >> 1) == (b & 7)) {
          LAS unsigned char* r = lds + (last ? L_RECF : L_RECX) + (cw * 2 + (c & 1)) * ROW;
#pragma unroll
          for (int blk = 0; blk < 8; ++blk) *(LAS f32x4*)(r + blk * 64 + g * 16) = s0.o[blk];
          if (g == 0) *(LAS f32x2*)(r + 512) = f32x2{s0.m, lt};
        }
        if (last) break;
        lds_drain();
        if (lane == 0) __hip_atomic_fetch_add((LAS int*)(ctl + C_RCNT), 1, __ATOMIC_RELAXED, __HIP_MEMORY_SCOPE_WORKGROUP);
        m &= m - 1; ++kx;
      }
      if (real) wg_barrier();
      if (cw < 2) {
        const int r = cw, d0 = 2 * lane;
        float M = -INFINITY; f32x2 ml[5], o[5];
#pragma unroll
        for (int k = 0; k < 5; ++k) { ml[k] = *(LAS f32x2*)(lds + L_RECF + (k * 2 + r) * ROW + 512); o[k] = *(LAS f32x2*)(lds + L_RECF + (k * 2 + r) * ROW + d0 * 4); }
#pragma unroll
        for (int k = 0; k < 5; ++k) M = fmaxf(M, ml[k][0]);
        float L = 0.f, o0 = 0.f, o1 = 0.f;
#pragma unroll
        for (int k = 0; k < 5; ++k) { const float f = mfac(ml[k][0], M); L = fmaf(f, ml[k][1], L); o0 = fmaf(f, o[k][0], o0); o1 = fmaf(f, o[k][1], o1); }
        const float inv = __builtin_amdgcn_rcpf(L);
        unsigned res = cvtpk(o0 * inv, o1 * inv);
        asm volatile("" : "+v"(res));
        if (real) OUT_STORE(outp + (((w * HQ + 2 * h + r) * D + d0) >> 1), res);
      }
    }
  } else {
    if (lane < 8) *(LAS int*)(ctl + 4 * lane) = 0;
    wg_barrier();
    STAMP(2);
    const u32x2 pws = *(LAS u32x2*)(ctl + C_PTR + 16);
    unsigned char* ws = (unsigned char*)(((uintptr_t)(unsigned)rfl((int)pws[1]) << 32) | (unsigned)rfl((int)pws[0]));
    const unsigned long long epoch = __hip_atomic_load((gu64*)(ws + W_EPOCH), RLX_AGENT);
    const __amdgpu_buffer_rsrc_t prsrc = __builtin_amdgcn_make_buffer_rsrc(ws + W_PART, 0, PART_BYTES, 0x00020000);
    len_raw = *(LAS int*)(ctl + C_LENS + 4 * c);
    const Part p = partition(rfl(*(LAS int*)(ctl + C_P)));
    const unsigned long long tag = epoch + 1ull;
    const int Pe = p.Pe, S = p.S, E = p.E, spare = p.spare, exc = p.exc, ns = p.ns; const unsigned xmask = p.xmask;
    const unsigned blk = w * 16 + h;
    const int r = lane >> 5, ch = lane & 31;
    if (ns > 0) {
      lds_wait_ge(ctl + C_CSTEP, NSW);
#pragma unroll 4
      for (int k = 0; k < 16; ++k) { const int off = (2 * k + r) * ROW + ch * 16;
        const u32x4 v = *(LAS u32x4*)(lds + L_MRG + off);
        __builtin_amdgcn_raw_buffer_store_b128(v, prsrc, off, blk * PS_BYTES, 16); }
      if (lane < 32) { const u32x2 v = *(LAS u32x2*)(lds + L_MRG + lane * ROW + 512);
        __builtin_amdgcn_raw_buffer_store_b64(v, prsrc, lane * ROW + 512, blk * PS_BYTES, 16); }
    }
    int kx = 0;
    for (unsigned m = xmask; m; m &= m - 1, ++kx) {
      const int b = __builtin_ctz(m);
      lds_wait_ge(ctl + C_RCNT, NSW * (kx + 1));
      float M = -INFINITY; f32x2 ml[4]; f32x4 ok[4];
#pragma unroll
      for (int k = 0; k < 4; ++k) { ml[k] = *(LAS f32x2*)(lds + L_RECX + (k * 2 + r) * ROW + 512); ok[k] = *(LAS f32x4*)(lds + L_RECX + (k * 2 + r) * ROW + ch * 16); M = fmaxf(M, ml[k][0]); }
      float L = 0.f; f32x4 o = {0.f, 0.f, 0.f, 0.f};
#pragma unroll
      for (int k = 0; k < 4; ++k) { const float f = mfac(ml[k][0], M); L = fmaf(f, ml[k][1], L); o += ok[k] * f; }
      asm volatile("s_waitcnt lgkmcnt(0)" : "+v"(o) :: "memory");
      lds_st(ctl + C_RFREE, kx + 1);
      const unsigned so = W_PX + (blk * 16 + b) * PX_BYTES;
      __builtin_amdgcn_raw_buffer_store_b128(__builtin_bit_cast(u32x4, o), prsrc, r * ROW + ch * 16, so, 16);
      if (ch == 0) __builtin_amdgcn_raw_buffer_store_b64(__builtin_bit_cast(u32x2, f32x2{M, L}), prsrc, r * ROW + 512, so, 16);
    }
    asm volatile("s_waitcnt vmcnt(0)" ::: "memory");
    if (lane == 0) __hip_atomic_store((gu64*)(ws + W_FLAG + blk * FLAG_STRIDE), tag, RLX_AGENT);
    STAMP(3);
    const int E_w = rdl(E, w), exc_w = rdl(exc, w);
    const bool sh_ov = spare > 0 && S < Pe;
    const bool ex_ov = min(S + spare, E_w + exc_w) > max(S, E_w);
    unsigned cmask = (unsigned)(__ballot(sh_ov) & 0xFFFFull) | ((unsigned)(__ballot(ex_ov) & 0xFFFFull) << 16);
    {
      const bool need = lane < 16 && (sh_ov || ex_ov);
      gu64* f = (gu64*)(ws + W_FLAG + (size_t)(c * 16 + h) * FLAG_STRIDE);
#pragma nounroll
      for (unsigned spins = 0; spins < (1u << 20); ++spins) {
        const bool ok = !need || __hip_atomic_load(f, RLX_AGENT) == tag;
        if (__all(ok)) break;
        __builtin_amdgcn_s_sleep(32);
      }
      asm volatile("" ::: "memory");
    }
    STAMP(4);
    {
      const unsigned vso = lane < 16 ? (unsigned)(c * 16 + h) * PS_BYTES + (unsigned)(2 * w) * ROW : W_PX + ((unsigned)(c * 16 + h) * 16 + w) * PX_BYTES;
      float M = -INFINITY, L = 0.f; f32x4 o = {0.f, 0.f, 0.f, 0.f};
      constexpr int GB = 10;
      while (cmask) {
        u32x4 po[GB]; u32x2 pml[GB]; bool pv[GB];
#pragma unroll
        for (int k = 0; k < GB; ++k) {
          pv[k] = cmask != 0;
          const int id = pv[k] ? __builtin_ctz(cmask) : 0; cmask &= cmask - 1;
          const unsigned so = pv[k] ? (unsigned)rdl((int)vso, id) : PART_BYTES;
          po[k] = __builtin_amdgcn_raw_buffer_load_b128(prsrc, r * ROW + ch * 16 + so, 0, 16);
          pml[k] = __builtin_amdgcn_raw_buffer_load_b64(prsrc, r * ROW + 512 + so, 0, 16);
        }
#pragma unroll
        for (int k = 0; k < GB; ++k) {
          const float mk = pv[k] ? __uint_as_float(pml[k][0]) : -INFINITY, lk = __uint_as_float(pml[k][1]);
          const float Mn = fmaxf(M, mk), f0 = mfac(M, Mn), f1 = mfac(mk, Mn);
          o = o * f0 + __builtin_bit_cast(f32x4, po[k]) * f1; L = L * f0 + lk * f1; M = Mn;
        }
      }
      LAS unsigned char* rec = lds + L_RECF + (4 * 2 + r) * ROW;
      *(LAS f32x4*)(rec + ch * 16) = o;
      if (ch == 0) *(LAS f32x2*)(rec + 512) = f32x2{M, L};
    }
    STAMP(5);
    wg_barrier();
    STAMP(6);
    if (blk == 0) {
#pragma nounroll
      for (unsigned spins = 0; spins < (1u << 20); ++spins) {
        bool ok = true;
#pragma unroll
        for (int k = 0; k < 4; ++k) ok &= __hip_atomic_load((gu64*)(ws + W_FLAG + (size_t)(k * 64 + lane) * FLAG_STRIDE), RLX_AGENT) == tag;
        if (__all(ok)) break;
        __builtin_amdgcn_s_sleep(8);
      }
      if (lane == 0) __hip_atomic_store((gu64*)(ws + W_EPOCH), tag, RLX_AGENT);
    }
  }
}

extern "C" void kernel_launch(void* const* d_in, const int* in_sizes, int n_in, void* d_out, int out_size, void* d_ws, size_t ws_size, hipStream_t stream) {
  Args a;
  a.q = (const bf16x8*)d_in[0]; a.k = d_in[1]; a.v = d_in[2]; a.bt = (const int*)d_in[3]; a.lens = (const int*)d_in[4];
  a.out = (unsigned*)d_out; a.ws = (unsigned char*)d_ws; a.kv_bytes = (unsigned)in_sizes[1] * 2u; a.pad = 0;
  hipFuncSetAttribute((const void*)radix_attn, hipFuncAttributeMaxDynamicSharedMemorySize, LDS_BYTES);
  hipLaunchKernelGGL(radix_attn, dim3(256), dim3(NTHR), LDS_BYTES, stream, a);
}
```

```cpp
#define SHCAP 21
#define POLLAT 9
#include <hip/hip_runtime.h>
#include <hip/hip_bf16.h>
#include <stdint.h>

#define NSLOT 16
#define INFL 20
#define KV_NT 1
#define TLBPF 0
#define OUTSC1 0
#define OUT_STORE(p, v) (*(p) = (v))
#define ABL_NOCOMP 0
#define SLEEPN 1
#define ABL_NOSVC 0
#define ABL_NOSTORE 0
#define ABL_NOGATHER 0
#define ABL_NOPOLL 0
#define POLLSLEEP 32
#define WARM 1
#define DIAG 0
#define MAPSWAP 1

constexpr int B = 16, HQ = 32, HKV = 16, D = 128, PAGE = 16, NB = 256, LK = NB * PAGE;
constexpr int NSW = 4, NTHR = (NSW + 2) * 64;
constexpr float C2 = 0.088388347648318440f * 1.4426950408889634f;
constexpr unsigned PAGE_BYTES = PAGE * HKV * D * 2;
constexpr unsigned TOK_BYTES = HKV * D * 2;

constexpr int ROW = 528;
constexpr int L_RING = 0;
constexpr int L_MRG = NSLOT * 8192;
constexpr int L_TAB = L_RING + (NSLOT - 2) * 8192;
constexpr int L_RECX = L_MRG + 32 * ROW;
constexpr int L_RECF = L_RECX + 4 * 2 * ROW;
constexpr int L_MISC = L_RECF + 5 * 2 * ROW;
constexpr int C_LANDED = 0, C_REL = 4, C_CSTEP = 20, C_RCNT = 24, C_RFREE = 28, C_P = 32, C_DUMMY = 36, C_READY = 40, C_PTR = 48, C_LENS = 80;
constexpr int LDS_BYTES = L_MISC + 160;
static_assert(NSLOT % 4 == 0 && INFL % 4 == 0 && (NSLOT & (NSLOT - 1)) == 0, "ring geometry");
static_assert(LDS_BYTES <= 160 * 1024, "LDS");

constexpr size_t W_EPOCH = 0, W_FLAG = 256, FLAG_STRIDE = 128;
constexpr size_t W_PART = 65536;
constexpr unsigned PS_BYTES = 32 * 512 + 256, PX_BYTES = 2 * 512 + 128;
constexpr unsigned W_PX = 256 * PS_BYTES;
constexpr unsigned PART_BYTES = W_PX + 256 * 16 * PX_BYTES;
#define STAMP(k) do {} while (0)

typedef __bf16 bf16x8 __attribute__((ext_vector_type(8)));
typedef __bf16 bf16x2 __attribute__((ext_vector_type(2)));
typedef short s16x4 __attribute__((ext_vector_type(4)));
typedef float f32x4 __attribute__((ext_vector_type(4)));
typedef float f32x2 __attribute__((ext_vector_type(2)));
typedef unsigned u32x4 __attribute__((ext_vector_type(4)));
typedef unsigned u32x2 __attribute__((ext_vector_type(2)));
typedef int i32x4 __attribute__((ext_vector_type(4)));
#define LAS __attribute__((address_space(3)))
#define GAS __attribute__((address_space(1)))
#define DI __device__ __forceinline__
typedef GAS unsigned long long gu64;
#define RLX_AGENT __ATOMIC_RELAXED, __HIP_MEMORY_SCOPE_AGENT

DI int rfl(int x) { return __builtin_amdgcn_readfirstlane(x); }
DI int rdl(int x, int l) { return __builtin_amdgcn_readlane(x, l); }
template <int CTRL> DI int dpp_i(int oldv, int x) { return __builtin_amdgcn_update_dpp(oldv, x, CTRL, 0xf, 0xf, false); }
DI int row_scan_add(int x) {
  x += dpp_i<0x111>(0, x); x += dpp_i<0x112>(0, x); x += dpp_i<0x114>(0, x); x += dpp_i<0x118>(0, x); return x;
}
DI int row_scan_max(int x) {
  x = max(x, dpp_i<0x111>(0, x)); x = max(x, dpp_i<0x112>(0, x)); x = max(x, dpp_i<0x114>(0, x)); x = max(x, dpp_i<0x118>(0, x)); return x;
}
DI float xrow16_max(float x) {
  auto s = __builtin_amdgcn_permlane16_swap(__float_as_uint(x), __float_as_uint(x), false, false);
  x = fmaxf(__uint_as_float(s[0]), __uint_as_float(s[1]));
  auto t = __builtin_amdgcn_permlane32_swap(__float_as_uint(x), __float_as_uint(x), false, false);
  return fmaxf(__uint_as_float(t[0]), __uint_as_float(t[1]));
}
DI float xrow16_sum(float x) {
  auto s = __builtin_amdgcn_permlane16_swap(__float_as_uint(x), __float_as_uint(x), false, false);
  x = __uint_as_float(s[0]) + __uint_as_float(s[1]);
  auto t = __builtin_amdgcn_permlane32_swap(__float_as_uint(x), __float_as_uint(x), false, false);
  return __uint_as_float(t[0]) + __uint_as_float(t[1]);
}
DI unsigned cvtpk(float lo, float hi) { f32x2 v = {lo, hi}; bf16x2 b = __builtin_convertvector(v, bf16x2); return __builtin_bit_cast(unsigned, b); }
DI float ex2(float x) { return __builtin_amdgcn_exp2f(x); }
DI void wg_barrier() { asm volatile("s_waitcnt lgkmcnt(0)\n\ts_barrier" ::: "memory"); }
template <int N> DI void wait_vm() { asm volatile("s_waitcnt vmcnt(%0)" :: "i"(N) : "memory"); }

#define NTS " nt"
DI void dma4(unsigned m0v, __amdgpu_buffer_rsrc_t rsrc, unsigned soff, unsigned v0, unsigned v1, unsigned v2, unsigned v3) {
  asm volatile("s_mov_b32 m0, %0\n\t"
               "s_nop 4\n\t"
               "buffer_load_dwordx4 %1, %5, %6 offen" NTS " lds\n\t"
               "buffer_load_dwordx4 %2, %5, %6 offen offset:1024" NTS " lds\n\t"
               "buffer_load_dwordx4 %3, %5, %6 offen offset:2048" NTS " lds\n\t"
               "buffer_load_dwordx4 %4, %5, %6 offen offset:3072" NTS " lds"
               :: "s"(m0v), "v"(v0), "v"(v1), "v"(v2), "v"(v3), "s"(rsrc), "s"(soff) : "memory");
}

DI void set_m0(unsigned m0v, unsigned so) { asm volatile("s_mov_b32 m0, %0\n\ts_nop 3" :: "s"(m0v), "s"(so) : "memory"); }
template <int X> DI void wait_dma4(__amdgpu_buffer_rsrc_t rsrc, unsigned soff, unsigned v0, unsigned v1, unsigned v2, unsigned v3) {
  asm volatile("s_waitcnt vmcnt(%6)\n\t"
               "buffer_load_dwordx4 %0, %4, %5 offen" NTS " lds\n\t"
               "buffer_load_dwordx4 %1, %4, %5 offen offset:1024" NTS " lds\n\t"
               "buffer_load_dwordx4 %2, %4, %5 offen offset:2048" NTS " lds\n\t"
               "buffer_load_dwordx4 %3, %4, %5 offen offset:3072" NTS " lds"
               :: "v"(v0), "v"(v1), "v"(v2), "v"(v3), "s"(rsrc), "s"(soff), "i"(X) : "memory");
}

struct Args { const bf16x8* q; const void* k; const void* v; const int* bt; const int* lens; unsigned* out; unsigned char* ws; unsigned kv_bytes; unsigned pad; };

DI float mfac(float m, float M) { return (m == -INFINITY) ? 0.f : ex2((m - M) * C2); }

struct Soft { float m, l; f32x4 o[8]; };
DI void soft_reset(Soft& s) { s.m = -INFINITY; s.l = 0.f;
#pragma unroll
  for (int i = 0; i < 8; ++i) s.o[i] = f32x4{0.f, 0.f, 0.f, 0.f}; }

DI s16x4 soft_step(Soft& s, f32x4 x, bool mask, int tok0, int len) {
  if (mask) {
#pragma unroll
    for (int r = 0; r < 4; ++r) x[r] = (tok0 + r < len) ? x[r] : -INFINITY;
  }
  float pm = fmaxf(fmaxf(x[0], x[1]), fmaxf(x[2], x[3]));
  pm = xrow16_max(pm);
  const float mn = fmaxf(s.m, pm);
  const float ms = (mn == -INFINITY) ? 0.f : mn * C2;
  const float alpha = ex2(s.m * C2 - ms);
  float p[4];
#pragma unroll
  for (int r = 0; r < 4; ++r) p[r] = ex2(fmaf(x[r], C2, -ms));
  s.l = fmaf(s.l, alpha, (p[0] + p[1]) + (p[2] + p[3]));
  s.m = mn;
#pragma unroll
  for (int i = 0; i < 8; ++i) s.o[i] *= alpha;
  u32x2 pk = {cvtpk(p[0], p[1]), cvtpk(p[2], p[3])};
  return __builtin_bit_cast(s16x4, pk);
}

DI int lds_ld(LAS unsigned char* p) { return __hip_atomic_load((LAS int*)p, __ATOMIC_RELAXED, __HIP_MEMORY_SCOPE_WORKGROUP); }
DI void lds_st(LAS unsigned char* p, int v) { __hip_atomic_store((LAS int*)p, v, __ATOMIC_RELAXED, __HIP_MEMORY_SCOPE_WORKGROUP); }
DI int lds_wait_ge(LAS unsigned char* p, int need) {
  int v;
#pragma nounroll
  for (unsigned spins = 0;; ++spins) { v = rfl(lds_ld(p)); if (v >= need || spins > (1u << 24)) break; __builtin_amdgcn_s_sleep(SLEEPN); }
  asm volatile("" ::: "memory");
  return v;
}
DI void lds_drain() { asm volatile("s_waitcnt lgkmcnt(0)" ::: "memory"); }

__global__ void __launch_bounds__(NTHR, 2) radix_attn(Args a_) {
  extern __shared__ __attribute__((aligned(16))) unsigned char lds_raw[];
  LAS unsigned char* lds = (LAS unsigned char*)lds_raw;
  LAS unsigned char* ctl = lds + L_MISC;
  const int tid = threadIdx.x, lane = tid & 63, wave = rfl(tid >> 6);
  const int w = blockIdx.x & 15, h = blockIdx.x >> 4;
  const int c = lane & 15, g = lane >> 4;
  int len_raw;

  struct Part { int len, Pe, S, E, spare, exc, lo, hi, xoff, S_w, spare_w, T_w, ns, ss, hp, nsh; unsigned xmask; bool all_masked; };
  auto partition = [&](int P) {
    Part p;
    p.all_masked = len_raw <= 0;
    p.len = p.all_masked ? LK : min(len_raw, LK);
    const int np = (p.len + PAGE - 1) >> 4;
    p.Pe = min(P, rdl(row_scan_max(np), 15));
    const int n = max(np - p.Pe, 0);
    const int W = p.Pe + rdl(row_scan_add(n), 15);
    const int T = (W >> 4) + (c < (W & 15) ? 1 : 0);
    const int cap = max(1, ((W >> 4) * SHCAP) >> 6);
    int lg = 0;
    if (p.Pe > cap) lg = 1;
    if (p.Pe > 2 * cap) lg = 2;
    if (p.Pe > 4 * cap) lg = 3;
    if (p.Pe > 8 * cap) lg = 4;
    const int q = p.Pe >> lg, rq = p.Pe & ((1 << lg) - 1);
    const int sh = c < (1 << lg) ? q + (c < rq ? 1 : 0) : 0;
    p.nsh = min(1 << lg, p.Pe);
    p.ns = w < (1 << lg) ? q + (w < rq ? 1 : 0) : 0;
    p.ss = w * q + min(w, rq);
    const int own = min(n, T - sh);
    p.exc = n - own; p.spare = T - sh - own;
    p.S = row_scan_add(p.spare) - p.spare;
    p.E = row_scan_add(p.exc) - p.exc;
    p.S_w = rdl(p.S, w); p.spare_w = rdl(p.spare, w); p.T_w = rdl(T, w);
    p.hp = p.ns + p.spare_w;
    p.lo = max(p.S_w, p.E); p.hi = min(p.S_w + p.spare_w, p.E + p.exc);
    p.xmask = (unsigned)(__ballot(p.hi > p.lo) & 0xFFFFull);
    p.xoff = p.Pe + own - p.E;
    return p;
  };

  if (__builtin_expect(wave == 0, 1)) {
    __builtin_amdgcn_s_setprio(3);
    typedef unsigned u32x16 __attribute__((ext_vector_type(16)));
    u32x16 ka = *(const __attribute__((address_space(4))) u32x16*)__builtin_amdgcn_kernarg_segment_ptr();
    asm volatile("" : "+s"(ka));
    Args a;
    a.q = (const bf16x8*)(((uintptr_t)ka[1] << 32) | ka[0]); a.k = (const void*)(((uintptr_t)ka[3] << 32) | ka[2]); a.v = (const void*)(((uintptr_t)ka[5] << 32) | ka[4]);
    a.bt = (const int*)(((uintptr_t)ka[7] << 32) | ka[6]); a.lens = (const int*)(((uintptr_t)ka[9] << 32) | ka[8]); a.out = (unsigned*)(((uintptr_t)ka[11] << 32) | ka[10]);
    a.ws = (unsigned char*)(((uintptr_t)ka[13] << 32) | ka[12]); a.kv_bytes = ka[14];
    len_raw = *(const GAS int*)((uintptr_t)a.lens + 4 * c);
    const GAS i32x4* bt4 = (const GAS i32x4*)(uintptr_t)a.bt;
    i32x4 rk16[16];
#pragma unroll
    for (int k = 0; k < 16; ++k) rk16[k] = bt4[k * (NB / 4) + lane];
    STAMP(8);
    asm volatile("s_barrier" ::: "memory");
    if (lane == 0) { *(LAS u32x4*)(ctl + C_PTR) = u32x4{(unsigned)(uintptr_t)a.q, (unsigned)((uintptr_t)a.q >> 32), (unsigned)(uintptr_t)a.out, (unsigned)((uintptr_t)a.out >> 32)};
                     *(LAS u32x2*)(ctl + C_PTR + 16) = u32x2{(unsigned)(uintptr_t)a.ws, (unsigned)((uintptr_t)a.ws >> 32)}; }
    unsigned vk[4], vv[4];
#pragma unroll
    for (int i = 0; i < 4; ++i) { const int t = 4 * i + g;
      vk[i] = t * TOK_BYTES + ((c ^ t) << 4) - 1024 * i;
      vv[i] = t * TOK_BYTES + ((c ^ ((t & 7) << 1)) << 4) - 1024 * i; }
    asm volatile("" : "+v"(vk[0]), "+v"(vk[1]), "+v"(vk[2]), "+v"(vk[3]), "+v"(vv[0]), "+v"(vv[1]), "+v"(vv[2]), "+v"(vv[3]));
    const unsigned ring = (unsigned)(uintptr_t)(lds + L_RING);
    const __amdgpu_buffer_rsrc_t rk = __builtin_amdgcn_make_buffer_rsrc((void*)a.k, 0, a.kv_bytes, 0x00020000);
    const __amdgpu_buffer_rsrc_t rv = __builtin_amdgcn_make_buffer_rsrc((void*)a.v, 0, a.kv_bytes, 0x00020000);
#pragma unroll
    for (int k = 0; k < 16; ++k) *(LAS i32x4*)(lds + L_TAB + (k * NB + 4 * lane) * 4) = rk16[k];
    i32x4 acc = rk16[1] ^ rk16[0];
#pragma unroll
    for (int k = 2; k < 16; ++k) acc |= rk16[k] ^ rk16[0];
    int P;
    {
      const unsigned long long b0 = __ballot(acc[0] != 0), b1 = __ballot(acc[1] != 0), b2 = __ballot(acc[2] != 0), b3 = __ballot(acc[3] != 0);
      const unsigned long long any = b0 | b1 | b2 | b3;
      const int l = any ? (int)__builtin_ctzll(any) : 64;
      const unsigned long long bit = any & (0ull - any);
      P = 4 * l + ((b0 & bit) ? 0 : (b1 & bit) ? 1 : (b2 & bit) ? 2 : (b3 & bit) ? 3 : 0);
    }
    STAMP(2);
    const Part p = partition(P);
    const int Pe = p.Pe, S_w = p.S_w - p.ns, hp = p.hp, ns = p.ns, ss = p.ss, T_w = p.T_w, lo = p.lo, hi = p.hi, xoff = p.xoff; const unsigned xmask = p.xmask;
    STAMP(7);
    auto page_src = [&](int r) {
      const int i = 64 * r + lane;
      int idx = (i < ns) ? ss + i : w * NB + Pe + (i - hp);
      for (unsigned m = xmask; m; m &= m - 1) {
        const int b = __builtin_ctz(m);
        const int lo_b = rdl(lo, b) - S_w, hi_b = rdl(hi, b) - S_w, off_b = rdl(xoff, b) + b * NB + S_w;
        if (i >= lo_b && i < hi_b) idx = i + off_b;
      }
      return (unsigned)*(LAS int*)(lds + L_TAB + 4 * (i < T_w ? idx : 0)) * PAGE_BYTES + (unsigned)h * (D * 2);
    };
    unsigned vp0 = page_src(0), vp1 = 0, vp2 = 0, vp3 = 0;
    STAMP(3);
    int pub = 0, relnext = 0;
    auto publish = [&](int nl) { if (nl > pub) { pub = nl; lds_st(ctl + C_LANDED, nl); } };
    auto open_up = [&]() {
      if (lane == 0) *(LAS int*)(ctl + C_P) = P;
      if (lane < 16) *(LAS int*)(ctl + C_LENS + 4 * lane) = len_raw;
      lds_drain();
      lds_st(ctl + C_READY, 1);
      vp1 = page_src(1); vp2 = page_src(2); vp3 = page_src(3);
      asm volatile("" : "+v"(vp1), "+v"(vp2), "+v"(vp3));
    };
    unsigned so = (unsigned)rdl((int)vp0, 0), m0k = ring;
    set_m0(m0k, so);
    auto step = [&](int i) {
      wait_dma4<INFL>(rk, so, vk[0], vk[1], vk[2], vk[3]);
      publish(2 * i - INFL / 4);
      relnext = lds_ld(ctl + C_REL + 4 * ((i + 1) & 3));
      set_m0(m0k + 4096, so);
      wait_dma4<INFL>(rv, so, vv[0], vv[1], vv[2], vv[3]);
      publish(2 * i + 1 - INFL / 4);
      const int in = i + 1;
      if (in >= NSLOT && in < T_w) {
        const int need = (in >> 2) - NSLOT / 4 + 1;
        if (rfl(relnext) < need) {
          LAS unsigned char* rel = ctl + C_REL + 4 * (in & 3);
          if (rfl(lds_ld(rel)) < need) {
            wait_vm<12>(); publish(2 * in - 3);
            if (rfl(lds_ld(rel)) < need) { wait_vm<4>(); publish(2 * in - 1);
              if (rfl(lds_ld(rel)) < need) { wait_vm<0>(); publish(2 * in); lds_wait_ge(rel, need); } }
          }
        }
        asm volatile("" ::: "memory");
      }
      if ((in & 63) == 0) { vp0 = vp1; vp1 = vp2; vp2 = vp3; }
      so = (unsigned)rdl((int)vp0, in & 63);
      m0k = ring + (in & (NSLOT - 1)) * 8192;
      set_m0(m0k, so);
    };
    int i = 0;
#pragma nounroll
    for (const int n0 = min(T_w, 3); i < n0; ++i) {
      wait_dma4<INFL>(rk, so, vk[0], vk[1], vk[2], vk[3]);
      set_m0(m0k + 4096, so);
      wait_dma4<INFL>(rv, so, vv[0], vv[1], vv[2], vv[3]);
      so = (unsigned)rdl((int)vp0, i + 1);
      m0k = ring + (i + 1) * 8192;
      set_m0(m0k, so);
    }
    open_up();
#pragma nounroll
    for (; i < T_w; ++i) step(i);
    STAMP(5);
    if (INFL > 16) { wait_vm<16>(); publish(2 * T_w - 4); }
    if (INFL > 12) { wait_vm<12>(); publish(2 * T_w - 3); }
    if (INFL > 8) { wait_vm<8>(); publish(2 * T_w - 2); }
    wait_vm<4>(); publish(2 * T_w - 1);
    wait_vm<0>(); publish(2 * T_w);
    STAMP(6);
  } else if (wave <= NSW) {
    const int cw = wave - 1;
    wg_barrier();
    const unsigned ka0 = c * 256 + ((g ^ c) << 4);
    const int tv = 4 * g + (c >> 2);
    const unsigned va0 = 4096 + tv * 256 + ((tv & 7) << 5) + (c & 3) * 8;
    bf16x8 qf[2][4];
#pragma unroll
    for (int cg = 0; cg < 2; ++cg)
#pragma unroll
      for (int s = 0; s < 4; ++s) qf[cg][s] = bf16x8{};
    GAS unsigned* outp = nullptr;
    int len = LK, Pe = 0, S_w = 0, hp = 0, ss = 0, T_w = 1, lo = 0, hi = 0, xoff = 0, ns = 2, minlen = 0, len0 = LK, len1 = LK;
    unsigned xmask = 0;
    int j = 0, seen = 0x7fffffff;
    LAS unsigned char* relp = ctl + C_DUMMY;
    LAS unsigned char* cstp = ctl + C_DUMMY;
    LAS unsigned char* mrg = lds + L_RING + (NSLOT - 6) * 8192;
    Soft s0, s1;
    auto page = [&](auto SHc, const bf16x8 (&qa)[4], const bf16x8 (&qb)[4], int la, int lb, int t0, bool mask) {
      constexpr bool SH = decltype(SHc)::value;
      const int i = 4 * j + cw;
      LAS unsigned char* sl = lds + L_RING + (i & (NSLOT - 1)) * 8192;
      if (seen < 2 * i + 1) seen = lds_wait_ge(ctl + C_LANDED, 2 * i + 1);
      bf16x8 kf[4];
#pragma unroll
      for (int s = 0; s < 4; ++s) kf[s] = *(LAS bf16x8*)(sl + (ka0 ^ (s << 6)));
      f32x4 xa = {0.f, 0.f, 0.f, 0.f}, xb = {0.f, 0.f, 0.f, 0.f};
#pragma unroll
      for (int s = 0; s < 4; ++s) { xa = __builtin_amdgcn_mfma_f32_16x16x32_bf16(kf[s], qa[s], xa, 0, 0, 0);
        if (SH) xb = __builtin_amdgcn_mfma_f32_16x16x32_bf16(kf[s], qb[s], xb, 0, 0, 0); }
      const s16x4 pa = soft_step(s0, xa, mask, t0 + 4 * g, la);
      s16x4 pb; if (SH) pb = soft_step(s1, xb, mask, t0 + 4 * g, lb);
      if (seen < 2 * i + 2) seen = lds_wait_ge(ctl + C_LANDED, 2 * i + 2);
      s16x4 vf[8];
#pragma unroll
      for (int blk = 0; blk < 8; ++blk) vf[blk] = __builtin_amdgcn_ds_read_tr16_b64_v4i16((LAS s16x4*)(sl + (va0 ^ (blk << 5))));
      ++j;
      asm volatile("s_waitcnt lgkmcnt(0)" : "+v"(vf[0]), "+v"(vf[1]), "+v"(vf[2]), "+v"(vf[3]), "+v"(vf[4]), "+v"(vf[5]), "+v"(vf[6]), "+v"(vf[7]) :: "memory");
      lds_st(relp, j);
#pragma unroll
      for (int blk = 0; blk < 8; ++blk) { s0.o[blk] = __builtin_amdgcn_mfma_f32_16x16x16bf16_1k(vf[blk], pa, s0.o[blk], 0, 0, 0);
        if (SH) s1.o[blk] = __builtin_amdgcn_mfma_f32_16x16x16bf16_1k(vf[blk], pb, s1.o[blk], 0, 0, 0); }
    };
    int pass0 = WARM ? 0 : 1;
    asm volatile("" : "+s"(pass0));
#pragma nounroll
    for (int pass = pass0; pass < 2; ++pass) {
      const bool real = pass != 0;
      if (real) {
        lds_wait_ge(ctl + C_READY, 1);
        STAMP(2);
        const u32x4 pq = *(LAS u32x4*)(ctl + C_PTR);
        const GAS bf16x8* qp = (const GAS bf16x8*)(((uintptr_t)(unsigned)rfl((int)pq[1]) << 32) | (unsigned)rfl((int)pq[0]));
        outp = (GAS unsigned*)(((uintptr_t)(unsigned)rfl((int)pq[3]) << 32) | (unsigned)rfl((int)pq[2]));
#pragma unroll
        for (int cg = 0; cg < 2; ++cg)
#pragma unroll
          for (int s = 0; s < 4; ++s) { const int r = 16 * cg + c; qf[cg][s] = qp[(((r >> 1) * HQ + 2 * h + (r & 1)) * D + 32 * s + 8 * g) / 8]; }
        len_raw = *(LAS int*)(ctl + C_LENS + 4 * c);
        const Part p = partition(rfl(*(LAS int*)(ctl + C_P)));
        len = p.len; Pe = p.Pe; S_w = p.S_w - p.ns; hp = p.hp; ss = p.ss; T_w = p.T_w; lo = p.lo; hi = p.hi; xoff = p.xoff; ns = p.ns; xmask = p.xmask;
        minlen = LK - rdl(row_scan_max(LK - len), 15);
        len0 = __shfl(len, c >> 1); len1 = __shfl(len, 8 + (c >> 1));
        const bool z0 = __shfl((int)p.all_masked, c >> 1) != 0, z1 = __shfl((int)p.all_masked, 8 + (c >> 1)) != 0;
#pragma unroll
        for (int s = 0; s < 4; ++s) {
          u32x4 t0 = __builtin_bit_cast(u32x4, qf[0][s]), t1 = __builtin_bit_cast(u32x4, qf[1][s]);
#pragma unroll
          for (int e = 0; e < 4; ++e) { t0[e] = z0 ? 0u : t0[e]; t1[e] = z1 ? 0u : t1[e]; }
          qf[0][s] = __builtin_bit_cast(bf16x8, t0); qf[1][s] = __builtin_bit_cast(bf16x8, t1);
        }
        j = 0; seen = 0; relp = ctl + C_REL + 4 * cw; cstp = ctl + C_CSTEP; mrg = lds + L_MRG;
      }
      if (real ? ns > 0 : (cw == 1 || cw == 2)) {
        soft_reset(s0); soft_reset(s1);
        for (int i = cw; i < ns; i += 4) { const int t0 = (ss + i) * PAGE; page(std::true_type{}, qf[0], qf[1], len0, len1, t0, t0 + PAGE > minlen); }
        if (real ? cw < ns : cw == 2) {
          s0.l = xrow16_sum(s0.l); s1.l = xrow16_sum(s1.l);
          if (real && cw > 0) lds_wait_ge(ctl + C_CSTEP, cw);
#pragma unroll
          for (int cg = 0; cg < 2; ++cg) { Soft& s = cg ? s1 : s0;
            LAS unsigned char* r = mrg + (16 * cg + c) * ROW;
            if (cw > 0) {
              const f32x2 ml = *(LAS f32x2*)(r + 512);
              const float M = fmaxf(ml[0], s.m), f0 = mfac(ml[0], M), f1 = mfac(s.m, M);
#pragma unroll
              for (int blk = 0; blk < 8; ++blk) s.o[blk] = *(LAS f32x4*)(r + blk * 64 + g * 16) * f0 + s.o[blk] * f1;
              s.l = ml[1] * f0 + s.l * f1; s.m = M;
            }
#pragma unroll
            for (int blk = 0; blk < 8; ++blk) *(LAS f32x4*)(r + blk * 64 + g * 16) = s.o[blk];
            lds_drain();
            if (g == 0) *(LAS f32x2*)(r + 512) = f32x2{s.m, s.l};
          }
          lds_drain();
          lds_st(cstp, cw + 1);
        }
      }
      if (real || cw == 0 || cw == 3) {
        unsigned m = xmask;
        int kx = 0;
        for (;;) {
          const bool last = m == 0;
          const int b = last ? w : __builtin_ctz(m);
          const int i0 = last ? hp : rdl(lo, b) - S_w;
          const int i1 = last ? T_w : rdl(hi, b) - S_w;
          const int lp0 = last ? Pe : rdl(lo, b) + rdl(xoff, b);
          bf16x8 qa[4];
          if (b < 8) {
#pragma unroll
            for (int s = 0; s < 4; ++s) qa[s] = qf[0][s];
          } else {
#pragma unroll
            for (int s = 0; s < 4; ++s) qa[s] = qf[1][s];
          }
          const int lb = rdl(len, b);
          soft_reset(s0);
          for (int i = 4 * j + cw; i < i1; i += 4) { const int t0 = (lp0 + i - i0) * PAGE; page(std::false_type{}, qa, qa, lb, lb, t0, t0 + PAGE > lb || !real);
          }
          if (!last && kx > 0) lds_wait_ge(ctl + C_RFREE, kx);
          const float lt = xrow16_sum(s0.l);
          if ((c >> 1) == (b & 7)) {
            LAS unsigned char* r = lds + (last ? L_RECF : L_RECX) + (cw * 2 + (c & 1)) * ROW;
#pragma unroll
            for (int blk = 0; blk < 8; ++blk) *(LAS f32x4*)(r + blk * 64 + g * 16) = s0.o[blk];
            if (g == 0) *(LAS f32x2*)(r + 512) = f32x2{s0.m, lt};
          }
          if (last) break;
          lds_drain();
          if (lane == 0) __hip_atomic_fetch_add((LAS int*)(ctl + C_RCNT), 1, __ATOMIC_RELAXED, __HIP_MEMORY_SCOPE_WORKGROUP);
          m &= m - 1; ++kx;
        }
      }
      if (real) wg_barrier();
      if (real ? cw < 2 : cw == 3) {
        const int r = cw & 1, d0 = 2 * lane;
        float M = -INFINITY; f32x2 ml[5], o[5];
#pragma unroll
        for (int k = 0; k < 5; ++k) { ml[k] = *(LAS f32x2*)(lds + L_RECF + (k * 2 + r) * ROW + 512); o[k] = *(LAS f32x2*)(lds + L_RECF + (k * 2 + r) * ROW + d0 * 4); }
#pragma unroll
        for (int k = 0; k < 5; ++k) M = fmaxf(M, ml[k][0]);
        float L = 0.f, o0 = 0.f, o1 = 0.f;
#pragma unroll
        for (int k = 0; k < 5; ++k) { const float f = mfac(ml[k][0], M); L = fmaf(f, ml[k][1], L); o0 = fmaf(f, o[k][0], o0); o1 = fmaf(f, o[k][1], o1); }
        const float inv = __builtin_amdgcn_rcpf(L);
        unsigned res = cvtpk(o0 * inv, o1 * inv);
        asm volatile("" : "+v"(res));
        if (real) OUT_STORE(outp + (((w * HQ + 2 * h + r) * D + d0) >> 1), res);
      }
    }
  } else {
    if (lane < 12) *(LAS int*)(ctl + 4 * lane) = 0;
    wg_barrier();
    lds_wait_ge(ctl + C_READY, 1);
    STAMP(2);
    const u32x2 pws = *(LAS u32x2*)(ctl + C_PTR + 16);
    unsigned char* ws = (unsigned char*)(((uintptr_t)(unsigned)rfl((int)pws[1]) << 32) | (unsigned)rfl((int)pws[0]));
    const unsigned long long epoch = __hip_atomic_load((gu64*)(ws + W_EPOCH), RLX_AGENT);
    const __amdgpu_buffer_rsrc_t prsrc = __builtin_amdgcn_make_buffer_rsrc(ws + W_PART, 0, PART_BYTES, 0x00020000);
    len_raw = *(LAS int*)(ctl + C_LENS + 4 * c);
    const Part p = partition(rfl(*(LAS int*)(ctl + C_P)));
    const unsigned long long tag = epoch + 1ull;
    const int S = p.S, E = p.E, spare = p.spare, exc = p.exc, ns = p.ns; const unsigned xmask = p.xmask;
    const unsigned blk = w * 16 + h;
    const int r = lane >> 5, ch = lane & 31;
    if (ns > 0 && !ABL_NOSVC && !ABL_NOSTORE) {
      lds_wait_ge(ctl + C_CSTEP, min(ns, NSW));
#pragma unroll 4
      for (int k = 0; k < 16; ++k) {
        const u32x4 v = *(LAS u32x4*)(lds + L_MRG + (2 * k + r) * ROW + ch * 16);
        __builtin_amdgcn_raw_buffer_store_b128(v, prsrc, k * 1024 + lane * 16, blk * PS_BYTES, 16); }
      if (lane < 32) { const u32x2 v = *(LAS u32x2*)(lds + L_MRG + lane * ROW + 512);
        __builtin_amdgcn_raw_buffer_store_b64(v, prsrc, 32 * 512 + lane * 8, blk * PS_BYTES, 16); }
    }
    int kx = 0;
    for (unsigned m = xmask; m; m &= m - 1, ++kx) {
      const int b = __builtin_ctz(m);
      lds_wait_ge(ctl + C_RCNT, NSW * (kx + 1));
      float M = -INFINITY; f32x2 ml[4]; f32x4 ok[4];
#pragma unroll
      for (int k = 0; k < 4; ++k) { ml[k] = *(LAS f32x2*)(lds + L_RECX + (k * 2 + r) * ROW + 512); ok[k] = *(LAS f32x4*)(lds + L_RECX + (k * 2 + r) * ROW + ch * 16); M = fmaxf(M, ml[k][0]); }
      float L = 0.f; f32x4 o = {0.f, 0.f, 0.f, 0.f};
#pragma unroll
      for (int k = 0; k < 4; ++k) { const float f = mfac(ml[k][0], M); L = fmaf(f, ml[k][1], L); o += ok[k] * f; }
      asm volatile("s_waitcnt lgkmcnt(0)" : "+v"(o) :: "memory");
      lds_st(ctl + C_RFREE, kx + 1);
      const unsigned so = W_PX + (blk * 16 + b) * PX_BYTES;
      __builtin_amdgcn_raw_buffer_store_b128(__builtin_bit_cast(u32x4, o), prsrc, lane * 16, so, 16);
      const f32x4 mlv = {__builtin_bit_cast(float, rdl(__builtin_bit_cast(int, M), 0)), __builtin_bit_cast(float, rdl(__builtin_bit_cast(int, L), 0)),
                         __builtin_bit_cast(float, rdl(__builtin_bit_cast(int, M), 32)), __builtin_bit_cast(float, rdl(__builtin_bit_cast(int, L), 32))};
      if (lane < 8) __builtin_amdgcn_raw_buffer_store_b128(__builtin_bit_cast(u32x4, mlv), prsrc, 1024 + lane * 16, so, 16);
    }
    asm volatile("s_waitcnt vmcnt(0)" ::: "memory");
    if (lane == 0) __hip_atomic_store((gu64*)(ws + W_FLAG + blk * FLAG_STRIDE), tag, RLX_AGENT);
    STAMP(3);
    const int E_w = rdl(E, w), exc_w = rdl(exc, w);
    const bool sh_ov = c < p.nsh;
    const bool ex_ov = min(S + spare, E_w + exc_w) > max(S, E_w);
    unsigned cmask = (ABL_NOSVC || ABL_NOGATHER) ? 0u : (unsigned)(__ballot(sh_ov) & 0xFFFFull) | ((unsigned)(__ballot(ex_ov) & 0xFFFFull) << 16);
    if (cmask) lds_wait_ge(ctl + C_LANDED, (p.T_w * POLLAT) >> 3);
    {
      const bool need = !ABL_NOSVC && !ABL_NOPOLL && lane < 16 && (sh_ov || ex_ov);
      gu64* f = (gu64*)(ws + W_FLAG + (size_t)(c * 16 + h) * FLAG_STRIDE);
#pragma nounroll
      for (unsigned spins = 0; spins < (1u << 20); ++spins) {
        const bool ok = !need || __hip_atomic_load(f, RLX_AGENT) == tag;
        if (__all(ok)) break;
        __builtin_amdgcn_s_sleep(POLLSLEEP);
      }
      asm volatile("" ::: "memory");
    }
    STAMP(4);
    {
      const unsigned vso = lane < 16 ? (unsigned)(c * 16 + h) * PS_BYTES + (unsigned)(2 * w) * 512 : W_PX + ((unsigned)(c * 16 + h) * 16 + w) * PX_BYTES;
      const unsigned vsm = lane < 16 ? (unsigned)(c * 16 + h) * PS_BYTES + 32 * 512 + (unsigned)(2 * w) * 8 : vso + 1024;
      float M = -INFINITY, L = 0.f; f32x4 o = {0.f, 0.f, 0.f, 0.f};
      constexpr int GB = 6;
      while (cmask) {
        u32x4 po[GB]; u32x2 pml[GB]; bool pv[GB];
#pragma unroll
        for (int k = 0; k < GB; ++k) {
          pv[k] = cmask != 0;
          const int id = pv[k] ? __builtin_ctz(cmask) : 0; cmask &= cmask - 1;
          const unsigned so = pv[k] ? (unsigned)rdl((int)vso, id) : PART_BYTES, sm = pv[k] ? (unsigned)rdl((int)vsm, id) : PART_BYTES;
          po[k] = __builtin_amdgcn_raw_buffer_load_b128(prsrc, lane * 16 + so, 0, 16);
          pml[k] = __builtin_amdgcn_raw_buffer_load_b64(prsrc, r * 8 + sm, 0, 16);
        }
#pragma unroll
        for (int k = 0; k < GB; ++k) {
          const float mk = pv[k] ? __uint_as_float(pml[k][0]) : -INFINITY, lk = __uint_as_float(pml[k][1]);
          const float Mn = fmaxf(M, mk), f0 = mfac(M, Mn), f1 = mfac(mk, Mn);
          o = o * f0 + __builtin_bit_cast(f32x4, po[k]) * f1; L = L * f0 + lk * f1; M = Mn;
        }
      }
      LAS unsigned char* rec = lds + L_RECF + (4 * 2 + r) * ROW;
      *(LAS f32x4*)(rec + ch * 16) = o;
      if (ch == 0) *(LAS f32x2*)(rec + 512) = f32x2{M, L};
    }
    STAMP(5);
    wg_barrier();
    STAMP(6);
    if (blk == 0) {
#pragma nounroll
      for (unsigned spins = 0; spins < (1u << 20); ++spins) {
        bool ok = true;
#pragma unroll
        for (int k = 0; k < 4; ++k) ok &= __hip_atomic_load((gu64*)(ws + W_FLAG + (size_t)(k * 64 + lane) * FLAG_STRIDE), RLX_AGENT) == tag;
        if (__all(ok)) break;
        __builtin_amdgcn_s_sleep(8);
      }
      if (lane == 0) __hip_atomic_store((gu64*)(ws + W_EPOCH), tag, RLX_AGENT);
    }
  }
}

extern "C" void kernel_launch(void* const* d_in, const int* in_sizes, int n_in, void* d_out, int out_size, void* d_ws, size_t ws_size, hipStream_t stream) {
  Args a;
  a.q = (const bf16x8*)d_in[0]; a.k = d_in[1]; a.v = d_in[2]; a.bt = (const int*)d_in[3]; a.lens = (const int*)d_in[4];
  a.out = (unsigned*)d_out; a.ws = (unsigned char*)d_ws; a.kv_bytes = (unsigned)in_sizes[1] * 2u; a.pad = 0;
  hipFuncSetAttribute((const void*)radix_attn, hipFuncAttributeMaxDynamicSharedMemorySize, LDS_BYTES);
  hipLaunchKernelGGL(radix_attn, dim3(256), dim3(NTHR), LDS_BYTES, stream, a);
}
```

```cpp
#define CLS01 0
#include <hip/hip_runtime.h>
#include <hip/hip_bf16.h>
#include <stdint.h>

#define NSLOT 16
#define INFL 20
#define KV_NT 1
#define TLBPF 0
#define OUTSC1 0
#define OUT_STORE(p, v) (*(p) = (v))
#define ABL_NOCOMP 0
#define SLEEPN 1
#define ABL_NOSVC 0
#define ABL_NOSTORE 0
#define ABL_NOGATHER 0
#define ABL_NOPOLL 0
#define POLLSLEEP 32
#define SHCAP 21
#define POLLAT 9
#define STAUX 16
#define LDAUX 16
#define STPACE 0
#define XBIG 0
#define LATEF 12
#define FASTT 49
#define FASTN 2
#define WARM 1
#define DIAG 0
#define MAPSWAP 1

constexpr int B = 16, HQ = 32, HKV = 16, D = 128, PAGE = 16, NB = 256, LK = NB * PAGE;
constexpr int NA = 4;
constexpr int NSW = 4, NTHR = (NSW + 2) * 64;
constexpr float C2 = 0.088388347648318440f * 1.4426950408889634f;
constexpr unsigned PAGE_BYTES = PAGE * HKV * D * 2;
constexpr unsigned TOK_BYTES = HKV * D * 2;

constexpr int ROW = 528;
constexpr int L_RING = 0;
constexpr int L_MRG = NSLOT * 8192;
constexpr int L_TAB = L_RING + (NSLOT - 2) * 8192;
constexpr int L_RECX = L_MRG + 32 * ROW;
constexpr int L_MISC = L_RECX + 4 * 2 * ROW;
constexpr int C_LANDED = 0, C_REL = 4, C_CSTEP = 20, C_RCNT = 24, C_RFREE = 28, C_P = 32, C_DUMMY = 36, C_READY = 40, C_PTR = 48, C_LENS = 80;
constexpr int LDS_BYTES = L_MISC + 160;
static_assert(NSLOT % 4 == 0 && (NSLOT & (NSLOT - 1)) == 0, "ring geometry");
static_assert(LDS_BYTES <= 160 * 1024, "LDS");

constexpr size_t W_EPOCH = 0, W_FLAG = 256, FLAG_STRIDE = 128;
constexpr size_t W_PART = 65536;
constexpr unsigned PS_BYTES = 32 * 512 + 256, PX_BYTES = 2 * 512 + 128;
constexpr unsigned W_PX = 256 * PS_BYTES;
constexpr unsigned PART_BYTES = W_PX + 256 * 16 * PX_BYTES;
#define STAMP(k) do {} while (0)

typedef __bf16 bf16x8 __attribute__((ext_vector_type(8)));
typedef __bf16 bf16x2 __attribute__((ext_vector_type(2)));
typedef short s16x4 __attribute__((ext_vector_type(4)));
typedef float f32x4 __attribute__((ext_vector_type(4)));
typedef float f32x2 __attribute__((ext_vector_type(2)));
typedef unsigned u32x4 __attribute__((ext_vector_type(4)));
typedef unsigned u32x2 __attribute__((ext_vector_type(2)));
typedef int i32x4 __attribute__((ext_vector_type(4)));
#define LAS __attribute__((address_space(3)))
#define GAS __attribute__((address_space(1)))
#define DI __device__ __forceinline__
typedef GAS unsigned long long gu64;
#define RLX_AGENT __ATOMIC_RELAXED, __HIP_MEMORY_SCOPE_AGENT

DI int rfl(int x) { return __builtin_amdgcn_readfirstlane(x); }
DI int rdl(int x, int l) { return __builtin_amdgcn_readlane(x, l); }
template <int CTRL> DI int dpp_i(int oldv, int x) { return __builtin_amdgcn_update_dpp(oldv, x, CTRL, 0xf, 0xf, false); }
DI int row_scan_add(int x) {
  x += dpp_i<0x111>(0, x); x += dpp_i<0x112>(0, x); x += dpp_i<0x114>(0, x); x += dpp_i<0x118>(0, x); return x;
}
DI int row_scan_max(int x) {
  x = max(x, dpp_i<0x111>(0, x)); x = max(x, dpp_i<0x112>(0, x)); x = max(x, dpp_i<0x114>(0, x)); x = max(x, dpp_i<0x118>(0, x)); return x;
}
DI float xrow16_max(float x) {
  auto s = __builtin_amdgcn_permlane16_swap(__float_as_uint(x), __float_as_uint(x), false, false);
  x = fmaxf(__uint_as_float(s[0]), __uint_as_float(s[1]));
  auto t = __builtin_amdgcn_permlane32_swap(__float_as_uint(x), __float_as_uint(x), false, false);
  return fmaxf(__uint_as_float(t[0]), __uint_as_float(t[1]));
}
DI float xrow16_sum(float x) {
  auto s = __builtin_amdgcn_permlane16_swap(__float_as_uint(x), __float_as_uint(x), false, false);
  x = __uint_as_float(s[0]) + __uint_as_float(s[1]);
  auto t = __builtin_amdgcn_permlane32_swap(__float_as_uint(x), __float_as_uint(x), false, false);
  return __uint_as_float(t[0]) + __uint_as_float(t[1]);
}
DI unsigned cvtpk(float lo, float hi) { f32x2 v = {lo, hi}; bf16x2 b = __builtin_convertvector(v, bf16x2); return __builtin_bit_cast(unsigned, b); }
DI float ex2(float x) { return __builtin_amdgcn_exp2f(x); }
DI void wg_barrier() { asm volatile("s_waitcnt lgkmcnt(0)\n\ts_barrier" ::: "memory"); }
template <int N> DI void wait_vm() { asm volatile("s_waitcnt vmcnt(%0)" :: "i"(N) : "memory"); }

#define NTS " nt"
DI void dma4(unsigned m0v, __amdgpu_buffer_rsrc_t rsrc, unsigned soff, unsigned v0, unsigned v1, unsigned v2, unsigned v3) {
  asm volatile("s_mov_b32 m0, %0\n\t"
               "s_nop 4\n\t"
               "buffer_load_dwordx4 %1, %5, %6 offen" NTS " lds\n\t"
               "buffer_load_dwordx4 %2, %5, %6 offen offset:1024" NTS " lds\n\t"
               "buffer_load_dwordx4 %3, %5, %6 offen offset:2048" NTS " lds\n\t"
               "buffer_load_dwordx4 %4, %5, %6 offen offset:3072" NTS " lds"
               :: "s"(m0v), "v"(v0), "v"(v1), "v"(v2), "v"(v3), "s"(rsrc), "s"(soff) : "memory");
}

DI void set_m0(unsigned m0v, unsigned so) { asm volatile("s_mov_b32 m0, %0\n\ts_nop 3" :: "s"(m0v), "s"(so) : "memory"); }
template <int X> DI void wait_dma4(__amdgpu_buffer_rsrc_t rsrc, unsigned soff, unsigned v0, unsigned v1, unsigned v2, unsigned v3) {
  asm volatile("s_waitcnt vmcnt(%6)\n\t"
               "buffer_load_dwordx4 %0, %4, %5 offen" NTS " lds\n\t"
               "buffer_load_dwordx4 %1, %4, %5 offen offset:1024" NTS " lds\n\t"
               "buffer_load_dwordx4 %2, %4, %5 offen offset:2048" NTS " lds\n\t"
               "buffer_load_dwordx4 %3, %4, %5 offen offset:3072" NTS " lds"
               :: "v"(v0), "v"(v1), "v"(v2), "v"(v3), "s"(rsrc), "s"(soff), "i"(X) : "memory");
}

struct Args { const bf16x8* q; const void* k; const void* v; const int* bt; const int* lens; unsigned* out; unsigned char* ws; unsigned kv_bytes; unsigned pad; };

DI float mfac(float m, float M) { return (m == -INFINITY) ? 0.f : ex2((m - M) * C2); }

struct Soft { float m, l; f32x4 o[8]; };
DI void soft_reset(Soft& s) { s.m = -INFINITY; s.l = 0.f;
#pragma unroll
  for (int i = 0; i < 8; ++i) s.o[i] = f32x4{0.f, 0.f, 0.f, 0.f}; }

DI s16x4 soft_step(Soft& s, f32x4 x, bool mask, int tok0, int len) {
  if (mask) {
#pragma unroll
    for (int r = 0; r < 4; ++r) x[r] = (tok0 + r < len) ? x[r] : -INFINITY;
  }
  float pm = fmaxf(fmaxf(x[0], x[1]), fmaxf(x[2], x[3]));
  pm = xrow16_max(pm);
  const float mn = fmaxf(s.m, pm);
  const float ms = (mn == -INFINITY) ? 0.f : mn * C2;
  const float alpha = ex2(s.m * C2 - ms);
  float p[4];
#pragma unroll
  for (int r = 0; r < 4; ++r) p[r] = ex2(fmaf(x[r], C2, -ms));
  s.l = fmaf(s.l, alpha, (p[0] + p[1]) + (p[2] + p[3]));
  s.m = mn;
#pragma unroll
  for (int i = 0; i < 8; ++i) s.o[i] *= alpha;
  u32x2 pk = {cvtpk(p[0], p[1]), cvtpk(p[2], p[3])};
  return __builtin_bit_cast(s16x4, pk);
}

DI int lds_ld(LAS unsigned char* p) { return __hip_atomic_load((LAS int*)p, __ATOMIC_RELAXED, __HIP_MEMORY_SCOPE_WORKGROUP); }
DI void lds_st(LAS unsigned char* p, int v) { __hip_atomic_store((LAS int*)p, v, __ATOMIC_RELAXED, __HIP_MEMORY_SCOPE_WORKGROUP); }
DI int lds_wait_ge(LAS unsigned char* p, int need) {
  int v;
#pragma nounroll
  for (unsigned spins = 0;; ++spins) { v = rfl(lds_ld(p)); if (v >= need || spins > (1u << 24)) break; __builtin_amdgcn_s_sleep(SLEEPN); }
  asm volatile("" ::: "memory");
  return v;
}
DI void lds_drain() { asm volatile("s_waitcnt lgkmcnt(0)" ::: "memory"); }

__global__ void __launch_bounds__(NTHR, 2) radix_attn(Args a_) {
  extern __shared__ __attribute__((aligned(16))) unsigned char lds_raw[];
  LAS unsigned char* lds = (LAS unsigned char*)lds_raw;
  LAS unsigned char* ctl = lds + L_MISC;
  const int tid = threadIdx.x, lane = tid & 63, wave = rfl(tid >> 6);
  const int w = blockIdx.x & 15, h = blockIdx.x >> 4;
  const int c = lane & 15, g = lane >> 4;
  int len_raw;

  struct Part { int len, Pe, S, E, spare, exc, lo, hi, xoff, S_w, spare_w, T_w, ns, ss, hp, nsh, np_w, own_w, vA, TL; unsigned xmask; bool all_masked; };
  auto partition = [&](int P) {
    Part p;
    p.all_masked = len_raw <= 0;
    p.len = p.all_masked ? LK : min(len_raw, LK);
    const int np = (p.len + PAGE - 1) >> 4;
    p.Pe = min(P, rdl(row_scan_max(np), 15));
    const int n = max(np - p.Pe, 0);
    const int W = p.Pe + rdl(row_scan_add(n), 15);
    const int T = (W >> 4) + (c < (W & 15) ? 1 : 0);
    const int cap = max(1, ((W >> 4) * SHCAP) >> 6);
    int lg = 0;
    if (p.Pe > cap) lg = 1;
    if (p.Pe > 2 * cap) lg = 2;
    if (p.Pe > 4 * cap) lg = 3;
    if (p.Pe > 8 * cap) lg = 4;
    const int q = p.Pe >> lg, rq = p.Pe & ((1 << lg) - 1);
    const int sh = c < (1 << lg) ? q + (c < rq ? 1 : 0) : 0;
    p.nsh = min(1 << lg, p.Pe);
    p.ns = w < (1 << lg) ? q + (w < rq ? 1 : 0) : 0;
    p.ss = w * q + min(w, rq);
    const int own = min(n, T - sh);
    p.exc = n - own; p.spare = T - sh - own;
    p.S = row_scan_add(p.spare) - p.spare;
    p.E = row_scan_add(p.exc) - p.exc;
    p.S_w = rdl(p.S, w); p.spare_w = rdl(p.spare, w); p.T_w = rdl(T, w);
    p.hp = p.ns + p.spare_w;
    p.lo = max(p.S_w, p.E); p.hi = min(p.S_w + p.spare_w, p.E + p.exc);
    p.xmask = (unsigned)(__ballot(p.hi > p.lo) & 0xFFFFull);
    p.xoff = p.Pe - p.E;
    p.np_w = rdl(np, w); p.own_w = rdl(own, w);
    p.vA = min(NA, p.own_w);
    p.TL = NA + p.hp + p.own_w - p.vA;
    return p;
  };

  if (__builtin_expect(wave == 0, 1)) {
    __builtin_amdgcn_s_setprio(3);
    typedef unsigned u32x16 __attribute__((ext_vector_type(16)));
    u32x16 ka = *(const __attribute__((address_space(4))) u32x16*)__builtin_amdgcn_kernarg_segment_ptr();
    asm volatile("" : "+s"(ka));
    Args a;
    a.q = (const bf16x8*)(((uintptr_t)ka[1] << 32) | ka[0]); a.k = (const void*)(((uintptr_t)ka[3] << 32) | ka[2]); a.v = (const void*)(((uintptr_t)ka[5] << 32) | ka[4]);
    a.bt = (const int*)(((uintptr_t)ka[7] << 32) | ka[6]); a.lens = (const int*)(((uintptr_t)ka[9] << 32) | ka[8]); a.out = (unsigned*)(((uintptr_t)ka[11] << 32) | ka[10]);
    a.ws = (unsigned char*)(((uintptr_t)ka[13] << 32) | ka[12]); a.kv_bytes = ka[14];
    len_raw = *(const GAS int*)((uintptr_t)a.lens + 4 * c);
    const GAS i32x4* bt4 = (const GAS i32x4*)(uintptr_t)a.bt;
    i32x4 rk16[16];
#pragma unroll
    for (int k = 0; k < 16; ++k) rk16[k] = bt4[k * (NB / 4) + lane];
    STAMP(8);
    asm volatile("s_barrier" ::: "memory");
    if (lane == 0) { *(LAS u32x4*)(ctl + C_PTR) = u32x4{(unsigned)(uintptr_t)a.q, (unsigned)((uintptr_t)a.q >> 32), (unsigned)(uintptr_t)a.out, (unsigned)((uintptr_t)a.out >> 32)};
                     *(LAS u32x2*)(ctl + C_PTR + 16) = u32x2{(unsigned)(uintptr_t)a.ws, (unsigned)((uintptr_t)a.ws >> 32)}; }
    unsigned vk[4], vv[4];
#pragma unroll
    for (int i = 0; i < 4; ++i) { const int t = 4 * i + g;
      vk[i] = t * TOK_BYTES + ((c ^ t) << 4) - 1024 * i;
      vv[i] = t * TOK_BYTES + ((c ^ ((t & 7) << 1)) << 4) - 1024 * i; }
    asm volatile("" : "+v"(vk[0]), "+v"(vk[1]), "+v"(vk[2]), "+v"(vk[3]), "+v"(vv[0]), "+v"(vv[1]), "+v"(vv[2]), "+v"(vv[3]));
    const unsigned ring = (unsigned)(uintptr_t)(lds + L_RING);
    const __amdgpu_buffer_rsrc_t rk = __builtin_amdgcn_make_buffer_rsrc((void*)a.k, 0, a.kv_bytes, 0x00020000);
    const __amdgpu_buffer_rsrc_t rv = __builtin_amdgcn_make_buffer_rsrc((void*)a.v, 0, a.kv_bytes, 0x00020000);
#pragma unroll
    for (int k = 0; k < 16; ++k) *(LAS i32x4*)(lds + L_TAB + (k * NB + 4 * lane) * 4) = rk16[k];
    unsigned vp0, vp1 = 0, vp2 = 0, vp3 = 0;
    {
      const int lw = rdl(len_raw, w);
      const int npw = ((lw <= 0 ? LK : min(lw, LK)) + PAGE - 1) >> 4;
      vp0 = (unsigned)*(LAS int*)(lds + L_TAB + 4 * (w * NB + max(npw - NA + (lane & 3), 0))) * PAGE_BYTES + (unsigned)h * (D * 2);
    }
    unsigned so = (unsigned)rdl((int)vp0, 0), m0k = ring;
    set_m0(m0k, so);
#pragma nounroll
    for (int i = 0; i < 3; ++i) {
      wait_dma4<63>(rk, so, vk[0], vk[1], vk[2], vk[3]);
      set_m0(m0k + 4096, so);
      wait_dma4<63>(rv, so, vv[0], vv[1], vv[2], vv[3]);
      so = (unsigned)rdl((int)vp0, i + 1);
      m0k = ring + (i + 1) * 8192;
      set_m0(m0k, so);
    }
    STAMP(3);
    i32x4 acc = rk16[1] ^ rk16[0];
#pragma unroll
    for (int k = 2; k < 16; ++k) acc |= rk16[k] ^ rk16[0];
    int P;
    {
      const unsigned long long b0 = __ballot(acc[0] != 0), b1 = __ballot(acc[1] != 0), b2 = __ballot(acc[2] != 0), b3 = __ballot(acc[3] != 0);
      const unsigned long long any = b0 | b1 | b2 | b3;
      const int l = any ? (int)__builtin_ctzll(any) : 64;
      const unsigned long long bit = any & (0ull - any);
      P = 4 * l + ((b0 & bit) ? 0 : (b1 & bit) ? 1 : (b2 & bit) ? 2 : (b3 & bit) ? 3 : 0);
    }
    if (lane == 0) *(LAS int*)(ctl + C_P) = P;
    if (lane < 16) *(LAS int*)(ctl + C_LENS + 4 * lane) = len_raw;
    lds_drain();
    lds_st(ctl + C_READY, 1);
    STAMP(2);
    const Part p = partition(P);
    const int S_w = p.S_w - p.ns - NA, hp = p.hp, ns = p.ns, ss = p.ss, T_w = p.TL, lo = p.lo, hi = p.hi, xoff = p.xoff; const unsigned xmask = p.xmask;
    const int lpA = p.np_w - NA, lpB = p.np_w - p.own_w;
    auto page_src = [&](int r) {
      const int i = 64 * r + lane;
      int idx = (i < NA) ? w * NB + max(lpA + i, 0) : (i < NA + ns) ? ss + (i - NA) : w * NB + lpB + (i - NA - hp);
      for (unsigned m = xmask; m; m &= m - 1) {
        const int b = __builtin_ctz(m);
        const int lo_b = rdl(lo, b) - S_w, hi_b = rdl(hi, b) - S_w, off_b = rdl(xoff, b) + b * NB + S_w;
        if (i >= lo_b && i < hi_b) idx = i + off_b;
      }
      return (unsigned)*(LAS int*)(lds + L_TAB + 4 * (i < T_w ? idx : 0)) * PAGE_BYTES + (unsigned)h * (D * 2);
    };
    vp0 = page_src(0); vp1 = page_src(1); vp2 = page_src(2); vp3 = page_src(3);
    asm volatile("" : "+v"(vp0), "+v"(vp1), "+v"(vp2), "+v"(vp3));
    STAMP(7);
    int pub = 0, relnext = 0;
    auto publish = [&](int nl) { if (nl > pub) { pub = nl; lds_st(ctl + C_LANDED, nl); } };
    const bool wide = CLS01 && ((((unsigned)(uintptr_t)a.k >> 8) + h) & 3) == 1;
    const int xc = wide ? (INFL + CLS01 + 3) / 4 : (INFL + 3) / 4;
    auto step = [&](int i) {
      if (wide) wait_dma4<INFL + CLS01>(rk, so, vk[0], vk[1], vk[2], vk[3]); else wait_dma4<INFL>(rk, so, vk[0], vk[1], vk[2], vk[3]);
      publish(2 * i - xc);
      relnext = lds_ld(ctl + C_REL + 4 * ((i + 1) & 3));
      set_m0(m0k + 4096, so);
      if (wide) wait_dma4<INFL + CLS01>(rv, so, vv[0], vv[1], vv[2], vv[3]); else wait_dma4<INFL>(rv, so, vv[0], vv[1], vv[2], vv[3]);
      publish(2 * i + 1 - xc);
      const int in = i + 1;
      if (in >= NSLOT && in < T_w) {
        const int need = (in >> 2) - NSLOT / 4 + 1;
        if (rfl(relnext) < need) {
          LAS unsigned char* rel = ctl + C_REL + 4 * (in & 3);
          if (rfl(lds_ld(rel)) < need) {
            wait_vm<12>(); publish(2 * in - 3);
            if (rfl(lds_ld(rel)) < need) { wait_vm<4>(); publish(2 * in - 1);
              if (rfl(lds_ld(rel)) < need) { wait_vm<0>(); publish(2 * in); lds_wait_ge(rel, need); } }
          }
        }
        asm volatile("" ::: "memory");
      }
      if ((in & 63) == 0) { vp0 = vp1; vp1 = vp2; vp2 = vp3; }
      so = (unsigned)rdl((int)vp0, in & 63);
      m0k = ring + (in & (NSLOT - 1)) * 8192;
      set_m0(m0k, so);
    };
#pragma nounroll
    for (int i = 3; i < T_w; ++i) step(i);
    STAMP(5);
    if (INFL + CLS01 + 4 > 24) { wait_vm<24>(); publish(2 * T_w - 6); }
    if (INFL + CLS01 + 4 > 20) { wait_vm<20>(); publish(2 * T_w - 5); }
    wait_vm<16>(); publish(2 * T_w - 4);
    wait_vm<12>(); publish(2 * T_w - 3);
    wait_vm<8>(); publish(2 * T_w - 2);
    wait_vm<4>(); publish(2 * T_w - 1);
    wait_vm<0>(); publish(2 * T_w);
    STAMP(6);
  } else if (wave <= NSW) {
    const int cw = wave - 1;
    wg_barrier();
    const unsigned ka0 = c * 256 + ((g ^ c) << 4);
    const int tv = 4 * g + (c >> 2);
    const unsigned va0 = 4096 + tv * 256 + ((tv & 7) << 5) + (c & 3) * 8;
    lds_wait_ge(ctl + C_READY, 1);
    STAMP(2);
    bf16x8 qf[2][4];
    {
      const u32x2 pq = *(LAS u32x2*)(ctl + C_PTR);
      const GAS bf16x8* qp = (const GAS bf16x8*)(((uintptr_t)(unsigned)rfl((int)pq[1]) << 32) | (unsigned)rfl((int)pq[0]));
#pragma unroll
      for (int cg = 0; cg < 2; ++cg)
#pragma unroll
        for (int s = 0; s < 4; ++s) { const int r = 16 * cg + c; qf[cg][s] = qp[(((r >> 1) * HQ + 2 * h + (r & 1)) * D + 32 * s + 8 * g) / 8]; }
    }
    len_raw = *(LAS int*)(ctl + C_LENS + 4 * c);
    const Part p = partition(rfl(*(LAS int*)(ctl + C_P)));
    const int len = p.len, S_w = p.S_w - p.ns - NA, hp = p.hp, ss = p.ss, T_w = p.TL, lo = p.lo, hi = p.hi, xoff = p.xoff, ns = p.ns, lpA = p.np_w - NA, lpB = p.np_w - p.own_w, vA = p.vA;
    const int minlen = LK - rdl(row_scan_max(LK - len), 15);
    const int len0 = __shfl(len, c >> 1), len1 = __shfl(len, 8 + (c >> 1));
    {
      const bool z0 = __shfl((int)p.all_masked, c >> 1) != 0, z1 = __shfl((int)p.all_masked, 8 + (c >> 1)) != 0;
#pragma unroll
      for (int s = 0; s < 4; ++s) {
        u32x4 t0 = __builtin_bit_cast(u32x4, qf[0][s]), t1 = __builtin_bit_cast(u32x4, qf[1][s]);
#pragma unroll
        for (int e = 0; e < 4; ++e) { t0[e] = z0 ? 0u : t0[e]; t1[e] = z1 ? 0u : t1[e]; }
        qf[0][s] = __builtin_bit_cast(bf16x8, t0); qf[1][s] = __builtin_bit_cast(bf16x8, t1);
      }
    }
    int j = 0, seen = 0;
    Soft s0, s1;
    auto page = [&](auto SHc, const bf16x8 (&qa)[4], const bf16x8 (&qb)[4], int la, int lb, int t0, bool mask) {
      constexpr bool SH = decltype(SHc)::value;
      const int i = 4 * j + cw;
      LAS unsigned char* sl = lds + L_RING + (i & (NSLOT - 1)) * 8192;
      if (seen < 2 * i + 1) seen = lds_wait_ge(ctl + C_LANDED, 2 * i + 1);
      bf16x8 kf[4];
#pragma unroll
      for (int s = 0; s < 4; ++s) kf[s] = *(LAS bf16x8*)(sl + (ka0 ^ (s << 6)));
      f32x4 xa = {0.f, 0.f, 0.f, 0.f}, xb = {0.f, 0.f, 0.f, 0.f};
#pragma unroll
      for (int s = 0; s < 4; ++s) { xa = __builtin_amdgcn_mfma_f32_16x16x32_bf16(kf[s], qa[s], xa, 0, 0, 0);
        if (SH) xb = __builtin_amdgcn_mfma_f32_16x16x32_bf16(kf[s], qb[s], xb, 0, 0, 0); }
      const s16x4 pa = soft_step(s0, xa, mask, t0 + 4 * g, la);
      s16x4 pb; if (SH) pb = soft_step(s1, xb, mask, t0 + 4 * g, lb);
      if (seen < 2 * i + 2) seen = lds_wait_ge(ctl + C_LANDED, 2 * i + 2);
      s16x4 vf[8];
#pragma unroll
      for (int blk = 0; blk < 8; ++blk) vf[blk] = __builtin_amdgcn_ds_read_tr16_b64_v4i16((LAS s16x4*)(sl + (va0 ^ (blk << 5))));
      ++j;
      asm volatile("s_waitcnt lgkmcnt(0)" : "+v"(vf[0]), "+v"(vf[1]), "+v"(vf[2]), "+v"(vf[3]), "+v"(vf[4]), "+v"(vf[5]), "+v"(vf[6]), "+v"(vf[7]) :: "memory");
      lds_st(ctl + C_REL + 4 * cw, j);
#pragma unroll
      for (int blk = 0; blk < 8; ++blk) { s0.o[blk] = __builtin_amdgcn_mfma_f32_16x16x16bf16_1k(vf[blk], pa, s0.o[blk], 0, 0, 0);
        if (SH) s1.o[blk] = __builtin_amdgcn_mfma_f32_16x16x16bf16_1k(vf[blk], pb, s1.o[blk], 0, 0, 0); }
    };
    unsigned m = p.xmask;
    for (int kx = 0;; ++kx) {
      const bool early = kx == 0, last = !early && m == 0;
      const int b = (early || last) ? w : __builtin_ctz(m);
      const int i0 = early ? 0 : last ? NA + hp : rdl(lo, b) - S_w;
      const int i1 = early ? NA : last ? T_w : rdl(hi, b) - S_w;
      const int lp0 = early ? lpA : last ? lpB : rdl(lo, b) + rdl(xoff, b);
      bf16x8 qa[4];
      if (b < 8) {
#pragma unroll
        for (int s = 0; s < 4; ++s) qa[s] = qf[0][s];
      } else {
#pragma unroll
        for (int s = 0; s < 4; ++s) qa[s] = qf[1][s];
      }
      const int lb = rdl(len, b);
      soft_reset(s0);
      if (last) lds_wait_ge(ctl + C_RFREE, kx);
      if (early && cw < NA - vA) {
        lds_wait_ge(ctl + C_LANDED, 2 * cw + 2); ++j; lds_st(ctl + C_REL + 4 * cw, j);
      }
      for (int i = 4 * j + cw; i < i1; i += 4) { const int t0 = (lp0 + i - i0) * PAGE; page(std::false_type{}, qa, qa, lb, lb, t0, t0 + PAGE > lb); }
      if (kx > 0 && !last) lds_wait_ge(ctl + C_RFREE, kx);
      const float lt = xrow16_sum(s0.l);
      if ((c >> 1) == (b & 7)) {
        LAS unsigned char* r = lds + L_RECX + (cw * 2 + (c & 1)) * ROW;
#pragma unroll
        for (int blk = 0; blk < 8; ++blk) *(LAS f32x4*)(r + blk * 64 + g * 16) = s0.o[blk];
        if (g == 0) *(LAS f32x2*)(r + 512) = f32x2{s0.m, lt};
      }
      lds_drain();
      if (lane == 0) __hip_atomic_fetch_add((LAS int*)(ctl + C_RCNT), 1, __ATOMIC_RELAXED, __HIP_MEMORY_SCOPE_WORKGROUP);
      if (last) break;
      if (early) {
        if (ns > 0) {
          soft_reset(s0); soft_reset(s1);
          for (int i = cw; i < ns; i += 4) { const int t0 = (ss + i) * PAGE; page(std::true_type{}, qf[0], qf[1], len0, len1, t0, t0 + PAGE > minlen); }
          if (cw < ns) {
            s0.l = xrow16_sum(s0.l); s1.l = xrow16_sum(s1.l);
            if (cw > 0) lds_wait_ge(ctl + C_CSTEP, cw);
#pragma unroll
            for (int cg = 0; cg < 2; ++cg) { Soft& s = cg ? s1 : s0;
              LAS unsigned char* r = lds + L_MRG + (16 * cg + c) * ROW;
              if (cw > 0) {
                const f32x2 ml = *(LAS f32x2*)(r + 512);
                const float M = fmaxf(ml[0], s.m), f0 = mfac(ml[0], M), f1 = mfac(s.m, M);
#pragma unroll
                for (int blk = 0; blk < 8; ++blk) s.o[blk] = *(LAS f32x4*)(r + blk * 64 + g * 16) * f0 + s.o[blk] * f1;
                s.l = ml[1] * f0 + s.l * f1; s.m = M;
              }
#pragma unroll
              for (int blk = 0; blk < 8; ++blk) *(LAS f32x4*)(r + blk * 64 + g * 16) = s.o[blk];
              lds_drain();
              if (g == 0) *(LAS f32x2*)(r + 512) = f32x2{s.m, s.l};
            }
            lds_drain();
            lds_st(ctl + C_CSTEP, cw + 1);
          }
        }
      } else m &= m - 1;
    }
  } else {
    if (lane < 12) *(LAS int*)(ctl + 4 * lane) = 0;
    wg_barrier();
    lds_wait_ge(ctl + C_READY, 1);
    STAMP(2);
    const u32x4 pq = *(LAS u32x4*)(ctl + C_PTR);
    const u32x2 pws = *(LAS u32x2*)(ctl + C_PTR + 16);
    GAS u32x2* outp = (GAS u32x2*)(((uintptr_t)(unsigned)rfl((int)pq[3]) << 32) | (unsigned)rfl((int)pq[2]));
    unsigned char* ws = (unsigned char*)(((uintptr_t)(unsigned)rfl((int)pws[1]) << 32) | (unsigned)rfl((int)pws[0]));
    const unsigned long long epoch = __hip_atomic_load((gu64*)(ws + W_EPOCH), RLX_AGENT);
    const __amdgpu_buffer_rsrc_t prsrc = __builtin_amdgcn_make_buffer_rsrc(ws + W_PART, 0, PART_BYTES, 0x00020000);
    len_raw = *(LAS int*)(ctl + C_LENS + 4 * c);
    const Part p = partition(rfl(*(LAS int*)(ctl + C_P)));
    const unsigned long long tag = epoch + 1ull;
    const int ns = p.ns;
    const unsigned blk = w * 16 + h;
    const int r = lane >> 5, ch = lane & 31;
    float M = -INFINITY, L = 0.f; f32x4 o = {0.f, 0.f, 0.f, 0.f};
    unsigned m = p.xmask;
#pragma nounroll
    for (int kx = 0;; ++kx) {
      const bool early = kx == 0, last = !early && m == 0;
      if (kx == 1 && ns > 0 && !ABL_NOSVC && !ABL_NOSTORE) {
        lds_wait_ge(ctl + C_CSTEP, min(ns, NSW));
#pragma unroll 4
        for (int k = 0; k < 16; ++k) {
          const u32x4 v = *(LAS u32x4*)(lds + L_MRG + (2 * k + r) * ROW + ch * 16);
          __builtin_amdgcn_raw_buffer_store_b128(v, prsrc, k * 1024 + lane * 16, blk * PS_BYTES, 16); }
        if (lane < 32) { const u32x2 v = *(LAS u32x2*)(lds + L_MRG + lane * ROW + 512);
          __builtin_amdgcn_raw_buffer_store_b64(v, prsrc, 32 * 512 + lane * 8, blk * PS_BYTES, 16); }
      }
      if (last) {
        asm volatile("s_waitcnt vmcnt(0)" ::: "memory");
        if (lane == 0) __hip_atomic_store((gu64*)(ws + W_FLAG + blk * FLAG_STRIDE), tag, RLX_AGENT);
        STAMP(3);
        const int E_w = rdl(p.E, w), exc_w = rdl(p.exc, w);
        const bool sh_ov = c < p.nsh;
        const bool ex_ov = min(p.S + p.spare, E_w + exc_w) > max(p.S, E_w);
        unsigned cmask = (ABL_NOSVC || ABL_NOGATHER) ? 0u : (unsigned)(__ballot(sh_ov) & 0xFFFFull) | ((unsigned)(__ballot(ex_ov) & 0xFFFFull) << 16);
        if (cmask) lds_wait_ge(ctl + C_LANDED, (p.TL * POLLAT) >> 3);
        {
          const bool need = !ABL_NOSVC && !ABL_NOPOLL && lane < 16 && (sh_ov || ex_ov);
          gu64* f = (gu64*)(ws + W_FLAG + (size_t)(c * 16 + h) * FLAG_STRIDE);
#pragma nounroll
          for (unsigned spins = 0; spins < (1u << 20); ++spins) {
            const bool ok = !need || __hip_atomic_load(f, RLX_AGENT) == tag;
            if (__all(ok)) break;
            __builtin_amdgcn_s_sleep(POLLSLEEP);
          }
          asm volatile("" ::: "memory");
        }
        STAMP(4);
        const unsigned vso = lane < 16 ? (unsigned)(c * 16 + h) * PS_BYTES + (unsigned)(2 * w) * 512 : W_PX + ((unsigned)(c * 16 + h) * 16 + w) * PX_BYTES;
        const unsigned vsm = lane < 16 ? (unsigned)(c * 16 + h) * PS_BYTES + 32 * 512 + (unsigned)(2 * w) * 8 : vso + 1024;
        constexpr int GB = 6;
        while (cmask) {
          u32x4 po[GB]; u32x2 pml[GB]; bool pv[GB];
#pragma unroll
          for (int k = 0; k < GB; ++k) {
            pv[k] = cmask != 0;
            const int id = pv[k] ? __builtin_ctz(cmask) : 0; cmask &= cmask - 1;
            const unsigned so = pv[k] ? (unsigned)rdl((int)vso, id) : PART_BYTES, sm = pv[k] ? (unsigned)rdl((int)vsm, id) : PART_BYTES;
            po[k] = __builtin_amdgcn_raw_buffer_load_b128(prsrc, lane * 16 + so, 0, 16);
            pml[k] = __builtin_amdgcn_raw_buffer_load_b64(prsrc, r * 8 + sm, 0, 16);
          }
#pragma unroll
          for (int k = 0; k < GB; ++k) {
            const float mk = pv[k] ? __uint_as_float(pml[k][0]) : -INFINITY, lk = __uint_as_float(pml[k][1]);
            const float Mn = fmaxf(M, mk), f0 = mfac(M, Mn), f1 = mfac(mk, Mn);
            o = o * f0 + __builtin_bit_cast(f32x4, po[k]) * f1; L = L * f0 + lk * f1; M = Mn;
          }
        }
        if (blk == 0) {
#pragma nounroll
          for (unsigned spins = 0; spins < (1u << 20); ++spins) {
            bool ok = true;
#pragma unroll
            for (int k = 0; k < 4; ++k) ok &= __hip_atomic_load((gu64*)(ws + W_FLAG + (size_t)(k * 64 + lane) * FLAG_STRIDE), RLX_AGENT) == tag;
            if (__all(ok)) break;
            __builtin_amdgcn_s_sleep(8);
          }
          if (lane == 0) __hip_atomic_store((gu64*)(ws + W_EPOCH), tag, RLX_AGENT);
        }
        STAMP(5);
      }
      lds_wait_ge(ctl + C_RCNT, NSW * (kx + 1));
      f32x2 ml[4]; f32x4 ok[4];
#pragma unroll
      for (int k = 0; k < 4; ++k) { ml[k] = *(LAS f32x2*)(lds + L_RECX + (k * 2 + r) * ROW + 512); ok[k] = *(LAS f32x4*)(lds + L_RECX + (k * 2 + r) * ROW + ch * 16); }
      const bool mine = early || last;
      float Mx = mine ? M : -INFINITY;
#pragma unroll
      for (int k = 0; k < 4; ++k) Mx = fmaxf(Mx, ml[k][0]);
      const float fr = mine ? mfac(M, Mx) : 0.f;
      float Lx = L * fr; f32x4 ox = o * fr;
#pragma unroll
      for (int k = 0; k < 4; ++k) { const float f = mfac(ml[k][0], Mx); Lx = fmaf(f, ml[k][1], Lx); ox += ok[k] * f; }
      asm volatile("s_waitcnt lgkmcnt(0)" : "+v"(ox) :: "memory");
      if (last) {
        const float inv = __builtin_amdgcn_rcpf(Lx);
        outp[((w * HQ + 2 * h + r) * D + 4 * ch) >> 2] = u32x2{cvtpk(ox[0] * inv, ox[1] * inv), cvtpk(ox[2] * inv, ox[3] * inv)};
        break;
      }
      lds_st(ctl + C_RFREE, kx + 1);
      if (early) { M = Mx; L = Lx; o = ox; }
      else {
        const unsigned so = W_PX + (blk * 16 + __builtin_ctz(m)) * PX_BYTES;
        __builtin_amdgcn_raw_buffer_store_b128(__builtin_bit_cast(u32x4, ox), prsrc, lane * 16, so, 16);
        const f32x4 mlv = {__builtin_bit_cast(float, rdl(__builtin_bit_cast(int, Mx), 0)), __builtin_bit_cast(float, rdl(__builtin_bit_cast(int, Lx), 0)),
                           __builtin_bit_cast(float, rdl(__builtin_bit_cast(int, Mx), 32)), __builtin_bit_cast(float, rdl(__builtin_bit_cast(int, Lx), 32))};
        if (lane < 8) __builtin_amdgcn_raw_buffer_store_b128(__builtin_bit_cast(u32x4, mlv), prsrc, 1024 + lane * 16, so, 16);
        m &= m - 1;
      }
    }
    STAMP(6);
  }
}

extern "C" void kernel_launch(void* const* d_in, const int* in_sizes, int n_in, void* d_out, int out_size, void* d_ws, size_t ws_size, hipStream_t stream) {
  Args a;
  a.q = (const bf16x8*)d_in[0]; a.k = d_in[1]; a.v = d_in[2]; a.bt = (const int*)d_in[3]; a.lens = (const int*)d_in[4];
  a.out = (unsigned*)d_out; a.ws = (unsigned char*)d_ws; a.kv_bytes = (unsigned)in_sizes[1] * 2u; a.pad = 0;
  hipFuncSetAttribute((const void*)radix_attn, hipFuncAttributeMaxDynamicSharedMemorySize, LDS_BYTES);
  hipLaunchKernelGGL(radix_attn, dim3(256), dim3(NTHR), LDS_BYTES, stream, a);
}
```

```cpp
#define CLS01 0
#include <hip/hip_runtime.h>
#include <hip/hip_bf16.h>
#include <stdint.h>

#define NSLOT 16
#define INFL 20
#define KV_NT 1
#define TLBPF 0
#define OUTSC1 0
#define OUT_STORE(p, v) (*(p) = (v))
#define ABL_NOCOMP 0
#define SLEEPN 1
#define ABL_NOSVC 0
#define ABL_NOSTORE 0
#define ABL_NOGATHER 0
#define ABL_NOPOLL 0
#define POLLSLEEP 32
#define SHCAP 21
#define POLLAT 9
#define STAUX 16
#define LDAUX 16
#define STPACE 0
#define XBIG 0
#define LATEF 12
#define FASTT 49
#define FASTN 2
#define WARM 1
#define DIAG 0
#define MAPSWAP 1

constexpr int B = 16, HQ = 32, HKV = 16, D = 128, PAGE = 16, NB = 256, LK = NB * PAGE;
constexpr int NA = 4;
constexpr int NSW = 4, NTHR = (NSW + 2) * 64;
constexpr float C2 = 0.088388347648318440f * 1.4426950408889634f;
constexpr unsigned PAGE_BYTES = PAGE * HKV * D * 2;
constexpr unsigned TOK_BYTES = HKV * D * 2;

constexpr int ROW = 528;
constexpr int L_RING = 0;
constexpr int L_MRG = NSLOT * 8192;
constexpr int L_TAB = L_RING + (NSLOT - 2) * 8192;
constexpr int L_RECX = L_MRG + 32 * ROW;
constexpr int L_MISC = L_RECX + 4 * 2 * ROW;
constexpr int C_LANDED = 0, C_REL = 4, C_CSTEP = 20, C_RCNT = 24, C_RFREE = 28, C_P = 32, C_DUMMY = 36, C_READY = 40, C_PTR = 48, C_LENS = 80;
constexpr int LDS_BYTES = L_MISC + 160;
static_assert(NSLOT % 4 == 0 && (NSLOT & (NSLOT - 1)) == 0, "ring geometry");
static_assert(LDS_BYTES <= 160 * 1024, "LDS");

constexpr size_t W_EPOCH = 0, W_FLAG = 256, FLAG_STRIDE = 128;
constexpr size_t W_PART = 65536;
constexpr unsigned PS_BYTES = 32 * 512 + 256, PX_BYTES = 2 * 512 + 128;
constexpr unsigned W_PX = 256 * PS_BYTES;
constexpr unsigned PART_BYTES = W_PX + 256 * 16 * PX_BYTES;
#define STAMP(k) do {} while (0)

typedef __bf16 bf16x8 __attribute__((ext_vector_type(8)));
typedef __bf16 bf16x2 __attribute__((ext_vector_type(2)));
typedef short s16x4 __attribute__((ext_vector_type(4)));
typedef float f32x4 __attribute__((ext_vector_type(4)));
typedef float f32x2 __attribute__((ext_vector_type(2)));
typedef unsigned u32x4 __attribute__((ext_vector_type(4)));
typedef unsigned u32x2 __attribute__((ext_vector_type(2)));
typedef int i32x4 __attribute__((ext_vector_type(4)));
#define LAS __attribute__((address_space(3)))
#define GAS __attribute__((address_space(1)))
#define DI __device__ __forceinline__
typedef GAS unsigned long long gu64;
#define RLX_AGENT __ATOMIC_RELAXED, __HIP_MEMORY_SCOPE_AGENT

DI int rfl(int x) { return __builtin_amdgcn_readfirstlane(x); }
DI int rdl(int x, int l) { return __builtin_amdgcn_readlane(x, l); }
template <int CTRL> DI int dpp_i(int oldv, int x) { return __builtin_amdgcn_update_dpp(oldv, x, CTRL, 0xf, 0xf, false); }
DI int row_scan_add(int x) {
  x += dpp_i<0x111>(0, x); x += dpp_i<0x112>(0, x); x += dpp_i<0x114>(0, x); x += dpp_i<0x118>(0, x); return x;
}
DI int row_scan_max(int x) {
  x = max(x, dpp_i<0x111>(0, x)); x = max(x, dpp_i<0x112>(0, x)); x = max(x, dpp_i<0x114>(0, x)); x = max(x, dpp_i<0x118>(0, x)); return x;
}
DI float xrow16_max(float x) {
  auto s = __builtin_amdgcn_permlane16_swap(__float_as_uint(x), __float_as_uint(x), false, false);
  x = fmaxf(__uint_as_float(s[0]), __uint_as_float(s[1]));
  auto t = __builtin_amdgcn_permlane32_swap(__float_as_uint(x), __float_as_uint(x), false, false);
  return fmaxf(__uint_as_float(t[0]), __uint_as_float(t[1]));
}
DI float xrow16_sum(float x) {
  auto s = __builtin_amdgcn_permlane16_swap(__float_as_uint(x), __float_as_uint(x), false, false);
  x = __uint_as_float(s[0]) + __uint_as_float(s[1]);
  auto t = __builtin_amdgcn_permlane32_swap(__float_as_uint(x), __float_as_uint(x), false, false);
  return __uint_as_float(t[0]) + __uint_as_float(t[1]);
}
DI unsigned cvtpk(float lo, float hi) { f32x2 v = {lo, hi}; bf16x2 b = __builtin_convertvector(v, bf16x2); return __builtin_bit_cast(unsigned, b); }
DI float ex2(float x) { return __builtin_amdgcn_exp2f(x); }
DI void wg_barrier() { asm volatile("s_waitcnt lgkmcnt(0)\n\ts_barrier" ::: "memory"); }
template <int N> DI void wait_vm() { asm volatile("s_waitcnt vmcnt(%0)" :: "i"(N) : "memory"); }

#define NTS " nt"
DI void dma4(unsigned m0v, __amdgpu_buffer_rsrc_t rsrc, unsigned soff, unsigned v0, unsigned v1, unsigned v2, unsigned v3) {
  asm volatile("s_mov_b32 m0, %0\n\t"
               "s_nop 4\n\t"
               "buffer_load_dwordx4 %1, %5, %6 offen" NTS " lds\n\t"
               "buffer_load_dwordx4 %2, %5, %6 offen offset:1024" NTS " lds\n\t"
               "buffer_load_dwordx4 %3, %5, %6 offen offset:2048" NTS " lds\n\t"
               "buffer_load_dwordx4 %4, %5, %6 offen offset:3072" NTS " lds"
               :: "s"(m0v), "v"(v0), "v"(v1), "v"(v2), "v"(v3), "s"(rsrc), "s"(soff) : "memory");
}

DI void set_m0(unsigned m0v, unsigned so) { asm volatile("s_mov_b32 m0, %0\n\ts_nop 3" :: "s"(m0v), "s"(so) : "memory"); }
template <int X, bool NT> DI void wait_dma4x(__amdgpu_buffer_rsrc_t rsrc, unsigned soff, unsigned v0, unsigned v1, unsigned v2, unsigned v3) {
  if (NT)
  asm volatile("s_waitcnt vmcnt(%6)\n\t"
               "buffer_load_dwordx4 %0, %4, %5 offen nt lds\n\t"
               "buffer_load_dwordx4 %1, %4, %5 offen offset:1024 nt lds\n\t"
               "buffer_load_dwordx4 %2, %4, %5 offen offset:2048 nt lds\n\t"
               "buffer_load_dwordx4 %3, %4, %5 offen offset:3072 nt lds"
               :: "v"(v0), "v"(v1), "v"(v2), "v"(v3), "s"(rsrc), "s"(soff), "i"(X) : "memory");
  else
  asm volatile("s_waitcnt vmcnt(%6)\n\t"
               "buffer_load_dwordx4 %0, %4, %5 offen lds\n\t"
               "buffer_load_dwordx4 %1, %4, %5 offen offset:1024 lds\n\t"
               "buffer_load_dwordx4 %2, %4, %5 offen offset:2048 lds\n\t"
               "buffer_load_dwordx4 %3, %4, %5 offen offset:3072 lds"
               :: "v"(v0), "v"(v1), "v"(v2), "v"(v3), "s"(rsrc), "s"(soff), "i"(X) : "memory");
}
#define K_NT 1
#define V_NT 1
#define WAIT_K(X, so) wait_dma4x<X, K_NT>(rk, so, vk[0], vk[1], vk[2], vk[3])
#define WAIT_V(X, so) wait_dma4x<X, V_NT>(rv, so, vv[0], vv[1], vv[2], vv[3])

struct Args { const bf16x8* q; const void* k; const void* v; const int* bt; const int* lens; unsigned* out; unsigned char* ws; unsigned kv_bytes; unsigned pad; };

DI float mfac(float m, float M) { return (m == -INFINITY) ? 0.f : ex2((m - M) * C2); }

struct Soft { float m, l; f32x4 o[8]; };
DI void soft_reset(Soft& s) { s.m = -INFINITY; s.l = 0.f;
#pragma unroll
  for (int i = 0; i < 8; ++i) s.o[i] = f32x4{0.f, 0.f, 0.f, 0.f}; }

DI s16x4 soft_step(Soft& s, f32x4 x, bool mask, int tok0, int len) {
  if (mask) {
#pragma unroll
    for (int r = 0; r < 4; ++r) x[r] = (tok0 + r < len) ? x[r] : -INFINITY;
  }
  float pm = fmaxf(fmaxf(x[0], x[1]), fmaxf(x[2], x[3]));
  pm = xrow16_max(pm);
  const float mn = fmaxf(s.m, pm);
  const float ms = (mn == -INFINITY) ? 0.f : mn * C2;
  const float alpha = ex2(s.m * C2 - ms);
  float p[4];
#pragma unroll
  for (int r = 0; r < 4; ++r) p[r] = ex2(fmaf(x[r], C2, -ms));
  s.l = fmaf(s.l, alpha, (p[0] + p[1]) + (p[2] + p[3]));
  s.m = mn;
#pragma unroll
  for (int i = 0; i < 8; ++i) s.o[i] *= alpha;
  u32x2 pk = {cvtpk(p[0], p[1]), cvtpk(p[2], p[3])};
  return __builtin_bit_cast(s16x4, pk);
}

DI int lds_ld(LAS unsigned char* p) { return __hip_atomic_load((LAS int*)p, __ATOMIC_RELAXED, __HIP_MEMORY_SCOPE_WORKGROUP); }
DI void lds_st(LAS unsigned char* p, int v) { __hip_atomic_store((LAS int*)p, v, __ATOMIC_RELAXED, __HIP_MEMORY_SCOPE_WORKGROUP); }
DI int lds_wait_ge(LAS unsigned char* p, int need) {
  int v;
#pragma nounroll
  for (unsigned spins = 0;; ++spins) { v = rfl(lds_ld(p)); if (v >= need || spins > (1u << 24)) break; __builtin_amdgcn_s_sleep(SLEEPN); }
  asm volatile("" ::: "memory");
  return v;
}
DI void lds_drain() { asm volatile("s_waitcnt lgkmcnt(0)" ::: "memory"); }

__global__ void __launch_bounds__(NTHR, 2) radix_attn(Args a_) {
  extern __shared__ __attribute__((aligned(16))) unsigned char lds_raw[];
  LAS unsigned char* lds = (LAS unsigned char*)lds_raw;
  LAS unsigned char* ctl = lds + L_MISC;
  const int tid = threadIdx.x, lane = tid & 63, wave = rfl(tid >> 6);
  const int w = blockIdx.x & 15, h = blockIdx.x >> 4;
  const int c = lane & 15, g = lane >> 4;
  int len_raw;

  struct Part { int len, Pe, S, E, spare, exc, lo, hi, xoff, S_w, spare_w, T_w, ns, ss, hp, nsh, np_w, own_w, vA, TL; unsigned xmask; bool all_masked; };
  auto partition = [&](int P) {
    Part p;
    p.all_masked = len_raw <= 0;
    p.len = p.all_masked ? LK : min(len_raw, LK);
    const int np = (p.len + PAGE - 1) >> 4;
    p.Pe = min(P, rdl(row_scan_max(np), 15));
    const int n = max(np - p.Pe, 0);
    const int W = p.Pe + rdl(row_scan_add(n), 15);
    const int T = (W >> 4) + (c < (W & 15) ? 1 : 0);
    const int cap = max(1, ((W >> 4) * SHCAP) >> 6);
    int lg = 0;
    if (p.Pe > cap) lg = 1;
    if (p.Pe > 2 * cap) lg = 2;
    if (p.Pe > 4 * cap) lg = 3;
    if (p.Pe > 8 * cap) lg = 4;
    const int q = p.Pe >> lg, rq = p.Pe & ((1 << lg) - 1);
    const int sh = c < (1 << lg) ? q + (c < rq ? 1 : 0) : 0;
    p.nsh = min(1 << lg, p.Pe);
    p.ns = w < (1 << lg) ? q + (w < rq ? 1 : 0) : 0;
    p.ss = w * q + min(w, rq);
    const int own = min(n, T - sh);
    p.exc = n - own; p.spare = T - sh - own;
    p.S = row_scan_add(p.spare) - p.spare;
    p.E = row_scan_add(p.exc) - p.exc;
    p.S_w = rdl(p.S, w); p.spare_w = rdl(p.spare, w); p.T_w = rdl(T, w);
    p.hp = p.ns + p.spare_w;
    p.lo = max(p.S_w, p.E); p.hi = min(p.S_w + p.spare_w, p.E + p.exc);
    p.xmask = (unsigned)(__ballot(p.hi > p.lo) & 0xFFFFull);
    p.xoff = p.Pe - p.E;
    p.np_w = rdl(np, w); p.own_w = rdl(own, w);
    p.vA = min(NA, p.own_w);
    p.TL = NA + p.hp + p.own_w - p.vA;
    return p;
  };

  if (__builtin_expect(wave == 0, 1)) {
    __builtin_amdgcn_s_setprio(3);
    typedef unsigned u32x16 __attribute__((ext_vector_type(16)));
    u32x16 ka = *(const __attribute__((address_space(4))) u32x16*)__builtin_amdgcn_kernarg_segment_ptr();
    asm volatile("" : "+s"(ka));
    Args a;
    a.q = (const bf16x8*)(((uintptr_t)ka[1] << 32) | ka[0]); a.k = (const void*)(((uintptr_t)ka[3] << 32) | ka[2]); a.v = (const void*)(((uintptr_t)ka[5] << 32) | ka[4]);
    a.bt = (const int*)(((uintptr_t)ka[7] << 32) | ka[6]); a.lens = (const int*)(((uintptr_t)ka[9] << 32) | ka[8]); a.out = (unsigned*)(((uintptr_t)ka[11] << 32) | ka[10]);
    a.ws = (unsigned char*)(((uintptr_t)ka[13] << 32) | ka[12]); a.kv_bytes = ka[14];
    len_raw = *(const GAS int*)((uintptr_t)a.lens + 4 * c);
    const GAS i32x4* bt4 = (const GAS i32x4*)(uintptr_t)a.bt;
    i32x4 rk16[16];
#pragma unroll
    for (int k = 0; k < 16; ++k) rk16[k] = bt4[k * (NB / 4) + lane];
    STAMP(8);
    asm volatile("s_barrier" ::: "memory");
    if (lane == 0) { *(LAS u32x4*)(ctl + C_PTR) = u32x4{(unsigned)(uintptr_t)a.q, (unsigned)((uintptr_t)a.q >> 32), (unsigned)(uintptr_t)a.out, (unsigned)((uintptr_t)a.out >> 32)};
                     *(LAS u32x2*)(ctl + C_PTR + 16) = u32x2{(unsigned)(uintptr_t)a.ws, (unsigned)((uintptr_t)a.ws >> 32)}; }
    unsigned vk[4], vv[4];
#pragma unroll
    for (int i = 0; i < 4; ++i) { const int t = 4 * i + g;
      vk[i] = t * TOK_BYTES + ((c ^ t) << 4) - 1024 * i;
      vv[i] = t * TOK_BYTES + ((c ^ ((t & 7) << 1)) << 4) - 1024 * i; }
    asm volatile("" : "+v"(vk[0]), "+v"(vk[1]), "+v"(vk[2]), "+v"(vk[3]), "+v"(vv[0]), "+v"(vv[1]), "+v"(vv[2]), "+v"(vv[3]));
    const unsigned ring = (unsigned)(uintptr_t)(lds + L_RING);
    const __amdgpu_buffer_rsrc_t rk = __builtin_amdgcn_make_buffer_rsrc((void*)a.k, 0, a.kv_bytes, 0x00020000);
    const __amdgpu_buffer_rsrc_t rv = __builtin_amdgcn_make_buffer_rsrc((void*)a.v, 0, a.kv_bytes, 0x00020000);
#pragma unroll
    for (int k = 0; k < 16; ++k) *(LAS i32x4*)(lds + L_TAB + (k * NB + 4 * lane) * 4) = rk16[k];
    unsigned vp0, vp1 = 0, vp2 = 0, vp3 = 0;
    {
      const int lw = rdl(len_raw, w);
      const int npw = ((lw <= 0 ? LK : min(lw, LK)) + PAGE - 1) >> 4;
      vp0 = (unsigned)*(LAS int*)(lds + L_TAB + 4 * (w * NB + max(npw - NA + (lane & 3), 0))) * PAGE_BYTES + (unsigned)h * (D * 2);
    }
    unsigned so = (unsigned)rdl((int)vp0, 0), m0k = ring;
    set_m0(m0k, so);
#pragma nounroll
    for (int i = 0; i < 3; ++i) {
      WAIT_K(63, so);
      set_m0(m0k + 4096, so);
      WAIT_V(63, so);
      so = (unsigned)rdl((int)vp0, i + 1);
      m0k = ring + (i + 1) * 8192;
      set_m0(m0k, so);
    }
    STAMP(3);
    i32x4 acc = rk16[1] ^ rk16[0];
#pragma unroll
    for (int k = 2; k < 16; ++k) acc |= rk16[k] ^ rk16[0];
    int P;
    {
      const unsigned long long b0 = __ballot(acc[0] != 0), b1 = __ballot(acc[1] != 0), b2 = __ballot(acc[2] != 0), b3 = __ballot(acc[3] != 0);
      const unsigned long long any = b0 | b1 | b2 | b3;
      const int l = any ? (int)__builtin_ctzll(any) : 64;
      const unsigned long long bit = any & (0ull - any);
      P = 4 * l + ((b0 & bit) ? 0 : (b1 & bit) ? 1 : (b2 & bit) ? 2 : (b3 & bit) ? 3 : 0);
    }
    if (lane == 0) *(LAS int*)(ctl + C_P) = P;
    if (lane < 16) *(LAS int*)(ctl + C_LENS + 4 * lane) = len_raw;
    lds_drain();
    lds_st(ctl + C_READY, 1);
    STAMP(2);
    const Part p = partition(P);
    const int S_w = p.S_w - p.ns - NA, hp = p.hp, ns = p.ns, ss = p.ss, T_w = p.TL, lo = p.lo, hi = p.hi, xoff = p.xoff; const unsigned xmask = p.xmask;
    const int lpA = p.np_w - NA, lpB = p.np_w - p.own_w;
    auto page_src = [&](int r) {
      const int i = 64 * r + lane;
      int idx = (i < NA) ? w * NB + max(lpA + i, 0) : (i < NA + ns) ? ss + (i - NA) : w * NB + lpB + (i - NA - hp);
      for (unsigned m = xmask; m; m &= m - 1) {
        const int b = __builtin_ctz(m);
        const int lo_b = rdl(lo, b) - S_w, hi_b = rdl(hi, b) - S_w, off_b = rdl(xoff, b) + b * NB + S_w;
        if (i >= lo_b && i < hi_b) idx = i + off_b;
      }
      return (unsigned)*(LAS int*)(lds + L_TAB + 4 * (i < T_w ? idx : 0)) * PAGE_BYTES + (unsigned)h * (D * 2);
    };
    vp0 = page_src(0); vp1 = page_src(1); vp2 = page_src(2); vp3 = page_src(3);
    asm volatile("" : "+v"(vp0), "+v"(vp1), "+v"(vp2), "+v"(vp3));
    STAMP(7);
    int pub = 0, relnext = 0;
    auto publish = [&](int nl) { if (nl > pub) { pub = nl; lds_st(ctl + C_LANDED, nl); } };
    const bool wide = CLS01 && ((((unsigned)(uintptr_t)a.k >> 8) + h) & 3) == 1;
    const int xc = wide ? (INFL + CLS01 + 3) / 4 : (INFL + 3) / 4;
    auto step = [&](int i) {
      if (wide) WAIT_K(INFL + CLS01, so); else WAIT_K(INFL, so);
      publish(2 * i - xc);
      relnext = lds_ld(ctl + C_REL + 4 * ((i + 1) & 3));
      set_m0(m0k + 4096, so);
      if (wide) WAIT_V(INFL + CLS01, so); else WAIT_V(INFL, so);
      publish(2 * i + 1 - xc);
      const int in = i + 1;
      if (in >= NSLOT && in < T_w) {
        const int need = (in >> 2) - NSLOT / 4 + 1;
        if (rfl(relnext) < need) {
          LAS unsigned char* rel = ctl + C_REL + 4 * (in & 3);
          if (rfl(lds_ld(rel)) < need) {
            wait_vm<12>(); publish(2 * in - 3);
            if (rfl(lds_ld(rel)) < need) { wait_vm<4>(); publish(2 * in - 1);
              if (rfl(lds_ld(rel)) < need) { wait_vm<0>(); publish(2 * in); lds_wait_ge(rel, need); } }
          }
        }
        asm volatile("" ::: "memory");
      }
      if ((in & 63) == 0) { vp0 = vp1; vp1 = vp2; vp2 = vp3; }
      so = (unsigned)rdl((int)vp0, in & 63);
      m0k = ring + (in & (NSLOT - 1)) * 8192;
      set_m0(m0k, so);
    };
#pragma nounroll
    for (int i = 3; i < T_w; ++i) step(i);
    STAMP(5);
    if (INFL + CLS01 + 4 > 24) { wait_vm<24>(); publish(2 * T_w - 6); }
    if (INFL + CLS01 + 4 > 20) { wait_vm<20>(); publish(2 * T_w - 5); }
    wait_vm<16>(); publish(2 * T_w - 4);
    wait_vm<12>(); publish(2 * T_w - 3);
    wait_vm<8>(); publish(2 * T_w - 2);
    wait_vm<4>(); publish(2 * T_w - 1);
    wait_vm<0>(); publish(2 * T_w);
    STAMP(6);
  } else if (wave <= NSW) {
    const int cw = wave - 1;
    wg_barrier();
    const unsigned ka0 = c * 256 + ((g ^ c) << 4);
    const int tv = 4 * g + (c >> 2);
    const unsigned va0 = 4096 + tv * 256 + ((tv & 7) << 5) + (c & 3) * 8;
    lds_wait_ge(ctl + C_READY, 1);
    STAMP(2);
    bf16x8 qf[2][4];
    {
      const u32x2 pq = *(LAS u32x2*)(ctl + C_PTR);
      const GAS bf16x8* qp = (const GAS bf16x8*)(((uintptr_t)(unsigned)rfl((int)pq[1]) << 32) | (unsigned)rfl((int)pq[0]));
#pragma unroll
      for (int cg = 0; cg < 2; ++cg)
#pragma unroll
        for (int s = 0; s < 4; ++s) { const int r = 16 * cg + c; qf[cg][s] = qp[(((r >> 1) * HQ + 2 * h + (r & 1)) * D + 32 * s + 8 * g) / 8]; }
    }
    len_raw = *(LAS int*)(ctl + C_LENS + 4 * c);
    const Part p = partition(rfl(*(LAS int*)(ctl + C_P)));
    const int len = p.len, S_w = p.S_w - p.ns - NA, hp = p.hp, ss = p.ss, T_w = p.TL, lo = p.lo, hi = p.hi, xoff = p.xoff, ns = p.ns, lpA = p.np_w - NA, lpB = p.np_w - p.own_w, vA = p.vA;
    const int minlen = LK - rdl(row_scan_max(LK - len), 15);
    const int len0 = __shfl(len, c >> 1), len1 = __shfl(len, 8 + (c >> 1));
    {
      const bool z0 = __shfl((int)p.all_masked, c >> 1) != 0, z1 = __shfl((int)p.all_masked, 8 + (c >> 1)) != 0;
#pragma unroll
      for (int s = 0; s < 4; ++s) {
        u32x4 t0 = __builtin_bit_cast(u32x4, qf[0][s]), t1 = __builtin_bit_cast(u32x4, qf[1][s]);
#pragma unroll
        for (int e = 0; e < 4; ++e) { t0[e] = z0 ? 0u : t0[e]; t1[e] = z1 ? 0u : t1[e]; }
        qf[0][s] = __builtin_bit_cast(bf16x8, t0); qf[1][s] = __builtin_bit_cast(bf16x8, t1);
      }
    }
    int j = 0, seen = 0;
    Soft s0, s1;
    auto page = [&](auto SHc, const bf16x8 (&qa)[4], const bf16x8 (&qb)[4], int la, int lb, int t0, bool mask) {
      constexpr bool SH = decltype(SHc)::value;
      const int i = 4 * j + cw;
      LAS unsigned char* sl = lds + L_RING + (i & (NSLOT - 1)) * 8192;
      if (seen < 2 * i + 1) seen = lds_wait_ge(ctl + C_LANDED, 2 * i + 1);
      bf16x8 kf[4];
#pragma unroll
      for (int s = 0; s < 4; ++s) kf[s] = *(LAS bf16x8*)(sl + (ka0 ^ (s << 6)));
      f32x4 xa = {0.f, 0.f, 0.f, 0.f}, xb = {0.f, 0.f, 0.f, 0.f};
#pragma unroll
      for (int s = 0; s < 4; ++s) { xa = __builtin_amdgcn_mfma_f32_16x16x32_bf16(kf[s], qa[s], xa, 0, 0, 0);
        if (SH) xb = __builtin_amdgcn_mfma_f32_16x16x32_bf16(kf[s], qb[s], xb, 0, 0, 0); }
      const s16x4 pa = soft_step(s0, xa, mask, t0 + 4 * g, la);
      s16x4 pb; if (SH) pb = soft_step(s1, xb, mask, t0 + 4 * g, lb);
      if (seen < 2 * i + 2) seen = lds_wait_ge(ctl + C_LANDED, 2 * i + 2);
      s16x4 vf[8];
#pragma unroll
      for (int blk = 0; blk < 8; ++blk) vf[blk] = __builtin_amdgcn_ds_read_tr16_b64_v4i16((LAS s16x4*)(sl + (va0 ^ (blk << 5))));
      ++j;
      asm volatile("s_waitcnt lgkmcnt(0)" : "+v"(vf[0]), "+v"(vf[1]), "+v"(vf[2]), "+v"(vf[3]), "+v"(vf[4]), "+v"(vf[5]), "+v"(vf[6]), "+v"(vf[7]) :: "memory");
      lds_st(ctl + C_REL + 4 * cw, j);
#pragma unroll
      for (int blk = 0; blk < 8; ++blk) { s0.o[blk] = __builtin_amdgcn_mfma_f32_16x16x16bf16_1k(vf[blk], pa, s0.o[blk], 0, 0, 0);
        if (SH) s1.o[blk] = __builtin_amdgcn_mfma_f32_16x16x16bf16_1k(vf[blk], pb, s1.o[blk], 0, 0, 0); }
    };
    unsigned m = p.xmask;
    for (int kx = 0;; ++kx) {
      const bool early = kx == 0, last = !early && m == 0;
      const int b = (early || last) ? w : __builtin_ctz(m);
      const int i0 = early ? 0 : last ? NA + hp : rdl(lo, b) - S_w;
      const int i1 = early ? NA : last ? T_w : rdl(hi, b) - S_w;
      const int lp0 = early ? lpA : last ? lpB : rdl(lo, b) + rdl(xoff, b);
      bf16x8 qa[4];
      if (b < 8) {
#pragma unroll
        for (int s = 0; s < 4; ++s) qa[s] = qf[0][s];
      } else {
#pragma unroll
        for (int s = 0; s < 4; ++s) qa[s] = qf[1][s];
      }
      const int lb = rdl(len, b);
      soft_reset(s0);
      if (last) lds_wait_ge(ctl + C_RFREE, kx);
      if (early && cw < NA - vA) {
        lds_wait_ge(ctl + C_LANDED, 2 * cw + 2); ++j; lds_st(ctl + C_REL + 4 * cw, j);
      }
      for (int i = 4 * j + cw; i < i1; i += 4) { const int t0 = (lp0 + i - i0) * PAGE; page(std::false_type{}, qa, qa, lb, lb, t0, t0 + PAGE > lb); }
      if (kx > 0 && !last) lds_wait_ge(ctl + C_RFREE, kx);
      const float lt = xrow16_sum(s0.l);
      if ((c >> 1) == (b & 7)) {
        LAS unsigned char* r = lds + L_RECX + (cw * 2 + (c & 1)) * ROW;
#pragma unroll
        for (int blk = 0; blk < 8; ++blk) *(LAS f32x4*)(r + blk * 64 + g * 16) = s0.o[blk];
        if (g == 0) *(LAS f32x2*)(r + 512) = f32x2{s0.m, lt};
      }
      lds_drain();
      if (lane == 0) __hip_atomic_fetch_add((LAS int*)(ctl + C_RCNT), 1, __ATOMIC_RELAXED, __HIP_MEMORY_SCOPE_WORKGROUP);
      if (last) break;
      if (early) {
        if (ns > 0) {
          soft_reset(s0); soft_reset(s1);
          for (int i = cw; i < ns; i += 4) { const int t0 = (ss + i) * PAGE; page(std::true_type{}, qf[0], qf[1], len0, len1, t0, t0 + PAGE > minlen); }
          if (cw < ns) {
            s0.l = xrow16_sum(s0.l); s1.l = xrow16_sum(s1.l);
            if (cw > 0) lds_wait_ge(ctl + C_CSTEP, cw);
#pragma unroll
            for (int cg = 0; cg < 2; ++cg) { Soft& s = cg ? s1 : s0;
              LAS unsigned char* r = lds + L_MRG + (16 * cg + c) * ROW;
              if (cw > 0) {
                const f32x2 ml = *(LAS f32x2*)(r + 512);
                const float M = fmaxf(ml[0], s.m), f0 = mfac(ml[0], M), f1 = mfac(s.m, M);
#pragma unroll
                for (int blk = 0; blk < 8; ++blk) s.o[blk] = *(LAS f32x4*)(r + blk * 64 + g * 16) * f0 + s.o[blk] * f1;
                s.l = ml[1] * f0 + s.l * f1; s.m = M;
              }
#pragma unroll
              for (int blk = 0; blk < 8; ++blk) *(LAS f32x4*)(r + blk * 64 + g * 16) = s.o[blk];
              lds_drain();
              if (g == 0) *(LAS f32x2*)(r + 512) = f32x2{s.m, s.l};
            }
            lds_drain();
            lds_st(ctl + C_CSTEP, cw + 1);
          }
        }
      } else m &= m - 1;
    }
  } else {
    if (lane < 12) *(LAS int*)(ctl + 4 * lane) = 0;
    wg_barrier();
    lds_wait_ge(ctl + C_READY, 1);
    STAMP(2);
    const u32x4 pq = *(LAS u32x4*)(ctl + C_PTR);
    const u32x2 pws = *(LAS u32x2*)(ctl + C_PTR + 16);
    GAS u32x2* outp = (GAS u32x2*)(((uintptr_t)(unsigned)rfl((int)pq[3]) << 32) | (unsigned)rfl((int)pq[2]));
    unsigned char* ws = (unsigned char*)(((uintptr_t)(unsigned)rfl((int)pws[1]) << 32) | (unsigned)rfl((int)pws[0]));
    const unsigned long long epoch = __hip_atomic_load((gu64*)(ws + W_EPOCH), RLX_AGENT);
    const __amdgpu_buffer_rsrc_t prsrc = __builtin_amdgcn_make_buffer_rsrc(ws + W_PART, 0, PART_BYTES, 0x00020000);
    len_raw = *(LAS int*)(ctl + C_LENS + 4 * c);
    const Part p = partition(rfl(*(LAS int*)(ctl + C_P)));
    const unsigned long long tag = epoch + 1ull;
    const int ns = p.ns;
    const unsigned blk = w * 16 + h;
    const int r = lane >> 5, ch = lane & 31;
    float M = -INFINITY, L = 0.f; f32x4 o = {0.f, 0.f, 0.f, 0.f};
    unsigned m = p.xmask;
#pragma nounroll
    for (int kx = 0;; ++kx) {
      const bool early = kx == 0, last = !early && m == 0;
      if (kx == 1 && ns > 0 && !ABL_NOSVC && !ABL_NOSTORE) {
        lds_wait_ge(ctl + C_CSTEP, min(ns, NSW));
#pragma unroll 4
        for (int k = 0; k < 16; ++k) {
          const u32x4 v = *(LAS u32x4*)(lds + L_MRG + (2 * k + r) * ROW + ch * 16);
          __builtin_amdgcn_raw_buffer_store_b128(v, prsrc, k * 1024 + lane * 16, blk * PS_BYTES, 16); }
        if (lane < 32) { const u32x2 v = *(LAS u32x2*)(lds + L_MRG + lane * ROW + 512);
          __builtin_amdgcn_raw_buffer_store_b64(v, prsrc, 32 * 512 + lane * 8, blk * PS_BYTES, 16); }
      }
      if (last) {
        asm volatile("s_waitcnt vmcnt(0)" ::: "memory");
        if (lane == 0) __hip_atomic_store((gu64*)(ws + W_FLAG + blk * FLAG_STRIDE), tag, RLX_AGENT);
        STAMP(3);
        const int E_w = rdl(p.E, w), exc_w = rdl(p.exc, w);
        const bool sh_ov = c < p.nsh;
        const bool ex_ov = min(p.S + p.spare, E_w + exc_w) > max(p.S, E_w);
        unsigned cmask = (ABL_NOSVC || ABL_NOGATHER) ? 0u : (unsigned)(__ballot(sh_ov) & 0xFFFFull) | ((unsigned)(__ballot(ex_ov) & 0xFFFFull) << 16);
        if (cmask) lds_wait_ge(ctl + C_LANDED, (p.TL * POLLAT) >> 3);
        {
          const bool need = !ABL_NOSVC && !ABL_NOPOLL && lane < 16 && (sh_ov || ex_ov);
          gu64* f = (gu64*)(ws + W_FLAG + (size_t)(c * 16 + h) * FLAG_STRIDE);
#pragma nounroll
          for (unsigned spins = 0; spins < (1u << 20); ++spins) {
            const bool ok = !need || __hip_atomic_load(f, RLX_AGENT) == tag;
            if (__all(ok)) break;
            __builtin_amdgcn_s_sleep(POLLSLEEP);
          }
          asm volatile("" ::: "memory");
        }
        STAMP(4);
        const unsigned vso = lane < 16 ? (unsigned)(c * 16 + h) * PS_BYTES + (unsigned)(2 * w) * 512 : W_PX + ((unsigned)(c * 16 + h) * 16 + w) * PX_BYTES;
        const unsigned vsm = lane < 16 ? (unsigned)(c * 16 + h) * PS_BYTES + 32 * 512 + (unsigned)(2 * w) * 8 : vso + 1024;
        constexpr int GB = 6;
        while (cmask) {
          u32x4 po[GB]; u32x2 pml[GB]; bool pv[GB];
#pragma unroll
          for (int k = 0; k < GB; ++k) {
            pv[k] = cmask != 0;
            const int id = pv[k] ? __builtin_ctz(cmask) : 0; cmask &= cmask - 1;
            const unsigned so = pv[k] ? (unsigned)rdl((int)vso, id) : PART_BYTES, sm = pv[k] ? (unsigned)rdl((int)vsm, id) : PART_BYTES;
            po[k] = __builtin_amdgcn_raw_buffer_load_b128(prsrc, lane * 16 + so, 0, 16);
            pml[k] = __builtin_amdgcn_raw_buffer_load_b64(prsrc, r * 8 + sm, 0, 16);
          }
#pragma unroll
          for (int k = 0; k < GB; ++k) {
            const float mk = pv[k] ? __uint_as_float(pml[k][0]) : -INFINITY, lk = __uint_as_float(pml[k][1]);
            const float Mn = fmaxf(M, mk), f0 = mfac(M, Mn), f1 = mfac(mk, Mn);
            o = o * f0 + __builtin_bit_cast(f32x4, po[k]) * f1; L = L * f0 + lk * f1; M = Mn;
          }
        }
        if (blk == 0) {
#pragma nounroll
          for (unsigned spins = 0; spins < (1u << 20); ++spins) {
            bool ok = true;
#pragma unroll
            for (int k = 0; k < 4; ++k) ok &= __hip_atomic_load((gu64*)(ws + W_FLAG + (size_t)(k * 64 + lane) * FLAG_STRIDE), RLX_AGENT) == tag;
            if (__all(ok)) break;
            __builtin_amdgcn_s_sleep(8);
          }
          if (lane == 0) __hip_atomic_store((gu64*)(ws + W_EPOCH), tag, RLX_AGENT);
        }
        STAMP(5);
      }
      lds_wait_ge(ctl + C_RCNT, NSW * (kx + 1));
      f32x2 ml[4]; f32x4 ok[4];
#pragma unroll
      for (int k = 0; k < 4; ++k) { ml[k] = *(LAS f32x2*)(lds + L_RECX + (k * 2 + r) * ROW + 512); ok[k] = *(LAS f32x4*)(lds + L_RECX + (k * 2 + r) * ROW + ch * 16); }
      const bool mine = early || last;
      float Mx = mine ? M : -INFINITY;
#pragma unroll
      for (int k = 0; k < 4; ++k) Mx = fmaxf(Mx, ml[k][0]);
      const float fr = mine ? mfac(M, Mx) : 0.f;
      float Lx = L * fr; f32x4 ox = o * fr;
#pragma unroll
      for (int k = 0; k < 4; ++k) { const float f = mfac(ml[k][0], Mx); Lx = fmaf(f, ml[k][1], Lx); ox += ok[k] * f; }
      asm volatile("s_waitcnt lgkmcnt(0)" : "+v"(ox) :: "memory");
      if (last) {
        const float inv = __builtin_amdgcn_rcpf(Lx);
        outp[((w * HQ + 2 * h + r) * D + 4 * ch) >> 2] = u32x2{cvtpk(ox[0] * inv, ox[1] * inv), cvtpk(ox[2] * inv, ox[3] * inv)};
        break;
      }
      lds_st(ctl + C_RFREE, kx + 1);
      if (early) { M = Mx; L = Lx; o = ox; }
      else {
        const unsigned so = W_PX + (blk * 16 + __builtin_ctz(m)) * PX_BYTES;
        __builtin_amdgcn_raw_buffer_store_b128(__builtin_bit_cast(u32x4, ox), prsrc, lane * 16, so, 16);
        const f32x4 mlv = {__builtin_bit_cast(float, rdl(__builtin_bit_cast(int, Mx), 0)), __builtin_bit_cast(float, rdl(__builtin_bit_cast(int, Lx), 0)),
                           __builtin_bit_cast(float, rdl(__builtin_bit_cast(int, Mx), 32)), __builtin_bit_cast(float, rdl(__builtin_bit_cast(int, Lx), 32))};
        if (lane < 8) __builtin_amdgcn_raw_buffer_store_b128(__builtin_bit_cast(u32x4, mlv), prsrc, 1024 + lane * 16, so, 16);
        m &= m - 1;
      }
    }
    STAMP(6);
  }
}

extern "C" void kernel_launch(void* const* d_in, const int* in_sizes, int n_in, void* d_out, int out_size, void* d_ws, size_t ws_size, hipStream_t stream) {
  Args a;
  a.q = (const bf16x8*)d_in[0]; a.k = d_in[1]; a.v = d_in[2]; a.bt = (const int*)d_in[3]; a.lens = (const int*)d_in[4];
  a.out = (unsigned*)d_out; a.ws = (unsigned char*)d_ws; a.kv_bytes = (unsigned)in_sizes[1] * 2u; a.pad = 0;
  hipFuncSetAttribute((const void*)radix_attn, hipFuncAttributeMaxDynamicSharedMemorySize, LDS_BYTES);
  hipLaunchKernelGGL(radix_attn, dim3(256), dim3(NTHR), LDS_BYTES, stream, a);
}
```

```cpp
#define CLS01 4
#define NDEP 0
#include <hip/hip_runtime.h>
#include <hip/hip_bf16.h>
#include <stdint.h>

#define NSLOT 16
#define INFL 20
#define KV_NT 1
#define TLBPF 0
#define OUTSC1 0
#define OUT_STORE(p, v) (*(p) = (v))
#define ABL_NOCOMP 0
#define SLEEPN 1
#define ABL_NOSVC 0
#define ABL_NOSTORE 0
#define ABL_NOGATHER 0
#define ABL_NOPOLL 0
#define POLLSLEEP 32
#define SHCAP 21
#define POLLAT 9
#define STAUX 16
#define LDAUX 16
#define STPACE 0
#define XBIG 0
#define LATEF 12
#define FASTT 49
#define FASTN 2
#define WARM 1
#define DIAG 0
#define MAPSWAP 1

constexpr int B = 16, HQ = 32, HKV = 16, D = 128, PAGE = 16, NB = 256, LK = NB * PAGE;
constexpr int NA = 4;
constexpr int NSW = 4, NTHR = (NSW + 2) * 64;
constexpr float C2 = 0.088388347648318440f * 1.4426950408889634f;
constexpr unsigned PAGE_BYTES = PAGE * HKV * D * 2;
constexpr unsigned TOK_BYTES = HKV * D * 2;

constexpr int ROW = 528;
constexpr int L_RING = 0;
constexpr int L_MRG = NSLOT * 8192;
constexpr int L_TAB = L_RING + (NSLOT - 2) * 8192;
constexpr int L_RECX = L_MRG + 32 * ROW;
constexpr int L_MISC = L_RECX + 4 * 2 * ROW;
constexpr int C_LANDED = 0, C_CSTEP = 4, C_RCNT = 8, C_RFREE = 12, C_REL = 16, C_P = 32, C_DUMMY = 36, C_READY = 40, C_PTR = 48, C_LENS = 80;
constexpr int LDS_BYTES = L_MISC + 160;
static_assert(NSLOT % 4 == 0 && (NSLOT & (NSLOT - 1)) == 0, "ring geometry");
static_assert(LDS_BYTES <= 160 * 1024, "LDS");

constexpr size_t W_EPOCH = 0, W_FLAG = 256, FLAG_STRIDE = 128;
constexpr size_t W_PART = 65536;
constexpr unsigned PS_BYTES = 32 * 512 + 256, PX_BYTES = 2 * 512 + 128;
constexpr unsigned W_PX = 256 * PS_BYTES;
constexpr unsigned PART_BYTES = W_PX + 256 * 16 * PX_BYTES;
#define STAMP(k) do {} while (0)

typedef __bf16 bf16x8 __attribute__((ext_vector_type(8)));
typedef __bf16 bf16x2 __attribute__((ext_vector_type(2)));
typedef short s16x4 __attribute__((ext_vector_type(4)));
typedef float f32x4 __attribute__((ext_vector_type(4)));
typedef float f32x2 __attribute__((ext_vector_type(2)));
typedef unsigned u32x4 __attribute__((ext_vector_type(4)));
typedef unsigned u32x2 __attribute__((ext_vector_type(2)));
typedef int i32x4 __attribute__((ext_vector_type(4)));
#define LAS __attribute__((address_space(3)))
#define GAS __attribute__((address_space(1)))
#define DI __device__ __forceinline__
typedef GAS unsigned long long gu64;
#define RLX_AGENT __ATOMIC_RELAXED, __HIP_MEMORY_SCOPE_AGENT

DI int rfl(int x) { return __builtin_amdgcn_readfirstlane(x); }
DI int rdl(int x, int l) { return __builtin_amdgcn_readlane(x, l); }
template <int CTRL> DI int dpp_i(int oldv, int x) { return __builtin_amdgcn_update_dpp(oldv, x, CTRL, 0xf, 0xf, false); }
DI int row_scan_add(int x) {
  x += dpp_i<0x111>(0, x); x += dpp_i<0x112>(0, x); x += dpp_i<0x114>(0, x); x += dpp_i<0x118>(0, x); return x;
}
DI int row_scan_max(int x) {
  x = max(x, dpp_i<0x111>(0, x)); x = max(x, dpp_i<0x112>(0, x)); x = max(x, dpp_i<0x114>(0, x)); x = max(x, dpp_i<0x118>(0, x)); return x;
}
DI float xrow16_max(float x) {
  auto s = __builtin_amdgcn_permlane16_swap(__float_as_uint(x), __float_as_uint(x), false, false);
  x = fmaxf(__uint_as_float(s[0]), __uint_as_float(s[1]));
  auto t = __builtin_amdgcn_permlane32_swap(__float_as_uint(x), __float_as_uint(x), false, false);
  return fmaxf(__uint_as_float(t[0]), __uint_as_float(t[1]));
}
DI float xrow16_sum(float x) {
  auto s = __builtin_amdgcn_permlane16_swap(__float_as_uint(x), __float_as_uint(x), false, false);
  x = __uint_as_float(s[0]) + __uint_as_float(s[1]);
  auto t = __builtin_amdgcn_permlane32_swap(__float_as_uint(x), __float_as_uint(x), false, false);
  return __uint_as_float(t[0]) + __uint_as_float(t[1]);
}
DI unsigned cvtpk(float lo, float hi) { f32x2 v = {lo, hi}; bf16x2 b = __builtin_convertvector(v, bf16x2); return __builtin_bit_cast(unsigned, b); }
DI float ex2(float x) { return __builtin_amdgcn_exp2f(x); }
DI void wg_barrier() { asm volatile("s_waitcnt lgkmcnt(0)\n\ts_barrier" ::: "memory"); }
template <int N> DI void wait_vm() { asm volatile("s_waitcnt vmcnt(%0)" :: "i"(N) : "memory"); }

#define NTS " nt"
DI void dma4(unsigned m0v, __amdgpu_buffer_rsrc_t rsrc, unsigned soff, unsigned v0, unsigned v1, unsigned v2, unsigned v3) {
  asm volatile("s_mov_b32 m0, %0\n\t"
               "s_nop 4\n\t"
               "buffer_load_dwordx4 %1, %5, %6 offen" NTS " lds\n\t"
               "buffer_load_dwordx4 %2, %5, %6 offen offset:1024" NTS " lds\n\t"
               "buffer_load_dwordx4 %3, %5, %6 offen offset:2048" NTS " lds\n\t"
               "buffer_load_dwordx4 %4, %5, %6 offen offset:3072" NTS " lds"
               :: "s"(m0v), "v"(v0), "v"(v1), "v"(v2), "v"(v3), "s"(rsrc), "s"(soff) : "memory");
}

DI void set_m0(unsigned m0v, unsigned so) { asm volatile("s_mov_b32 m0, %0\n\ts_nop 3" :: "s"(m0v), "s"(so) : "memory"); }
template <int X, bool NT> DI void wait_dma4x(__amdgpu_buffer_rsrc_t rsrc, unsigned soff, unsigned v0, unsigned v1, unsigned v2, unsigned v3) {
  if (NT)
  asm volatile("s_waitcnt vmcnt(%6)\n\t"
               "buffer_load_dwordx4 %0, %4, %5 offen nt lds\n\t"
               "buffer_load_dwordx4 %1, %4, %5 offen offset:1024 nt lds\n\t"
               "buffer_load_dwordx4 %2, %4, %5 offen offset:2048 nt lds\n\t"
               "buffer_load_dwordx4 %3, %4, %5 offen offset:3072 nt lds"
               :: "v"(v0), "v"(v1), "v"(v2), "v"(v3), "s"(rsrc), "s"(soff), "i"(X) : "memory");
  else
  asm volatile("s_waitcnt vmcnt(%6)\n\t"
               "buffer_load_dwordx4 %0, %4, %5 offen lds\n\t"
               "buffer_load_dwordx4 %1, %4, %5 offen offset:1024 lds\n\t"
               "buffer_load_dwordx4 %2, %4, %5 offen offset:2048 lds\n\t"
               "buffer_load_dwordx4 %3, %4, %5 offen offset:3072 lds"
               :: "v"(v0), "v"(v1), "v"(v2), "v"(v3), "s"(rsrc), "s"(soff), "i"(X) : "memory");
}
template <bool NT> DI void dma4r(__amdgpu_buffer_rsrc_t r0, __amdgpu_buffer_rsrc_t r1, __amdgpu_buffer_rsrc_t r2, __amdgpu_buffer_rsrc_t r3, unsigned soff, unsigned v0, unsigned v1, unsigned v2, unsigned v3) {
  if (NT)
  asm volatile("s_nop 0\n\t"
               "buffer_load_dwordx4 %0, %4, %8 offen nt lds\n\t"
               "buffer_load_dwordx4 %1, %5, %8 offen offset:1024 nt lds\n\t"
               "buffer_load_dwordx4 %2, %6, %8 offen offset:2048 nt lds\n\t"
               "buffer_load_dwordx4 %3, %7, %8 offen offset:3072 nt lds"
               :: "v"(v0), "v"(v1), "v"(v2), "v"(v3), "s"(r0), "s"(r1), "s"(r2), "s"(r3), "s"(soff) : "memory");
  else
  asm volatile("s_nop 0\n\t"
               "buffer_load_dwordx4 %0, %4, %8 offen lds\n\t"
               "buffer_load_dwordx4 %1, %5, %8 offen offset:1024 lds\n\t"
               "buffer_load_dwordx4 %2, %6, %8 offen offset:2048 lds\n\t"
               "buffer_load_dwordx4 %3, %7, %8 offen offset:3072 lds"
               :: "v"(v0), "v"(v1), "v"(v2), "v"(v3), "s"(r0), "s"(r1), "s"(r2), "s"(r3), "s"(soff) : "memory");
}
#define K_NT 1
#define V_NT 1
#define WAIT_K(X, so) wait_dma4x<X, K_NT>(rk, so, vk[0], vk[1], vk[2], vk[3])
#define WAIT_V(X, so) wait_dma4x<X, V_NT>(rv, so, vv[0], vv[1], vv[2], vv[3])

template <int X, bool KNT, bool VNT>
DI void page_asm(__amdgpu_buffer_rsrc_t rk, __amdgpu_buffer_rsrc_t rv, const unsigned (&vk)[4], const unsigned (&vv)[4], unsigned vctl, unsigned vp,
                 int& i, int& pub, unsigned& so, unsigned& mk) {
  unsigned vt;
#define PAGE_ASM(KS, VS) \
  asm volatile("s_waitcnt vmcnt(%[X])\n\t" \
               "buffer_load_dwordx4 %[k0], %[rk], %[so] offen" KS " lds\n\t" \
               "buffer_load_dwordx4 %[k1], %[rk], %[so] offen offset:1024" KS " lds\n\t" \
               "buffer_load_dwordx4 %[k2], %[rk], %[so] offen offset:2048" KS " lds\n\t" \
               "buffer_load_dwordx4 %[k3], %[rk], %[so] offen offset:3072" KS " lds\n\t" \
               "v_mov_b32 %[vt], %[pub]\n\t" \
               "s_add_u32 m0, m0, 0x1000\n\t" \
               "ds_write_b32 %[vctl], %[vt]\n\t" \
               "s_add_u32 %[pub], %[pub], 1\n\t" \
               "s_waitcnt vmcnt(%[X])\n\t" \
               "buffer_load_dwordx4 %[v0], %[rv], %[so] offen" VS " lds\n\t" \
               "buffer_load_dwordx4 %[v1], %[rv], %[so] offen offset:1024" VS " lds\n\t" \
               "buffer_load_dwordx4 %[v2], %[rv], %[so] offen offset:2048" VS " lds\n\t" \
               "buffer_load_dwordx4 %[v3], %[rv], %[so] offen offset:3072" VS " lds\n\t" \
               "v_mov_b32 %[vt], %[pub]\n\t" \
               "s_add_u32 %[i], %[i], 1\n\t" \
               "ds_write_b32 %[vctl], %[vt]\n\t" \
               "v_readlane_b32 %[so], %[vp], %[i]\n\t" \
               "s_add_u32 %[pub], %[pub], 1\n\t" \
               "s_add_u32 %[mk], %[mk], 0x2000\n\t" \
               "s_and_b32 %[mk], %[mk], %[msk]\n\t" \
               "s_mov_b32 m0, %[mk]" \
               : [i] "+s"(i), [pub] "+s"(pub), [so] "+s"(so), [mk] "+s"(mk), [vt] "=&v"(vt) \
               : [X] "i"(X), [msk] "i"(NSLOT * 8192 - 1), [k0] "v"(vk[0]), [k1] "v"(vk[1]), [k2] "v"(vk[2]), [k3] "v"(vk[3]), [v0] "v"(vv[0]), [v1] "v"(vv[1]), [v2] "v"(vv[2]), [v3] "v"(vv[3]), \
                 [rk] "s"(rk), [rv] "s"(rv), [vctl] "v"(vctl), [vp] "v"(vp) : "memory", "scc")
  if (KNT && VNT) PAGE_ASM(" nt", " nt"); else if (KNT) PAGE_ASM(" nt", ""); else if (VNT) PAGE_ASM("", " nt"); else PAGE_ASM("", "");
#undef PAGE_ASM
}

struct ArgsPad { unsigned z[16]; };
struct Args { const bf16x8* q; const void* k; const void* v; const int* bt; const int* lens; unsigned* out; unsigned char* ws; unsigned kv_bytes; unsigned pad; };

DI float mfac(float m, float M) { return (m == -INFINITY) ? 0.f : ex2((m - M) * C2); }

struct Soft { float m, l; f32x4 o[8]; };
DI void soft_reset(Soft& s) { s.m = -INFINITY; s.l = 0.f;
#pragma unroll
  for (int i = 0; i < 8; ++i) s.o[i] = f32x4{0.f, 0.f, 0.f, 0.f}; }

DI s16x4 soft_step(Soft& s, f32x4 x, bool mask, int tok0, int len) {
  if (mask) {
#pragma unroll
    for (int r = 0; r < 4; ++r) x[r] = (tok0 + r < len) ? x[r] : -INFINITY;
  }
  float pm = fmaxf(fmaxf(x[0], x[1]), fmaxf(x[2], x[3]));
  pm = xrow16_max(pm);
  const float mn = fmaxf(s.m, pm);
  const float ms = (mn == -INFINITY) ? 0.f : mn * C2;
  const float alpha = ex2(s.m * C2 - ms);
  float p[4];
#pragma unroll
  for (int r = 0; r < 4; ++r) p[r] = ex2(fmaf(x[r], C2, -ms));
  s.l = fmaf(s.l, alpha, (p[0] + p[1]) + (p[2] + p[3]));
  s.m = mn;
#pragma unroll
  for (int i = 0; i < 8; ++i) s.o[i] *= alpha;
  u32x2 pk = {cvtpk(p[0], p[1]), cvtpk(p[2], p[3])};
  return __builtin_bit_cast(s16x4, pk);
}

DI int lds_ld(LAS unsigned char* p) { return __hip_atomic_load((LAS int*)p, __ATOMIC_RELAXED, __HIP_MEMORY_SCOPE_WORKGROUP); }
DI void lds_st(LAS unsigned char* p, int v) { __hip_atomic_store((LAS int*)p, v, __ATOMIC_RELAXED, __HIP_MEMORY_SCOPE_WORKGROUP); }
DI int lds_wait_ge(LAS unsigned char* p, int need) {
  int v;
#pragma nounroll
  for (unsigned spins = 0;; ++spins) { v = rfl(lds_ld(p)); if (v >= need || spins > (1u << 24)) break; __builtin_amdgcn_s_sleep(SLEEPN); }
  asm volatile("" ::: "memory");
  return v;
}
DI void lds_drain() { asm volatile("s_waitcnt lgkmcnt(0)" ::: "memory"); }

__global__ void __launch_bounds__(NTHR, 2) radix_attn(Args a_, ArgsPad pad_) {
  extern __shared__ __attribute__((aligned(16))) unsigned char lds_raw[];
  LAS unsigned char* lds = (LAS unsigned char*)lds_raw;
  LAS unsigned char* ctl = lds + L_MISC;
  const int tid = threadIdx.x, lane = tid & 63, wave = rfl(tid >> 6);
  const int w = blockIdx.x & 15, h = blockIdx.x >> 4;
  const int c = lane & 15, g = lane >> 4;
  int len_raw;

  struct Part { int len, Pe, S, E, spare, exc, lo, hi, xoff, S_w, spare_w, T_w, ns, ss, hp, nsh, np_w, own_w, vA, TL; unsigned xmask; bool all_masked; };
  auto partition = [&](int P) {
    Part p;
    p.all_masked = len_raw <= 0;
    p.len = p.all_masked ? LK : min(len_raw, LK);
    const int np = (p.len + PAGE - 1) >> 4;
    p.Pe = min(P, rdl(row_scan_max(np), 15));
    const int n = max(np - p.Pe, 0);
    const int W = p.Pe + rdl(row_scan_add(n), 15);
    const int T = (W >> 4) + (c < (W & 15) ? 1 : 0);
    const int cap = max(1, ((W >> 4) * SHCAP) >> 6);
    int lg = 0;
    if (p.Pe > cap) lg = 1;
    if (p.Pe > 2 * cap) lg = 2;
    if (p.Pe > 4 * cap) lg = 3;
    if (p.Pe > 8 * cap) lg = 4;
    const int q = p.Pe >> lg, rq = p.Pe & ((1 << lg) - 1);
    const int sh = c < (1 << lg) ? q + (c < rq ? 1 : 0) : 0;
    p.nsh = min(1 << lg, p.Pe);
    p.ns = w < (1 << lg) ? q + (w < rq ? 1 : 0) : 0;
    p.ss = w * q + min(w, rq);
    const int own = min(n, T - sh);
    p.exc = n - own; p.spare = T - sh - own;
    p.S = row_scan_add(p.spare) - p.spare;
    p.E = row_scan_add(p.exc) - p.exc;
    p.S_w = rdl(p.S, w); p.spare_w = rdl(p.spare, w); p.T_w = rdl(T, w);
    p.hp = p.ns + p.spare_w;
    p.lo = max(p.S_w, p.E); p.hi = min(p.S_w + p.spare_w, p.E + p.exc);
    p.xmask = (unsigned)(__ballot(p.hi > p.lo) & 0xFFFFull);
    p.xoff = p.Pe - p.E;
    p.np_w = rdl(np, w); p.own_w = rdl(own, w);
    p.vA = min(NA, p.own_w);
    p.TL = NA + p.hp + p.own_w - p.vA;
    return p;
  };

  if (__builtin_expect(wave == 0, 1)) {
    __builtin_amdgcn_s_setprio(3);
    typedef unsigned u32x16 __attribute__((ext_vector_type(16)));
    u32x16 ka = *(const __attribute__((address_space(4))) u32x16*)__builtin_amdgcn_kernarg_segment_ptr();
    asm volatile("" : "+s"(ka));
    {
      unsigned off = 64;
#pragma unroll
      for (int t = 0; t < NDEP; ++t) {
        unsigned z = *(const __attribute__((address_space(4))) unsigned*)((const __attribute__((address_space(4))) char*)__builtin_amdgcn_kernarg_segment_ptr() + off + (ka[15] & 0));
        asm volatile("" : "+s"(z));
        ka[14] += z;
      }
    }
    Args a;
    a.q = (const bf16x8*)(((uintptr_t)ka[1] << 32) | ka[0]); a.k = (const void*)(((uintptr_t)ka[3] << 32) | ka[2]); a.v = (const void*)(((uintptr_t)ka[5] << 32) | ka[4]);
    a.bt = (const int*)(((uintptr_t)ka[7] << 32) | ka[6]); a.lens = (const int*)(((uintptr_t)ka[9] << 32) | ka[8]); a.out = (unsigned*)(((uintptr_t)ka[11] << 32) | ka[10]);
    a.ws = (unsigned char*)(((uintptr_t)ka[13] << 32) | ka[12]); a.kv_bytes = ka[14];
    len_raw = *(const GAS int*)((uintptr_t)a.lens + 4 * c);
    const GAS i32x4* bt4 = (const GAS i32x4*)(uintptr_t)a.bt;
    i32x4 rk16[16];
#pragma unroll
    for (int k = 0; k < 16; ++k) rk16[k] = bt4[k * (NB / 4) + lane];
    STAMP(8);
    asm volatile("s_barrier" ::: "memory");
    if (lane == 0) { *(LAS u32x4*)(ctl + C_PTR) = u32x4{(unsigned)(uintptr_t)a.q, (unsigned)((uintptr_t)a.q >> 32), (unsigned)(uintptr_t)a.out, (unsigned)((uintptr_t)a.out >> 32)};
                     *(LAS u32x2*)(ctl + C_PTR + 16) = u32x2{(unsigned)(uintptr_t)a.ws, (unsigned)((uintptr_t)a.ws >> 32)}; }
    unsigned vk[4], vv[4];
#pragma unroll
    for (int i = 0; i < 4; ++i) { const int t = 4 * i + g;
      vk[i] = t * TOK_BYTES + ((c ^ t) << 4) - 1024 * i;
      vv[i] = t * TOK_BYTES + ((c ^ ((t & 7) << 1)) << 4) - 1024 * i; }
    asm volatile("" : "+v"(vk[0]), "+v"(vk[1]), "+v"(vk[2]), "+v"(vk[3]), "+v"(vv[0]), "+v"(vv[1]), "+v"(vv[2]), "+v"(vv[3]));
    const unsigned ring = (unsigned)(uintptr_t)(lds + L_RING);
    const __amdgpu_buffer_rsrc_t rk = __builtin_amdgcn_make_buffer_rsrc((void*)a.k, 0, a.kv_bytes, 0x00020000);
    const __amdgpu_buffer_rsrc_t rv = __builtin_amdgcn_make_buffer_rsrc((void*)a.v, 0, a.kv_bytes, 0x00020000);
#pragma unroll
    for (int k = 0; k < 16; ++k) *(LAS i32x4*)(lds + L_TAB + (k * NB + 4 * lane) * 4) = rk16[k];
    unsigned vp0, vp1 = 0, vp2 = 0, vp3 = 0;
    int nq0;
    {
      const int lw = rdl(len_raw, w);
      const int npw = ((lw <= 0 ? LK : min(lw, LK)) + PAGE - 1) >> 4;
      vp0 = (unsigned)*(LAS int*)(lds + L_TAB + 4 * (w * NB + max(npw - 1 - (lane & 3), 0))) * PAGE_BYTES + (unsigned)h * (D * 2);
      nq0 = ((lw <= 0 ? LK : min(lw, LK)) - (npw - 1) * PAGE + 3) >> 2;
    }
    unsigned so = (unsigned)rdl((int)vp0, 0), m0k = ring;
    {
      const __amdgpu_buffer_rsrc_t k1 = __builtin_amdgcn_make_buffer_rsrc((void*)a.k, 0, nq0 > 1 ? a.kv_bytes : 0u, 0x00020000), k2 = __builtin_amdgcn_make_buffer_rsrc((void*)a.k, 0, nq0 > 2 ? a.kv_bytes : 0u, 0x00020000),
                                   k3 = __builtin_amdgcn_make_buffer_rsrc((void*)a.k, 0, nq0 > 3 ? a.kv_bytes : 0u, 0x00020000);
      set_m0(m0k, so);
      dma4r<K_NT>(rk, k1, k2, k3, so, vk[0], vk[1], vk[2], vk[3]);
      const __amdgpu_buffer_rsrc_t v1 = __builtin_amdgcn_make_buffer_rsrc((void*)a.v, 0, nq0 > 1 ? a.kv_bytes : 0u, 0x00020000), v2 = __builtin_amdgcn_make_buffer_rsrc((void*)a.v, 0, nq0 > 2 ? a.kv_bytes : 0u, 0x00020000),
                                   v3 = __builtin_amdgcn_make_buffer_rsrc((void*)a.v, 0, nq0 > 3 ? a.kv_bytes : 0u, 0x00020000);
      set_m0(m0k + 4096, so);
      dma4r<V_NT>(rv, v1, v2, v3, so, vv[0], vv[1], vv[2], vv[3]);
      so = (unsigned)rdl((int)vp0, 1); m0k = ring + 8192;
      set_m0(m0k, so);
    }
#pragma nounroll
    for (int i = 1; i < 3; ++i) {
      WAIT_K(63, so);
      set_m0(m0k + 4096, so);
      WAIT_V(63, so);
      so = (unsigned)rdl((int)vp0, i + 1);
      m0k = ring + (i + 1) * 8192;
      set_m0(m0k, so);
    }
    STAMP(3);
    i32x4 acc = rk16[1] ^ rk16[0];
#pragma unroll
    for (int k = 2; k < 16; ++k) acc |= rk16[k] ^ rk16[0];
    int P;
    {
      const unsigned long long b0 = __ballot(acc[0] != 0), b1 = __ballot(acc[1] != 0), b2 = __ballot(acc[2] != 0), b3 = __ballot(acc[3] != 0);
      const unsigned long long any = b0 | b1 | b2 | b3;
      const int l = any ? (int)__builtin_ctzll(any) : 64;
      const unsigned long long bit = any & (0ull - any);
      P = 4 * l + ((b0 & bit) ? 0 : (b1 & bit) ? 1 : (b2 & bit) ? 2 : (b3 & bit) ? 3 : 0);
    }
    if (lane == 0) *(LAS int*)(ctl + C_P) = P;
    if (lane < 16) *(LAS int*)(ctl + C_LENS + 4 * lane) = len_raw;
    lds_drain();
    lds_st(ctl + C_READY, 1);
    STAMP(2);
    const Part p = partition(P);
    const int S_w = p.S_w - p.ns - NA, hp = p.hp, ns = p.ns, ss = p.ss, T_w = p.TL, lo = p.lo, hi = p.hi, xoff = p.xoff; const unsigned xmask = p.xmask;
    const int lpA = p.np_w - 1, lpB = p.np_w - p.own_w;
    auto page_src = [&](int r) {
      const int i = 64 * r + lane;
      int idx = (i < NA) ? w * NB + max(lpA - i, 0) : (i < NA + ns) ? ss + (i - NA) : w * NB + lpB + (i - NA - hp);
      for (unsigned m = xmask; m; m &= m - 1) {
        const int b = __builtin_ctz(m);
        const int lo_b = rdl(lo, b) - S_w, hi_b = rdl(hi, b) - S_w, off_b = rdl(xoff, b) + b * NB + S_w;
        if (i >= lo_b && i < hi_b) idx = i + off_b;
      }
      return (unsigned)*(LAS int*)(lds + L_TAB + 4 * (i < T_w ? idx : 0)) * PAGE_BYTES + (unsigned)h * (D * 2);
    };
    vp0 = page_src(0); vp1 = page_src(1); vp2 = page_src(2); vp3 = page_src(3);
    asm volatile("" : "+v"(vp0), "+v"(vp1), "+v"(vp2), "+v"(vp3));
    STAMP(7);
    static_assert(L_RING == 0 && C_LANDED == 0 && C_REL == 16, "page_asm addresses the ring from LDS offset 0 and the control words by these offsets");
    const unsigned vctl = (unsigned)(uintptr_t)ctl;
    auto publish = [&](int nl) { lds_st(ctl + C_LANDED, nl); };
    const bool wide = CLS01 && ((((unsigned)(uintptr_t)a.k >> 8) + h) & 3) == 1;
    const int TM = T_w >= 8 ? T_w - 4 : T_w;
    int i = 3, credit = NSLOT;
    unsigned mk = m0k;
    for (;;) {
      const int iend = min(min(TM, credit), (i & ~63) + 64);
      if (wide) { int pub = 2 * i - (INFL + CLS01 + 3) / 4;
#pragma nounroll
        while (i < iend) page_asm<INFL + CLS01, K_NT, V_NT>(rk, rv, vk, vv, vctl, vp0, i, pub, so, mk);
      } else { int pub = 2 * i - (INFL + 3) / 4;
#pragma nounroll
        while (i < iend) page_asm<INFL, K_NT, V_NT>(rk, rv, vk, vv, vctl, vp0, i, pub, so, mk);
      }
      if (i >= T_w) break;
      const bool endgroup = i >= TM;
      const u32x4 rel = *(volatile LAS u32x4*)(ctl + C_REL);
      credit = min(min(4 * rfl((int)rel[0]), 4 * rfl((int)rel[1]) + 1), min(4 * rfl((int)rel[2]) + 2, 4 * rfl((int)rel[3]) + 3)) + NSLOT;
      if ((i & 63) == 0) { vp0 = vp1; vp1 = vp2; vp2 = vp3; so = (unsigned)rdl((int)vp0, 0); asm volatile("s_nop 4" :: "s"(so)); }
      if (credit <= i) {
        LAS unsigned char* relp = ctl + C_REL + 4 * (i & 3);
        const int need = (i >> 2) - NSLOT / 4 + 1;
        if (rfl(lds_ld(relp)) < need) { wait_vm<12>(); publish(2 * i - 3);
          if (rfl(lds_ld(relp)) < need) { wait_vm<4>(); publish(2 * i - 1);
            if (rfl(lds_ld(relp)) < need) { wait_vm<0>(); publish(2 * i); lds_wait_ge(relp, need); } } }
        credit = i + 1;
      }
      if (endgroup) {
        if (credit < T_w) { wait_vm<0>(); publish(2 * i);
          for (int e = i; e < T_w; ++e) if (e >= NSLOT) lds_wait_ge(ctl + C_REL + 4 * (e & 3), (e >> 2) - NSLOT / 4 + 1); }
        unsigned se[4];
        int pub = 2 * i - (INFL + 3) / 4;
#pragma unroll
        for (int r = 0; r < 4; ++r) {
          if (r > 0) { if (((i + r) & 63) == 0) { vp0 = vp1; vp1 = vp2; } so = (unsigned)rdl((int)vp0, (i + r) & 63); }
          se[r] = so;
          set_m0(((i + r) & (NSLOT - 1)) * 8192, so);
          WAIT_K(INFL, so);
          publish(pub + r);
        }
#pragma unroll
        for (int r = 0; r < 4; ++r) {
          set_m0(((i + r) & (NSLOT - 1)) * 8192 + 4096, se[r]);
          WAIT_V(INFL, se[r]);
          publish(pub + 4 + r);
        }
        break;
      }
    }
    STAMP(5);
    if (INFL + CLS01 + 4 > 24) { wait_vm<24>(); publish(2 * T_w - 6); }
    if (INFL + CLS01 + 4 > 20) { wait_vm<20>(); publish(2 * T_w - 5); }
    wait_vm<16>(); publish(2 * T_w - 4);
    wait_vm<12>(); publish(2 * T_w - 3);
    wait_vm<8>(); publish(2 * T_w - 2);
    wait_vm<4>(); publish(2 * T_w - 1);
    wait_vm<0>(); publish(2 * T_w);
    STAMP(6);
  } else if (wave <= NSW) {
    const int cw = wave - 1;
    wg_barrier();
    const unsigned ka0 = c * 256 + ((g ^ c) << 4);
    const int tv = 4 * g + (c >> 2);
    const unsigned va0 = 4096 + tv * 256 + ((tv & 7) << 5) + (c & 3) * 8;
    lds_wait_ge(ctl + C_READY, 1);
    STAMP(2);
    bf16x8 qf[2][4];
    {
      const u32x2 pq = *(LAS u32x2*)(ctl + C_PTR);
      const GAS bf16x8* qp = (const GAS bf16x8*)(((uintptr_t)(unsigned)rfl((int)pq[1]) << 32) | (unsigned)rfl((int)pq[0]));
#pragma unroll
      for (int cg = 0; cg < 2; ++cg)
#pragma unroll
        for (int s = 0; s < 4; ++s) { const int r = 16 * cg + c; qf[cg][s] = qp[(((r >> 1) * HQ + 2 * h + (r & 1)) * D + 32 * s + 8 * g) / 8]; }
    }
    len_raw = *(LAS int*)(ctl + C_LENS + 4 * c);
    const Part p = partition(rfl(*(LAS int*)(ctl + C_P)));
    const int len = p.len, S_w = p.S_w - p.ns - NA, hp = p.hp, ss = p.ss, T_w = p.TL, lo = p.lo, hi = p.hi, xoff = p.xoff, ns = p.ns, lpA = p.np_w - 1, lpB = p.np_w - p.own_w, vA = p.vA;
    const int minlen = LK - rdl(row_scan_max(LK - len), 15);
    const int len0 = __shfl(len, c >> 1), len1 = __shfl(len, 8 + (c >> 1));
    {
      const bool z0 = __shfl((int)p.all_masked, c >> 1) != 0, z1 = __shfl((int)p.all_masked, 8 + (c >> 1)) != 0;
#pragma unroll
      for (int s = 0; s < 4; ++s) {
        u32x4 t0 = __builtin_bit_cast(u32x4, qf[0][s]), t1 = __builtin_bit_cast(u32x4, qf[1][s]);
#pragma unroll
        for (int e = 0; e < 4; ++e) { t0[e] = z0 ? 0u : t0[e]; t1[e] = z1 ? 0u : t1[e]; }
        qf[0][s] = __builtin_bit_cast(bf16x8, t0); qf[1][s] = __builtin_bit_cast(bf16x8, t1);
      }
    }
    const int TM = T_w >= 8 ? T_w - 4 : T_w + 4;
    int j = 0, seen = 0;
    Soft s0, s1;
    auto page = [&](auto SHc, const bf16x8 (&qa)[4], const bf16x8 (&qb)[4], int la, int lb, int t0, bool mask) {
      constexpr bool SH = decltype(SHc)::value;
      const int i = 4 * j + cw;
      LAS unsigned char* sl = lds + L_RING + (i & (NSLOT - 1)) * 8192;
      const int re = i - TM;
      const int needk = re >= 0 ? 2 * TM + re + 1 : 2 * i + 1, needv = re >= 0 ? 2 * TM + re + 5 : 2 * i + 2;
      if (seen < needk) seen = lds_wait_ge(ctl + C_LANDED, needk);
      bf16x8 kf[4];
#pragma unroll
      for (int s = 0; s < 4; ++s) kf[s] = *(LAS bf16x8*)(sl + (ka0 ^ (s << 6)));
      f32x4 xa = {0.f, 0.f, 0.f, 0.f}, xb = {0.f, 0.f, 0.f, 0.f};
#pragma unroll
      for (int s = 0; s < 4; ++s) { xa = __builtin_amdgcn_mfma_f32_16x16x32_bf16(kf[s], qa[s], xa, 0, 0, 0);
        if (SH) xb = __builtin_amdgcn_mfma_f32_16x16x32_bf16(kf[s], qb[s], xb, 0, 0, 0); }
      const s16x4 pa = soft_step(s0, xa, mask, t0 + 4 * g, la);
      s16x4 pb; if (SH) pb = soft_step(s1, xb, mask, t0 + 4 * g, lb);
      if (seen < needv) seen = lds_wait_ge(ctl + C_LANDED, needv);
      s16x4 vf[8];
#pragma unroll
      for (int blk = 0; blk < 8; ++blk) vf[blk] = __builtin_amdgcn_ds_read_tr16_b64_v4i16((LAS s16x4*)(sl + (va0 ^ (blk << 5))));
      ++j;
      asm volatile("s_waitcnt lgkmcnt(0)" : "+v"(vf[0]), "+v"(vf[1]), "+v"(vf[2]), "+v"(vf[3]), "+v"(vf[4]), "+v"(vf[5]), "+v"(vf[6]), "+v"(vf[7]) :: "memory");
      lds_st(ctl + C_REL + 4 * cw, j);
#pragma unroll
      for (int blk = 0; blk < 8; ++blk) { s0.o[blk] = __builtin_amdgcn_mfma_f32_16x16x16bf16_1k(vf[blk], pa, s0.o[blk], 0, 0, 0);
        if (SH) s1.o[blk] = __builtin_amdgcn_mfma_f32_16x16x16bf16_1k(vf[blk], pb, s1.o[blk], 0, 0, 0); }
    };
    unsigned m = p.xmask;
    for (int kx = 0;; ++kx) {
      const bool early = kx == 0, last = !early && m == 0;
      const int b = (early || last) ? w : __builtin_ctz(m);
      const int i0 = early ? 0 : last ? NA + hp : rdl(lo, b) - S_w;
      const int i1 = early ? NA : last ? T_w : rdl(hi, b) - S_w;
      const int lp0 = early ? lpA - 2 * cw : last ? lpB : rdl(lo, b) + rdl(xoff, b);
      bf16x8 qa[4];
      if (b < 8) {
#pragma unroll
        for (int s = 0; s < 4; ++s) qa[s] = qf[0][s];
      } else {
#pragma unroll
        for (int s = 0; s < 4; ++s) qa[s] = qf[1][s];
      }
      const int lb = rdl(len, b);
      soft_reset(s0);
      if (last) lds_wait_ge(ctl + C_RFREE, kx);
      if (early && cw >= vA) {
        lds_wait_ge(ctl + C_LANDED, 2 * cw + 2); ++j; lds_st(ctl + C_REL + 4 * cw, j);
      }
      for (int i = 4 * j + cw; i < i1; i += 4) { const int t0 = (lp0 + i - i0) * PAGE; page(std::false_type{}, qa, qa, lb, lb, t0, t0 + PAGE > lb); }
      if (kx > 0 && !last) lds_wait_ge(ctl + C_RFREE, kx);
      const float lt = xrow16_sum(s0.l);
      if ((c >> 1) == (b & 7)) {
        LAS unsigned char* r = lds + L_RECX + (cw * 2 + (c & 1)) * ROW;
#pragma unroll
        for (int blk = 0; blk < 8; ++blk) *(LAS f32x4*)(r + blk * 64 + g * 16) = s0.o[blk];
        if (g == 0) *(LAS f32x2*)(r + 512) = f32x2{s0.m, lt};
      }
      lds_drain();
      if (lane == 0) __hip_atomic_fetch_add((LAS int*)(ctl + C_RCNT), 1, __ATOMIC_RELAXED, __HIP_MEMORY_SCOPE_WORKGROUP);
      if (last) break;
      if (early) {
        if (ns > 0) {
          soft_reset(s0); soft_reset(s1);
          for (int i = cw; i < ns; i += 4) { const int t0 = (ss + i) * PAGE; page(std::true_type{}, qf[0], qf[1], len0, len1, t0, t0 + PAGE > minlen); }
          if (cw < ns) {
            s0.l = xrow16_sum(s0.l); s1.l = xrow16_sum(s1.l);
            if (cw > 0) lds_wait_ge(ctl + C_CSTEP, cw);
#pragma unroll
            for (int cg = 0; cg < 2; ++cg) { Soft& s = cg ? s1 : s0;
              LAS unsigned char* r = lds + L_MRG + (16 * cg + c) * ROW;
              if (cw > 0) {
                const f32x2 ml = *(LAS f32x2*)(r + 512);
                const float M = fmaxf(ml[0], s.m), f0 = mfac(ml[0], M), f1 = mfac(s.m, M);
#pragma unroll
                for (int blk = 0; blk < 8; ++blk) s.o[blk] = *(LAS f32x4*)(r + blk * 64 + g * 16) * f0 + s.o[blk] * f1;
                s.l = ml[1] * f0 + s.l * f1; s.m = M;
              }
#pragma unroll
              for (int blk = 0; blk < 8; ++blk) *(LAS f32x4*)(r + blk * 64 + g * 16) = s.o[blk];
              lds_drain();
              if (g == 0) *(LAS f32x2*)(r + 512) = f32x2{s.m, s.l};
            }
            lds_drain();
            lds_st(ctl + C_CSTEP, cw + 1);
          }
        }
      } else m &= m - 1;
    }
  } else {
    if (lane < 12) *(LAS int*)(ctl + 4 * lane) = 0;
    wg_barrier();
    lds_wait_ge(ctl + C_READY, 1);
    STAMP(2);
    const u32x4 pq = *(LAS u32x4*)(ctl + C_PTR);
    const u32x2 pws = *(LAS u32x2*)(ctl + C_PTR + 16);
    GAS u32x2* outp = (GAS u32x2*)(((uintptr_t)(unsigned)rfl((int)pq[3]) << 32) | (unsigned)rfl((int)pq[2]));
    unsigned char* ws = (unsigned char*)(((uintptr_t)(unsigned)rfl((int)pws[1]) << 32) | (unsigned)rfl((int)pws[0]));
    const unsigned long long epoch = __hip_atomic_load((gu64*)(ws + W_EPOCH), RLX_AGENT);
    const __amdgpu_buffer_rsrc_t prsrc = __builtin_amdgcn_make_buffer_rsrc(ws + W_PART, 0, PART_BYTES, 0x00020000);
    len_raw = *(LAS int*)(ctl + C_LENS + 4 * c);
    const Part p = partition(rfl(*(LAS int*)(ctl + C_P)));
    const unsigned long long tag = epoch + 1ull;
    const int ns = p.ns;
    const unsigned blk = w * 16 + h;
    const int r = lane >> 5, ch = lane & 31;
    float M = -INFINITY, L = 0.f; f32x4 o = {0.f, 0.f, 0.f, 0.f};
    unsigned m = p.xmask;
#pragma nounroll
    for (int kx = 0;; ++kx) {
      const bool early = kx == 0, last = !early && m == 0;
      if (kx == 1 && ns > 0 && !ABL_NOSVC && !ABL_NOSTORE) {
        lds_wait_ge(ctl + C_CSTEP, min(ns, NSW));
#pragma unroll 4
        for (int k = 0; k < 16; ++k) {
          const u32x4 v = *(LAS u32x4*)(lds + L_MRG + (2 * k + r) * ROW + ch * 16);
          __builtin_amdgcn_raw_buffer_store_b128(v, prsrc, k * 1024 + lane * 16, blk * PS_BYTES, 16); }
        if (lane < 32) { const u32x2 v = *(LAS u32x2*)(lds + L_MRG + lane * ROW + 512);
          __builtin_amdgcn_raw_buffer_store_b64(v, prsrc, 32 * 512 + lane * 8, blk * PS_BYTES, 16); }
      }
      if (last) {
        asm volatile("s_waitcnt vmcnt(0)" ::: "memory");
        if (lane == 0) __hip_atomic_store((gu64*)(ws + W_FLAG + blk * FLAG_STRIDE), tag, RLX_AGENT);
        STAMP(3);
        const int E_w = rdl(p.E, w), exc_w = rdl(p.exc, w);
        const bool sh_ov = c < p.nsh;
        const bool ex_ov = min(p.S + p.spare, E_w + exc_w) > max(p.S, E_w);
        unsigned cmask = (ABL_NOSVC || ABL_NOGATHER) ? 0u : (unsigned)(__ballot(sh_ov) & 0xFFFFull) | ((unsigned)(__ballot(ex_ov) & 0xFFFFull) << 16);
        if (cmask) lds_wait_ge(ctl + C_LANDED, (p.TL * POLLAT) >> 3);
        {
          const bool need = !ABL_NOSVC && !ABL_NOPOLL && lane < 16 && (sh_ov || ex_ov);
          gu64* f = (gu64*)(ws + W_FLAG + (size_t)(c * 16 + h) * FLAG_STRIDE);
#pragma nounroll
          for (unsigned spins = 0; spins < (1u << 20); ++spins) {
            const bool ok = !need || __hip_atomic_load(f, RLX_AGENT) == tag;
            if (__all(ok)) break;
            __builtin_amdgcn_s_sleep(POLLSLEEP);
          }
          asm volatile("" ::: "memory");
        }
        STAMP(4);
        const unsigned vso = lane < 16 ? (unsigned)(c * 16 + h) * PS_BYTES + (unsigned)(2 * w) * 512 : W_PX + ((unsigned)(c * 16 + h) * 16 + w) * PX_BYTES;
        const unsigned vsm = lane < 16 ? (unsigned)(c * 16 + h) * PS_BYTES + 32 * 512 + (unsigned)(2 * w) * 8 : vso + 1024;
        constexpr int GB = 6;
        while (cmask) {
          u32x4 po[GB]; u32x2 pml[GB]; bool pv[GB];
#pragma unroll
          for (int k = 0; k < GB; ++k) {
            pv[k] = cmask != 0;
            const int id = pv[k] ? __builtin_ctz(cmask) : 0; cmask &= cmask - 1;
            const unsigned so = pv[k] ? (unsigned)rdl((int)vso, id) : PART_BYTES, sm = pv[k] ? (unsigned)rdl((int)vsm, id) : PART_BYTES;
            po[k] = __builtin_amdgcn_raw_buffer_load_b128(prsrc, lane * 16 + so, 0, 16);
            pml[k] = __builtin_amdgcn_raw_buffer_load_b64(prsrc, r * 8 + sm, 0, 16);
          }
#pragma unroll
          for (int k = 0; k < GB; ++k) {
            const float mk = pv[k] ? __uint_as_float(pml[k][0]) : -INFINITY, lk = __uint_as_float(pml[k][1]);
            const float Mn = fmaxf(M, mk), f0 = mfac(M, Mn), f1 = mfac(mk, Mn);
            o = o * f0 + __builtin_bit_cast(f32x4, po[k]) * f1; L = L * f0 + lk * f1; M = Mn;
          }
        }
        if (blk == 0) {
#pragma nounroll
          for (unsigned spins = 0; spins < (1u << 20); ++spins) {
            bool ok = true;
#pragma unroll
            for (int k = 0; k < 4; ++k) ok &= __hip_atomic_load((gu64*)(ws + W_FLAG + (size_t)(k * 64 + lane) * FLAG_STRIDE), RLX_AGENT) == tag;
            if (__all(ok)) break;
            __builtin_amdgcn_s_sleep(8);
          }
          if (lane == 0) __hip_atomic_store((gu64*)(ws + W_EPOCH), tag, RLX_AGENT);
        }
        STAMP(5);
      }
      lds_wait_ge(ctl + C_RCNT, NSW * (kx + 1));
      f32x2 ml[4]; f32x4 ok[4];
#pragma unroll
      for (int k = 0; k < 4; ++k) { ml[k] = *(LAS f32x2*)(lds + L_RECX + (k * 2 + r) * ROW + 512); ok[k] = *(LAS f32x4*)(lds + L_RECX + (k * 2 + r) * ROW + ch * 16); }
      const bool mine = early || last;
      float Mx = mine ? M : -INFINITY;
#pragma unroll
      for (int k = 0; k < 4; ++k) Mx = fmaxf(Mx, ml[k][0]);
      const float fr = mine ? mfac(M, Mx) : 0.f;
      float Lx = L * fr; f32x4 ox = o * fr;
#pragma unroll
      for (int k = 0; k < 4; ++k) { const float f = mfac(ml[k][0], Mx); Lx = fmaf(f, ml[k][1], Lx); ox += ok[k] * f; }
      asm volatile("s_waitcnt lgkmcnt(0)" : "+v"(ox) :: "memory");
      if (last) {
        const float inv = __builtin_amdgcn_rcpf(Lx);
        outp[((w * HQ + 2 * h + r) * D + 4 * ch) >> 2] = u32x2{cvtpk(ox[0] * inv, ox[1] * inv), cvtpk(ox[2] * inv, ox[3] * inv)};
        break;
      }
      lds_st(ctl + C_RFREE, kx + 1);
      if (early) { M = Mx; L = Lx; o = ox; }
      else {
        const unsigned so = W_PX + (blk * 16 + __builtin_ctz(m)) * PX_BYTES;
        __builtin_amdgcn_raw_buffer_store_b128(__builtin_bit_cast(u32x4, ox), prsrc, lane * 16, so, 16);
        const f32x4 mlv = {__builtin_bit_cast(float, rdl(__builtin_bit_cast(int, Mx), 0)), __builtin_bit_cast(float, rdl(__builtin_bit_cast(int, Lx), 0)),
                           __builtin_bit_cast(float, rdl(__builtin_bit_cast(int, Mx), 32)), __builtin_bit_cast(float, rdl(__builtin_bit_cast(int, Lx), 32))};
        if (lane < 8) __builtin_amdgcn_raw_buffer_store_b128(__builtin_bit_cast(u32x4, mlv), prsrc, 1024 + lane * 16, so, 16);
        m &= m - 1;
      }
    }
    STAMP(6);
  }
}

extern "C" void kernel_launch(void* const* d_in, const int* in_sizes, int n_in, void* d_out, int out_size, void* d_ws, size_t ws_size, hipStream_t stream) {
  Args a;
  a.q = (const bf16x8*)d_in[0]; a.k = d_in[1]; a.v = d_in[2]; a.bt = (const int*)d_in[3]; a.lens = (const int*)d_in[4];
  a.out = (unsigned*)d_out; a.ws = (unsigned char*)d_ws; a.kv_bytes = (unsigned)in_sizes[1] * 2u; a.pad = 0;
  hipFuncSetAttribute((const void*)radix_attn, hipFuncAttributeMaxDynamicSharedMemorySize, LDS_BYTES);
  ArgsPad pad; for (int i = 0; i < 16; ++i) pad.z[i] = 0;
  hipLaunchKernelGGL(radix_attn, dim3(256), dim3(NTHR), LDS_BYTES, stream, a, pad);
}
```

```cpp
#define CLS01 4
#define DELAY0 0
#include <hip/hip_runtime.h>
#include <hip/hip_bf16.h>
#include <stdint.h>

#define NSLOT 16
#define INFL 20
#define KV_NT 1
#define TLBPF 0
#define OUTSC1 0
#define OUT_STORE(p, v) (*(p) = (v))
#define ABL_NOCOMP 0
#define SLEEPN 1
#define ABL_NOSVC 0
#define ABL_NOSTORE 0
#define ABL_NOGATHER 0
#define ABL_NOPOLL 0
#define POLLSLEEP 32
#define SHCAP 21
#define POLLAT 9
#define STAUX 16
#define LDAUX 16
#define STPACE 0
#define XBIG 0
#define LATEF 12
#define FASTT 49
#define FASTN 2
#define WARM 1
#define DIAG 0
#define MAPSWAP 1

constexpr int B = 16, HQ = 32, HKV = 16, D = 128, PAGE = 16, NB = 256, LK = NB * PAGE;
constexpr int NA = 4;
constexpr int NSW = 4, NTHR = (NSW + 2) * 64;
constexpr float C2 = 0.088388347648318440f * 1.4426950408889634f;
constexpr unsigned PAGE_BYTES = PAGE * HKV * D * 2;
constexpr unsigned TOK_BYTES = HKV * D * 2;

constexpr int ROW = 528;
constexpr int L_RING = 0;
constexpr int L_MRG = NSLOT * 8192;
constexpr int L_TAB = L_RING + (NSLOT - 2) * 8192;
constexpr int L_RECX = L_MRG + 32 * ROW;
constexpr int L_MISC = L_RECX + 4 * 2 * ROW;
constexpr int C_LANDED = 0, C_CSTEP = 4, C_RCNT = 8, C_RFREE = 12, C_REL = 16, C_P = 32, C_DUMMY = 36, C_READY = 40, C_PTR = 48, C_LENS = 80;
constexpr int LDS_BYTES = L_MISC + 160;
static_assert(NSLOT % 4 == 0 && (NSLOT & (NSLOT - 1)) == 0, "ring geometry");
static_assert(LDS_BYTES <= 160 * 1024, "LDS");

constexpr size_t W_EPOCH = 0, W_FLAG = 256, FLAG_STRIDE = 128;
constexpr size_t W_PART = 65536;
constexpr unsigned PS_BYTES = 32 * 512 + 256, PX_BYTES = 2 * 512 + 128;
constexpr unsigned W_PX = 256 * PS_BYTES;
constexpr unsigned PART_BYTES = W_PX + 256 * 16 * PX_BYTES;
#define STAMP(k) do {} while (0)

typedef __bf16 bf16x8 __attribute__((ext_vector_type(8)));
typedef __bf16 bf16x2 __attribute__((ext_vector_type(2)));
typedef short s16x4 __attribute__((ext_vector_type(4)));
typedef float f32x4 __attribute__((ext_vector_type(4)));
typedef float f32x2 __attribute__((ext_vector_type(2)));
typedef unsigned u32x4 __attribute__((ext_vector_type(4)));
typedef unsigned u32x2 __attribute__((ext_vector_type(2)));
typedef int i32x4 __attribute__((ext_vector_type(4)));
#define LAS __attribute__((address_space(3)))
#define GAS __attribute__((address_space(1)))
#define DI __device__ __forceinline__
typedef GAS unsigned long long gu64;
#define RLX_AGENT __ATOMIC_RELAXED, __HIP_MEMORY_SCOPE_AGENT

DI int rfl(int x) { return __builtin_amdgcn_readfirstlane(x); }
DI int rdl(int x, int l) { return __builtin_amdgcn_readlane(x, l); }
template <int CTRL> DI int dpp_i(int oldv, int x) { return __builtin_amdgcn_update_dpp(oldv, x, CTRL, 0xf, 0xf, false); }
DI int row_scan_add(int x) {
  x += dpp_i<0x111>(0, x); x += dpp_i<0x112>(0, x); x += dpp_i<0x114>(0, x); x += dpp_i<0x118>(0, x); return x;
}
DI int row_scan_max(int x) {
  x = max(x, dpp_i<0x111>(0, x)); x = max(x, dpp_i<0x112>(0, x)); x = max(x, dpp_i<0x114>(0, x)); x = max(x, dpp_i<0x118>(0, x)); return x;
}
DI float xrow16_max(float x) {
  auto s = __builtin_amdgcn_permlane16_swap(__float_as_uint(x), __float_as_uint(x), false, false);
  x = fmaxf(__uint_as_float(s[0]), __uint_as_float(s[1]));
  auto t = __builtin_amdgcn_permlane32_swap(__float_as_uint(x), __float_as_uint(x), false, false);
  return fmaxf(__uint_as_float(t[0]), __uint_as_float(t[1]));
}
DI float xrow16_sum(float x) {
  auto s = __builtin_amdgcn_permlane16_swap(__float_as_uint(x), __float_as_uint(x), false, false);
  x = __uint_as_float(s[0]) + __uint_as_float(s[1]);
  auto t = __builtin_amdgcn_permlane32_swap(__float_as_uint(x), __float_as_uint(x), false, false);
  return __uint_as_float(t[0]) + __uint_as_float(t[1]);
}
DI unsigned cvtpk(float lo, float hi) { f32x2 v = {lo, hi}; bf16x2 b = __builtin_convertvector(v, bf16x2); return __builtin_bit_cast(unsigned, b); }
DI float ex2(float x) { return __builtin_amdgcn_exp2f(x); }
DI void wg_barrier() { asm volatile("s_waitcnt lgkmcnt(0)\n\ts_barrier" ::: "memory"); }
template <int N> DI void wait_vm() { asm volatile("s_waitcnt vmcnt(%0)" :: "i"(N) : "memory"); }

#define NTS " nt"
DI void dma4(unsigned m0v, __amdgpu_buffer_rsrc_t rsrc, unsigned soff, unsigned v0, unsigned v1, unsigned v2, unsigned v3) {
  asm volatile("s_mov_b32 m0, %0\n\t"
               "s_nop 4\n\t"
               "buffer_load_dwordx4 %1, %5, %6 offen" NTS " lds\n\t"
               "buffer_load_dwordx4 %2, %5, %6 offen offset:1024" NTS " lds\n\t"
               "buffer_load_dwordx4 %3, %5, %6 offen offset:2048" NTS " lds\n\t"
               "buffer_load_dwordx4 %4, %5, %6 offen offset:3072" NTS " lds"
               :: "s"(m0v), "v"(v0), "v"(v1), "v"(v2), "v"(v3), "s"(rsrc), "s"(soff) : "memory");
}

DI void set_m0(unsigned m0v, unsigned so) { asm volatile("s_mov_b32 m0, %0\n\ts_nop 3" :: "s"(m0v), "s"(so) : "memory"); }
template <int X, bool NT> DI void wait_dma4x(__amdgpu_buffer_rsrc_t rsrc, unsigned soff, unsigned v0, unsigned v1, unsigned v2, unsigned v3) {
  if (NT)
  asm volatile("s_waitcnt vmcnt(%6)\n\t"
               "buffer_load_dwordx4 %0, %4, %5 offen nt lds\n\t"
               "buffer_load_dwordx4 %1, %4, %5 offen offset:1024 nt lds\n\t"
               "buffer_load_dwordx4 %2, %4, %5 offen offset:2048 nt lds\n\t"
               "buffer_load_dwordx4 %3, %4, %5 offen offset:3072 nt lds"
               :: "v"(v0), "v"(v1), "v"(v2), "v"(v3), "s"(rsrc), "s"(soff), "i"(X) : "memory");
  else
  asm volatile("s_waitcnt vmcnt(%6)\n\t"
               "buffer_load_dwordx4 %0, %4, %5 offen lds\n\t"
               "buffer_load_dwordx4 %1, %4, %5 offen offset:1024 lds\n\t"
               "buffer_load_dwordx4 %2, %4, %5 offen offset:2048 lds\n\t"
               "buffer_load_dwordx4 %3, %4, %5 offen offset:3072 lds"
               :: "v"(v0), "v"(v1), "v"(v2), "v"(v3), "s"(rsrc), "s"(soff), "i"(X) : "memory");
}
template <bool NT> DI void dma4r(__amdgpu_buffer_rsrc_t r0, __amdgpu_buffer_rsrc_t r1, __amdgpu_buffer_rsrc_t r2, __amdgpu_buffer_rsrc_t r3, unsigned soff, unsigned v0, unsigned v1, unsigned v2, unsigned v3) {
  if (NT)
  asm volatile("s_nop 0\n\t"
               "buffer_load_dwordx4 %0, %4, %8 offen nt lds\n\t"
               "buffer_load_dwordx4 %1, %5, %8 offen offset:1024 nt lds\n\t"
               "buffer_load_dwordx4 %2, %6, %8 offen offset:2048 nt lds\n\t"
               "buffer_load_dwordx4 %3, %7, %8 offen offset:3072 nt lds"
               :: "v"(v0), "v"(v1), "v"(v2), "v"(v3), "s"(r0), "s"(r1), "s"(r2), "s"(r3), "s"(soff) : "memory");
  else
  asm volatile("s_nop 0\n\t"
               "buffer_load_dwordx4 %0, %4, %8 offen lds\n\t"
               "buffer_load_dwordx4 %1, %5, %8 offen offset:1024 lds\n\t"
               "buffer_load_dwordx4 %2, %6, %8 offen offset:2048 lds\n\t"
               "buffer_load_dwordx4 %3, %7, %8 offen offset:3072 lds"
               :: "v"(v0), "v"(v1), "v"(v2), "v"(v3), "s"(r0), "s"(r1), "s"(r2), "s"(r3), "s"(soff) : "memory");
}
#define K_NT 1
#define V_NT 1
#define WAIT_K(X, so) wait_dma4x<X, K_NT>(rk, so, vk[0], vk[1], vk[2], vk[3])
#define WAIT_V(X, so) wait_dma4x<X, V_NT>(rv, so, vv[0], vv[1], vv[2], vv[3])

template <int X, bool KNT, bool VNT>
DI void page_asm(__amdgpu_buffer_rsrc_t rk, __amdgpu_buffer_rsrc_t rv, const unsigned (&vk)[4], const unsigned (&vv)[4], unsigned vctl, unsigned vp,
                 int& i, int& pub, unsigned& so, unsigned& mk) {
  unsigned vt;
#define PAGE_ASM(KS, VS) \
  asm volatile("s_waitcnt vmcnt(%[X])\n\t" \
               "buffer_load_dwordx4 %[k0], %[rk], %[so] offen" KS " lds\n\t" \
               "buffer_load_dwordx4 %[k1], %[rk], %[so] offen offset:1024" KS " lds\n\t" \
               "buffer_load_dwordx4 %[k2], %[rk], %[so] offen offset:2048" KS " lds\n\t" \
               "buffer_load_dwordx4 %[k3], %[rk], %[so] offen offset:3072" KS " lds\n\t" \
               "v_mov_b32 %[vt], %[pub]\n\t" \
               "s_add_u32 m0, m0, 0x1000\n\t" \
               "ds_write_b32 %[vctl], %[vt]\n\t" \
               "s_add_u32 %[pub], %[pub], 1\n\t" \
               "s_waitcnt vmcnt(%[X])\n\t" \
               "buffer_load_dwordx4 %[v0], %[rv], %[so] offen" VS " lds\n\t" \
               "buffer_load_dwordx4 %[v1], %[rv], %[so] offen offset:1024" VS " lds\n\t" \
               "buffer_load_dwordx4 %[v2], %[rv], %[so] offen offset:2048" VS " lds\n\t" \
               "buffer_load_dwordx4 %[v3], %[rv], %[so] offen offset:3072" VS " lds\n\t" \
               "v_mov_b32 %[vt], %[pub]\n\t" \
               "s_add_u32 %[i], %[i], 1\n\t" \
               "ds_write_b32 %[vctl], %[vt]\n\t" \
               "v_readlane_b32 %[so], %[vp], %[i]\n\t" \
               "s_add_u32 %[pub], %[pub], 1\n\t" \
               "s_add_u32 %[mk], %[mk], 0x2000\n\t" \
               "s_and_b32 %[mk], %[mk], %[msk]\n\t" \
               "s_mov_b32 m0, %[mk]" \
               : [i] "+s"(i), [pub] "+s"(pub), [so] "+s"(so), [mk] "+s"(mk), [vt] "=&v"(vt) \
               : [X] "i"(X), [msk] "i"(NSLOT * 8192 - 1), [k0] "v"(vk[0]), [k1] "v"(vk[1]), [k2] "v"(vk[2]), [k3] "v"(vk[3]), [v0] "v"(vv[0]), [v1] "v"(vv[1]), [v2] "v"(vv[2]), [v3] "v"(vv[3]), \
                 [rk] "s"(rk), [rv] "s"(rv), [vctl] "v"(vctl), [vp] "v"(vp) : "memory", "scc")
  if (KNT && VNT) PAGE_ASM(" nt", " nt"); else if (KNT) PAGE_ASM(" nt", ""); else if (VNT) PAGE_ASM("", " nt"); else PAGE_ASM("", "");
#undef PAGE_ASM
}

struct Args { const bf16x8* q; const void* k; const void* v; const int* bt; const int* lens; unsigned* out; unsigned char* ws; unsigned kv_bytes; unsigned pad; };

DI float mfac(float m, float M) { return (m == -INFINITY) ? 0.f : ex2((m - M) * C2); }

struct Soft { float m, l; f32x4 o[8]; };
DI void soft_reset(Soft& s) { s.m = -INFINITY; s.l = 0.f;
#pragma unroll
  for (int i = 0; i < 8; ++i) s.o[i] = f32x4{0.f, 0.f, 0.f, 0.f}; }

DI s16x4 soft_step(Soft& s, f32x4 x, bool mask, int tok0, int len) {
  if (mask) {
#pragma unroll
    for (int r = 0; r < 4; ++r) x[r] = (tok0 + r < len) ? x[r] : -INFINITY;
  }
  float pm = fmaxf(fmaxf(x[0], x[1]), fmaxf(x[2], x[3]));
  pm = xrow16_max(pm);
  const float mn = fmaxf(s.m, pm);
  const float ms = (mn == -INFINITY) ? 0.f : mn * C2;
  const float alpha = ex2(s.m * C2 - ms);
  float p[4];
#pragma unroll
  for (int r = 0; r < 4; ++r) p[r] = ex2(fmaf(x[r], C2, -ms));
  s.l = fmaf(s.l, alpha, (p[0] + p[1]) + (p[2] + p[3]));
  s.m = mn;
#pragma unroll
  for (int i = 0; i < 8; ++i) s.o[i] *= alpha;
  u32x2 pk = {cvtpk(p[0], p[1]), cvtpk(p[2], p[3])};
  return __builtin_bit_cast(s16x4, pk);
}

DI int lds_ld(LAS unsigned char* p) { return __hip_atomic_load((LAS int*)p, __ATOMIC_RELAXED, __HIP_MEMORY_SCOPE_WORKGROUP); }
DI void lds_st(LAS unsigned char* p, int v) { __hip_atomic_store((LAS int*)p, v, __ATOMIC_RELAXED, __HIP_MEMORY_SCOPE_WORKGROUP); }
DI int lds_wait_ge(LAS unsigned char* p, int need) {
  int v;
#pragma nounroll
  for (unsigned spins = 0;; ++spins) { v = rfl(lds_ld(p)); if (v >= need || spins > (1u << 24)) break; __builtin_amdgcn_s_sleep(SLEEPN); }
  asm volatile("" ::: "memory");
  return v;
}
DI void lds_drain() { asm volatile("s_waitcnt lgkmcnt(0)" ::: "memory"); }

__global__ void __launch_bounds__(NTHR, 2) radix_attn(Args a_) {
  extern __shared__ __attribute__((aligned(16))) unsigned char lds_raw[];
  LAS unsigned char* lds = (LAS unsigned char*)lds_raw;
  LAS unsigned char* ctl = lds + L_MISC;
  const int tid = threadIdx.x, lane = tid & 63, wave = rfl(tid >> 6);
  const int w = blockIdx.x & 15, h = blockIdx.x >> 4;
  const int c = lane & 15, g = lane >> 4;
  int len_raw;

  struct Part { int len, Pe, S, E, spare, exc, lo, hi, xoff, S_w, spare_w, T_w, ns, ss, hp, nsh, np_w, own_w, vA, TL; unsigned xmask; bool all_masked; };
  auto partition = [&](int P) {
    Part p;
    p.all_masked = len_raw <= 0;
    p.len = p.all_masked ? LK : min(len_raw, LK);
    const int np = (p.len + PAGE - 1) >> 4;
    p.Pe = min(P, rdl(row_scan_max(np), 15));
    const int n = max(np - p.Pe, 0);
    const int W = p.Pe + rdl(row_scan_add(n), 15);
    const int T = (W >> 4) + (c < (W & 15) ? 1 : 0);
    const int cap = max(1, ((W >> 4) * SHCAP) >> 6);
    int lg = 0;
    if (p.Pe > cap) lg = 1;
    if (p.Pe > 2 * cap) lg = 2;
    if (p.Pe > 4 * cap) lg = 3;
    if (p.Pe > 8 * cap) lg = 4;
    const int q = p.Pe >> lg, rq = p.Pe & ((1 << lg) - 1);
    const int sh = c < (1 << lg) ? q + (c < rq ? 1 : 0) : 0;
    p.nsh = min(1 << lg, p.Pe);
    p.ns = w < (1 << lg) ? q + (w < rq ? 1 : 0) : 0;
    p.ss = w * q + min(w, rq);
    const int own = min(n, T - sh);
    p.exc = n - own; p.spare = T - sh - own;
    p.S = row_scan_add(p.spare) - p.spare;
    p.E = row_scan_add(p.exc) - p.exc;
    p.S_w = rdl(p.S, w); p.spare_w = rdl(p.spare, w); p.T_w = rdl(T, w);
    p.hp = p.ns + p.spare_w;
    p.lo = max(p.S_w, p.E); p.hi = min(p.S_w + p.spare_w, p.E + p.exc);
    p.xmask = (unsigned)(__ballot(p.hi > p.lo) & 0xFFFFull);
    p.xoff = p.Pe - p.E;
    p.np_w = rdl(np, w); p.own_w = rdl(own, w);
    p.vA = min(NA, p.own_w);
    p.TL = NA + p.hp + p.own_w - p.vA;
    return p;
  };

  if (__builtin_expect(wave == 0, 1)) {
    __builtin_amdgcn_s_setprio(3);
    typedef unsigned u32x16 __attribute__((ext_vector_type(16)));
    u32x16 ka = *(const __attribute__((address_space(4))) u32x16*)__builtin_amdgcn_kernarg_segment_ptr();
    asm volatile("" : "+s"(ka));
    Args a;
    a.q = (const bf16x8*)(((uintptr_t)ka[1] << 32) | ka[0]); a.k = (const void*)(((uintptr_t)ka[3] << 32) | ka[2]); a.v = (const void*)(((uintptr_t)ka[5] << 32) | ka[4]);
    a.bt = (const int*)(((uintptr_t)ka[7] << 32) | ka[6]); a.lens = (const int*)(((uintptr_t)ka[9] << 32) | ka[8]); a.out = (unsigned*)(((uintptr_t)ka[11] << 32) | ka[10]);
    a.ws = (unsigned char*)(((uintptr_t)ka[13] << 32) | ka[12]); a.kv_bytes = ka[14];
    len_raw = *(const GAS int*)((uintptr_t)a.lens + 4 * c);
    const GAS i32x4* bt4 = (const GAS i32x4*)(uintptr_t)a.bt;
    i32x4 rk16[16];
#pragma unroll
    for (int k = 0; k < 16; ++k) rk16[k] = bt4[k * (NB / 4) + lane];
    STAMP(8);
    asm volatile("s_barrier" ::: "memory");
    if (lane == 0) { *(LAS u32x4*)(ctl + C_PTR) = u32x4{(unsigned)(uintptr_t)a.q, (unsigned)((uintptr_t)a.q >> 32), (unsigned)(uintptr_t)a.out, (unsigned)((uintptr_t)a.out >> 32)};
                     *(LAS u32x2*)(ctl + C_PTR + 16) = u32x2{(unsigned)(uintptr_t)a.ws, (unsigned)((uintptr_t)a.ws >> 32)}; }
    unsigned vk[4], vv[4];
#pragma unroll
    for (int i = 0; i < 4; ++i) { const int t = 4 * i + g;
      vk[i] = t * TOK_BYTES + ((c ^ t) << 4) - 1024 * i;
      vv[i] = t * TOK_BYTES + ((c ^ ((t & 7) << 1)) << 4) - 1024 * i; }
    asm volatile("" : "+v"(vk[0]), "+v"(vk[1]), "+v"(vk[2]), "+v"(vk[3]), "+v"(vv[0]), "+v"(vv[1]), "+v"(vv[2]), "+v"(vv[3]));
    const unsigned ring = (unsigned)(uintptr_t)(lds + L_RING);
    const __amdgpu_buffer_rsrc_t rk = __builtin_amdgcn_make_buffer_rsrc((void*)a.k, 0, a.kv_bytes, 0x00020000);
    const __amdgpu_buffer_rsrc_t rv = __builtin_amdgcn_make_buffer_rsrc((void*)a.v, 0, a.kv_bytes, 0x00020000);
#pragma unroll
    for (int k = 0; k < 16; ++k) *(LAS i32x4*)(lds + L_TAB + (k * NB + 4 * lane) * 4) = rk16[k];
    unsigned vp0, vp1 = 0, vp2 = 0, vp3 = 0;
    int nq0;
    {
      const int lw = rdl(len_raw, w);
      const int npw = ((lw <= 0 ? LK : min(lw, LK)) + PAGE - 1) >> 4;
      vp0 = (unsigned)*(LAS int*)(lds + L_TAB + 4 * (w * NB + max(npw - 1 - (lane & 3), 0))) * PAGE_BYTES + (unsigned)h * (D * 2);
      nq0 = ((lw <= 0 ? LK : min(lw, LK)) - (npw - 1) * PAGE + 3) >> 2;
    }
    unsigned so = (unsigned)rdl((int)vp0, 0), m0k = ring;
    {
      const __amdgpu_buffer_rsrc_t k1 = __builtin_amdgcn_make_buffer_rsrc((void*)a.k, 0, nq0 > 1 ? a.kv_bytes : 0u, 0x00020000), k2 = __builtin_amdgcn_make_buffer_rsrc((void*)a.k, 0, nq0 > 2 ? a.kv_bytes : 0u, 0x00020000),
                                   k3 = __builtin_amdgcn_make_buffer_rsrc((void*)a.k, 0, nq0 > 3 ? a.kv_bytes : 0u, 0x00020000);
      set_m0(m0k, so);
      dma4r<K_NT>(rk, k1, k2, k3, so, vk[0], vk[1], vk[2], vk[3]);
      const __amdgpu_buffer_rsrc_t v1 = __builtin_amdgcn_make_buffer_rsrc((void*)a.v, 0, nq0 > 1 ? a.kv_bytes : 0u, 0x00020000), v2 = __builtin_amdgcn_make_buffer_rsrc((void*)a.v, 0, nq0 > 2 ? a.kv_bytes : 0u, 0x00020000),
                                   v3 = __builtin_amdgcn_make_buffer_rsrc((void*)a.v, 0, nq0 > 3 ? a.kv_bytes : 0u, 0x00020000);
      set_m0(m0k + 4096, so);
      dma4r<V_NT>(rv, v1, v2, v3, so, vv[0], vv[1], vv[2], vv[3]);
      so = (unsigned)rdl((int)vp0, 1); m0k = ring + 8192;
      set_m0(m0k, so);
    }
#pragma nounroll
    for (int i = 1; i < 3; ++i) {
      WAIT_K(63, so);
      set_m0(m0k + 4096, so);
      WAIT_V(63, so);
      so = (unsigned)rdl((int)vp0, i + 1);
      m0k = ring + (i + 1) * 8192;
      set_m0(m0k, so);
    }
    STAMP(3);
    i32x4 acc = rk16[1] ^ rk16[0];
#pragma unroll
    for (int k = 2; k < 16; ++k) acc |= rk16[k] ^ rk16[0];
    int P;
    {
      const unsigned long long b0 = __ballot(acc[0] != 0), b1 = __ballot(acc[1] != 0), b2 = __ballot(acc[2] != 0), b3 = __ballot(acc[3] != 0);
      const unsigned long long any = b0 | b1 | b2 | b3;
      const int l = any ? (int)__builtin_ctzll(any) : 64;
      const unsigned long long bit = any & (0ull - any);
      P = 4 * l + ((b0 & bit) ? 0 : (b1 & bit) ? 1 : (b2 & bit) ? 2 : (b3 & bit) ? 3 : 0);
    }
    if (lane == 0) *(LAS int*)(ctl + C_P) = P;
    if (lane < 16) *(LAS int*)(ctl + C_LENS + 4 * lane) = len_raw;
    lds_drain();
    lds_st(ctl + C_READY, 1);
    STAMP(2);
    const Part p = partition(P);
    const int S_w = p.S_w - p.ns - NA, hp = p.hp, ns = p.ns, ss = p.ss, T_w = p.TL, lo = p.lo, hi = p.hi, xoff = p.xoff; const unsigned xmask = p.xmask;
    const int lpA = p.np_w - 1, lpB = p.np_w - p.own_w;
    auto page_src = [&](int r) {
      const int i = 64 * r + lane;
      int idx = (i < NA) ? w * NB + max(lpA - i, 0) : (i < NA + ns) ? ss + (i - NA) : w * NB + lpB + (i - NA - hp);
      for (unsigned m = xmask; m; m &= m - 1) {
        const int b = __builtin_ctz(m);
        const int lo_b = rdl(lo, b) - S_w, hi_b = rdl(hi, b) - S_w, off_b = rdl(xoff, b) + b * NB + S_w;
        if (i >= lo_b && i < hi_b) idx = i + off_b;
      }
      return (unsigned)*(LAS int*)(lds + L_TAB + 4 * (i < T_w ? idx : 0)) * PAGE_BYTES + (unsigned)h * (D * 2);
    };
    vp0 = page_src(0); vp1 = page_src(1); vp2 = page_src(2); vp3 = page_src(3);
    asm volatile("" : "+v"(vp0), "+v"(vp1), "+v"(vp2), "+v"(vp3));
    STAMP(7);
    static_assert(L_RING == 0 && C_LANDED == 0 && C_REL == 16, "page_asm addresses the ring from LDS offset 0 and the control words by these offsets");
    const unsigned vctl = (unsigned)(uintptr_t)ctl;
    auto publish = [&](int nl) { lds_st(ctl + C_LANDED, nl); };
    const bool wide = CLS01 && ((((unsigned)(uintptr_t)a.k >> 8) + h) & 3) == 1;
    const int TM = T_w >= 8 ? T_w - 4 : T_w;
    int i = 3, credit = NSLOT;
    unsigned mk = m0k;
    for (;;) {
      const int iend = min(min(TM, credit), (i & ~63) + 64);
      if (wide) { int pub = 2 * i - (INFL + CLS01 + 3) / 4;
#pragma nounroll
        while (i < iend) page_asm<INFL + CLS01, K_NT, V_NT>(rk, rv, vk, vv, vctl, vp0, i, pub, so, mk);
      } else { int pub = 2 * i - (INFL + 3) / 4;
#pragma nounroll
        while (i < iend) page_asm<INFL, K_NT, V_NT>(rk, rv, vk, vv, vctl, vp0, i, pub, so, mk);
      }
      if (i >= T_w) break;
      const bool endgroup = i >= TM;
      const u32x4 rel = *(volatile LAS u32x4*)(ctl + C_REL);
      credit = min(min(4 * rfl((int)rel[0]), 4 * rfl((int)rel[1]) + 1), min(4 * rfl((int)rel[2]) + 2, 4 * rfl((int)rel[3]) + 3)) + NSLOT;
      if ((i & 63) == 0) { vp0 = vp1; vp1 = vp2; vp2 = vp3; so = (unsigned)rdl((int)vp0, 0); asm volatile("s_nop 4" :: "s"(so)); }
      if (credit <= i) {
        LAS unsigned char* relp = ctl + C_REL + 4 * (i & 3);
        const int need = (i >> 2) - NSLOT / 4 + 1;
        if (rfl(lds_ld(relp)) < need) { wait_vm<12>(); publish(2 * i - 3);
          if (rfl(lds_ld(relp)) < need) { wait_vm<4>(); publish(2 * i - 1);
            if (rfl(lds_ld(relp)) < need) { wait_vm<0>(); publish(2 * i); lds_wait_ge(relp, need); } } }
        credit = i + 1;
      }
      if (endgroup) {
        if (credit < T_w) { wait_vm<0>(); publish(2 * i);
          for (int e = i; e < T_w; ++e) if (e >= NSLOT) lds_wait_ge(ctl + C_REL + 4 * (e & 3), (e >> 2) - NSLOT / 4 + 1); }
        unsigned se[4];
        int pub = 2 * i - (INFL + 3) / 4;
#pragma unroll
        for (int r = 0; r < 4; ++r) {
          if (r > 0) { if (((i + r) & 63) == 0) { vp0 = vp1; vp1 = vp2; } so = (unsigned)rdl((int)vp0, (i + r) & 63); }
          se[r] = so;
          set_m0(((i + r) & (NSLOT - 1)) * 8192, so);
          WAIT_K(INFL, so);
          publish(pub + r);
        }
#pragma unroll
        for (int r = 0; r < 4; ++r) {
          set_m0(((i + r) & (NSLOT - 1)) * 8192 + 4096, se[r]);
          WAIT_V(INFL, se[r]);
          publish(pub + 4 + r);
        }
        break;
      }
    }
    STAMP(5);
    if (INFL + CLS01 + 4 > 24) { wait_vm<24>(); publish(2 * T_w - 6); }
    if (INFL + CLS01 + 4 > 20) { wait_vm<20>(); publish(2 * T_w - 5); }
    wait_vm<16>(); publish(2 * T_w - 4);
    wait_vm<12>(); publish(2 * T_w - 3);
    wait_vm<8>(); publish(2 * T_w - 2);
    wait_vm<4>(); publish(2 * T_w - 1);
    wait_vm<0>(); publish(2 * T_w);
    STAMP(6);
  } else if (wave <= NSW) {
    const int cw = wave - 1;
    wg_barrier();
    const unsigned ka0 = c * 256 + ((g ^ c) << 4);
    const int tv = 4 * g + (c >> 2);
    const unsigned va0 = 4096 + tv * 256 + ((tv & 7) << 5) + (c & 3) * 8;
    lds_wait_ge(ctl + C_READY, 1);
    STAMP(2);
    bf16x8 qf[2][4];
    {
      const u32x2 pq = *(LAS u32x2*)(ctl + C_PTR);
      const GAS bf16x8* qp = (const GAS bf16x8*)(((uintptr_t)(unsigned)rfl((int)pq[1]) << 32) | (unsigned)rfl((int)pq[0]));
#pragma unroll
      for (int cg = 0; cg < 2; ++cg)
#pragma unroll
        for (int s = 0; s < 4; ++s) { const int r = 16 * cg + c; qf[cg][s] = qp[(((r >> 1) * HQ + 2 * h + (r & 1)) * D + 32 * s + 8 * g) / 8]; }
    }
    len_raw = *(LAS int*)(ctl + C_LENS + 4 * c);
    const Part p = partition(rfl(*(LAS int*)(ctl + C_P)));
    const int len = p.len, S_w = p.S_w - p.ns - NA, hp = p.hp, ss = p.ss, T_w = p.TL, lo = p.lo, hi = p.hi, xoff = p.xoff, ns = p.ns, lpA = p.np_w - 1, lpB = p.np_w - p.own_w, vA = p.vA;
    const int minlen = LK - rdl(row_scan_max(LK - len), 15);
    const int len0 = __shfl(len, c >> 1), len1 = __shfl(len, 8 + (c >> 1));
    {
      const bool z0 = __shfl((int)p.all_masked, c >> 1) != 0, z1 = __shfl((int)p.all_masked, 8 + (c >> 1)) != 0;
#pragma unroll
      for (int s = 0; s < 4; ++s) {
        u32x4 t0 = __builtin_bit_cast(u32x4, qf[0][s]), t1 = __builtin_bit_cast(u32x4, qf[1][s]);
#pragma unroll
        for (int e = 0; e < 4; ++e) { t0[e] = z0 ? 0u : t0[e]; t1[e] = z1 ? 0u : t1[e]; }
        qf[0][s] = __builtin_bit_cast(bf16x8, t0); qf[1][s] = __builtin_bit_cast(bf16x8, t1);
      }
    }
    const int TM = T_w >= 8 ? T_w - 4 : T_w + 4;
    int j = 0, seen = 0;
    Soft s0, s1;
    auto page = [&](auto SHc, const bf16x8 (&qa)[4], const bf16x8 (&qb)[4], int la, int lb, int t0, bool mask) {
      constexpr bool SH = decltype(SHc)::value;
      const int i = 4 * j + cw;
      LAS unsigned char* sl = lds + L_RING + (i & (NSLOT - 1)) * 8192;
      const int re = i - TM;
      const int needk = re >= 0 ? 2 * TM + re + 1 : 2 * i + 1, needv = re >= 0 ? 2 * TM + re + 5 : 2 * i + 2;
      if (seen < needk) seen = lds_wait_ge(ctl + C_LANDED, needk);
      bf16x8 kf[4];
#pragma unroll
      for (int s = 0; s < 4; ++s) kf[s] = *(LAS bf16x8*)(sl + (ka0 ^ (s << 6)));
      f32x4 xa = {0.f, 0.f, 0.f, 0.f}, xb = {0.f, 0.f, 0.f, 0.f};
#pragma unroll
      for (int s = 0; s < 4; ++s) { xa = __builtin_amdgcn_mfma_f32_16x16x32_bf16(kf[s], qa[s], xa, 0, 0, 0);
        if (SH) xb = __builtin_amdgcn_mfma_f32_16x16x32_bf16(kf[s], qb[s], xb, 0, 0, 0); }
      const s16x4 pa = soft_step(s0, xa, mask, t0 + 4 * g, la);
      s16x4 pb; if (SH) pb = soft_step(s1, xb, mask, t0 + 4 * g, lb);
      if (seen < needv) seen = lds_wait_ge(ctl + C_LANDED, needv);
      s16x4 vf[8];
#pragma unroll
      for (int blk = 0; blk < 8; ++blk) vf[blk] = __builtin_amdgcn_ds_read_tr16_b64_v4i16((LAS s16x4*)(sl + (va0 ^ (blk << 5))));
      ++j;
      asm volatile("s_waitcnt lgkmcnt(0)" : "+v"(vf[0]), "+v"(vf[1]), "+v"(vf[2]), "+v"(vf[3]), "+v"(vf[4]), "+v"(vf[5]), "+v"(vf[6]), "+v"(vf[7]) :: "memory");
      lds_st(ctl + C_REL + 4 * cw, j);
#pragma unroll
      for (int blk = 0; blk < 8; ++blk) { s0.o[blk] = __builtin_amdgcn_mfma_f32_16x16x16bf16_1k(vf[blk], pa, s0.o[blk], 0, 0, 0);
        if (SH) s1.o[blk] = __builtin_amdgcn_mfma_f32_16x16x16bf16_1k(vf[blk], pb, s1.o[blk], 0, 0, 0); }
    };
    unsigned m = p.xmask;
    for (int kx = 0;; ++kx) {
      const bool early = kx == 0, last = !early && m == 0;
      const int b = (early || last) ? w : __builtin_ctz(m);
      const int i0 = early ? 0 : last ? NA + hp : rdl(lo, b) - S_w;
      const int i1 = early ? NA : last ? T_w : rdl(hi, b) - S_w;
      const int lp0 = early ? lpA - 2 * cw : last ? lpB : rdl(lo, b) + rdl(xoff, b);
      bf16x8 qa[4];
      if (b < 8) {
#pragma unroll
        for (int s = 0; s < 4; ++s) qa[s] = qf[0][s];
      } else {
#pragma unroll
        for (int s = 0; s < 4; ++s) qa[s] = qf[1][s];
      }
      const int lb = rdl(len, b);
      soft_reset(s0);
      if (last) lds_wait_ge(ctl + C_RFREE, kx);
      if (early && cw >= vA) {
        lds_wait_ge(ctl + C_LANDED, 2 * cw + 2); ++j; lds_st(ctl + C_REL + 4 * cw, j);
      }
      for (int i = 4 * j + cw; i < i1; i += 4) { const int t0 = (lp0 + i - i0) * PAGE; page(std::false_type{}, qa, qa, lb, lb, t0, t0 + PAGE > lb); }
      if (kx > 0 && !last) lds_wait_ge(ctl + C_RFREE, kx);
      const float lt = xrow16_sum(s0.l);
      if ((c >> 1) == (b & 7)) {
        LAS unsigned char* r = lds + L_RECX + (cw * 2 + (c & 1)) * ROW;
#pragma unroll
        for (int blk = 0; blk < 8; ++blk) *(LAS f32x4*)(r + blk * 64 + g * 16) = s0.o[blk];
        if (g == 0) *(LAS f32x2*)(r + 512) = f32x2{s0.m, lt};
      }
      lds_drain();
      if (lane == 0) __hip_atomic_fetch_add((LAS int*)(ctl + C_RCNT), 1, __ATOMIC_RELAXED, __HIP_MEMORY_SCOPE_WORKGROUP);
      if (last) break;
      if (early) {
        if (ns > 0) {
          soft_reset(s0); soft_reset(s1);
          for (int i = cw; i < ns; i += 4) { const int t0 = (ss + i) * PAGE; page(std::true_type{}, qf[0], qf[1], len0, len1, t0, t0 + PAGE > minlen); }
          if (cw < ns) {
            s0.l = xrow16_sum(s0.l); s1.l = xrow16_sum(s1.l);
            if (cw > 0) lds_wait_ge(ctl + C_CSTEP, cw);
#pragma unroll
            for (int cg = 0; cg < 2; ++cg) { Soft& s = cg ? s1 : s0;
              LAS unsigned char* r = lds + L_MRG + (16 * cg + c) * ROW;
              if (cw > 0) {
                const f32x2 ml = *(LAS f32x2*)(r + 512);
                const float M = fmaxf(ml[0], s.m), f0 = mfac(ml[0], M), f1 = mfac(s.m, M);
#pragma unroll
                for (int blk = 0; blk < 8; ++blk) s.o[blk] = *(LAS f32x4*)(r + blk * 64 + g * 16) * f0 + s.o[blk] * f1;
                s.l = ml[1] * f0 + s.l * f1; s.m = M;
              }
#pragma unroll
              for (int blk = 0; blk < 8; ++blk) *(LAS f32x4*)(r + blk * 64 + g * 16) = s.o[blk];
              lds_drain();
              if (g == 0) *(LAS f32x2*)(r + 512) = f32x2{s.m, s.l};
            }
            lds_drain();
            lds_st(ctl + C_CSTEP, cw + 1);
          }
        }
      } else m &= m - 1;
    }
  } else {
    if (lane < 12) *(LAS int*)(ctl + 4 * lane) = 0;
    wg_barrier();
    lds_wait_ge(ctl + C_READY, 1);
    STAMP(2);
    const u32x4 pq = *(LAS u32x4*)(ctl + C_PTR);
    const u32x2 pws = *(LAS u32x2*)(ctl + C_PTR + 16);
    GAS u32x2* outp = (GAS u32x2*)(((uintptr_t)(unsigned)rfl((int)pq[3]) << 32) | (unsigned)rfl((int)pq[2]));
    unsigned char* ws = (unsigned char*)(((uintptr_t)(unsigned)rfl((int)pws[1]) << 32) | (unsigned)rfl((int)pws[0]));
    const unsigned long long epoch = __hip_atomic_load((gu64*)(ws + W_EPOCH), RLX_AGENT);
    const __amdgpu_buffer_rsrc_t prsrc = __builtin_amdgcn_make_buffer_rsrc(ws + W_PART, 0, PART_BYTES, 0x00020000);
    len_raw = *(LAS int*)(ctl + C_LENS + 4 * c);
    const Part p = partition(rfl(*(LAS int*)(ctl + C_P)));
    const unsigned long long tag = epoch + 1ull;
    const int ns = p.ns;
    const unsigned blk = w * 16 + h;
    const int r = lane >> 5, ch = lane & 31;
    float M = -INFINITY, L = 0.f; f32x4 o = {0.f, 0.f, 0.f, 0.f};
    unsigned m = p.xmask;
#pragma nounroll
    for (int kx = 0;; ++kx) {
      const bool early = kx == 0, last = !early && m == 0;
      if (kx == 1 && ns > 0 && !ABL_NOSVC && !ABL_NOSTORE) {
        lds_wait_ge(ctl + C_CSTEP, min(ns, NSW));
#pragma unroll 4
        for (int k = 0; k < 16; ++k) {
          const u32x4 v = *(LAS u32x4*)(lds + L_MRG + (2 * k + r) * ROW + ch * 16);
          __builtin_amdgcn_raw_buffer_store_b128(v, prsrc, k * 1024 + lane * 16, blk * PS_BYTES, 16); }
        if (lane < 32) { const u32x2 v = *(LAS u32x2*)(lds + L_MRG + lane * ROW + 512);
          __builtin_amdgcn_raw_buffer_store_b64(v, prsrc, 32 * 512 + lane * 8, blk * PS_BYTES, 16); }
      }
      if (last) {
        asm volatile("s_waitcnt vmcnt(0)" ::: "memory");
        if (lane == 0) __hip_atomic_store((gu64*)(ws + W_FLAG + blk * FLAG_STRIDE), tag, RLX_AGENT);
        STAMP(3);
        const int E_w = rdl(p.E, w), exc_w = rdl(p.exc, w);
        const bool sh_ov = c < p.nsh;
        const bool ex_ov = min(p.S + p.spare, E_w + exc_w) > max(p.S, E_w);
        unsigned cmask = (ABL_NOSVC || ABL_NOGATHER) ? 0u : (unsigned)(__ballot(sh_ov) & 0xFFFFull) | ((unsigned)(__ballot(ex_ov) & 0xFFFFull) << 16);
        if (cmask) lds_wait_ge(ctl + C_LANDED, (p.TL * POLLAT) >> 3);
        {
          const bool need = !ABL_NOSVC && !ABL_NOPOLL && lane < 16 && (sh_ov || ex_ov);
          gu64* f = (gu64*)(ws + W_FLAG + (size_t)(c * 16 + h) * FLAG_STRIDE);
#pragma nounroll
          for (unsigned spins = 0; spins < (1u << 20); ++spins) {
            const bool ok = !need || __hip_atomic_load(f, RLX_AGENT) == tag;
            if (__all(ok)) break;
            __builtin_amdgcn_s_sleep(POLLSLEEP);
          }
          asm volatile("" ::: "memory");
        }
        STAMP(4);
        const unsigned vso = lane < 16 ? (unsigned)(c * 16 + h) * PS_BYTES + (unsigned)(2 * w) * 512 : W_PX + ((unsigned)(c * 16 + h) * 16 + w) * PX_BYTES;
        const unsigned vsm = lane < 16 ? (unsigned)(c * 16 + h) * PS_BYTES + 32 * 512 + (unsigned)(2 * w) * 8 : vso + 1024;
        constexpr int GB = 6;
        while (cmask) {
          u32x4 po[GB]; u32x2 pml[GB]; bool pv[GB];
#pragma unroll
          for (int k = 0; k < GB; ++k) {
            pv[k] = cmask != 0;
            const int id = pv[k] ? __builtin_ctz(cmask) : 0; cmask &= cmask - 1;
            const unsigned so = pv[k] ? (unsigned)rdl((int)vso, id) : PART_BYTES, sm = pv[k] ? (unsigned)rdl((int)vsm, id) : PART_BYTES;
            po[k] = __builtin_amdgcn_raw_buffer_load_b128(prsrc, lane * 16 + so, 0, 16);
            pml[k] = __builtin_amdgcn_raw_buffer_load_b64(prsrc, r * 8 + sm, 0, 16);
          }
#pragma unroll
          for (int k = 0; k < GB; ++k) {
            const float mk = pv[k] ? __uint_as_float(pml[k][0]) : -INFINITY, lk = __uint_as_float(pml[k][1]);
            const float Mn = fmaxf(M, mk), f0 = mfac(M, Mn), f1 = mfac(mk, Mn);
            o = o * f0 + __builtin_bit_cast(f32x4, po[k]) * f1; L = L * f0 + lk * f1; M = Mn;
          }
        }
        if (blk == 0) {
#pragma nounroll
          for (unsigned spins = 0; spins < (1u << 20); ++spins) {
            bool ok = true;
#pragma unroll
            for (int k = 0; k < 4; ++k) ok &= __hip_atomic_load((gu64*)(ws + W_FLAG + (size_t)(k * 64 + lane) * FLAG_STRIDE), RLX_AGENT) == tag;
            if (__all(ok)) break;
            __builtin_amdgcn_s_sleep(8);
          }
          if (lane == 0) __hip_atomic_store((gu64*)(ws + W_EPOCH), tag, RLX_AGENT);
        }
        STAMP(5);
      }
      lds_wait_ge(ctl + C_RCNT, NSW * (kx + 1));
      f32x2 ml[4]; f32x4 ok[4];
#pragma unroll
      for (int k = 0; k < 4; ++k) { ml[k] = *(LAS f32x2*)(lds + L_RECX + (k * 2 + r) * ROW + 512); ok[k] = *(LAS f32x4*)(lds + L_RECX + (k * 2 + r) * ROW + ch * 16); }
      const bool mine = early || last;
      float Mx = mine ? M : -INFINITY;
#pragma unroll
      for (int k = 0; k < 4; ++k) Mx = fmaxf(Mx, ml[k][0]);
      const float fr = mine ? mfac(M, Mx) : 0.f;
      float Lx = L * fr; f32x4 ox = o * fr;
#pragma unroll
      for (int k = 0; k < 4; ++k) { const float f = mfac(ml[k][0], Mx); Lx = fmaf(f, ml[k][1], Lx); ox += ok[k] * f; }
      asm volatile("s_waitcnt lgkmcnt(0)" : "+v"(ox) :: "memory");
      if (last) {
        const float inv = __builtin_amdgcn_rcpf(Lx);
        outp[((w * HQ + 2 * h + r) * D + 4 * ch) >> 2] = u32x2{cvtpk(ox[0] * inv, ox[1] * inv), cvtpk(ox[2] * inv, ox[3] * inv)};
        break;
      }
      lds_st(ctl + C_RFREE, kx + 1);
      if (early) { M = Mx; L = Lx; o = ox; }
      else {
        const unsigned so = W_PX + (blk * 16 + __builtin_ctz(m)) * PX_BYTES;
        __builtin_amdgcn_raw_buffer_store_b128(__builtin_bit_cast(u32x4, ox), prsrc, lane * 16, so, 16);
        const f32x4 mlv = {__builtin_bit_cast(float, rdl(__builtin_bit_cast(int, Mx), 0)), __builtin_bit_cast(float, rdl(__builtin_bit_cast(int, Lx), 0)),
                           __builtin_bit_cast(float, rdl(__builtin_bit_cast(int, Mx), 32)), __builtin_bit_cast(float, rdl(__builtin_bit_cast(int, Lx), 32))};
        if (lane < 8) __builtin_amdgcn_raw_buffer_store_b128(__builtin_bit_cast(u32x4, mlv), prsrc, 1024 + lane * 16, so, 16);
        m &= m - 1;
      }
    }
    STAMP(6);
  }
}

extern "C" void kernel_launch(void* const* d_in, const int* in_sizes, int n_in, void* d_out, int out_size, void* d_ws, size_t ws_size, hipStream_t stream) {
  Args a;
  a.q = (const bf16x8*)d_in[0]; a.k = d_in[1]; a.v = d_in[2]; a.bt = (const int*)d_in[3]; a.lens = (const int*)d_in[4];
  a.out = (unsigned*)d_out; a.ws = (unsigned char*)d_ws; a.kv_bytes = (unsigned)in_sizes[1] * 2u; a.pad = 0;
  hipFuncSetAttribute((const void*)radix_attn, hipFuncAttributeMaxDynamicSharedMemorySize, LDS_BYTES);
  hipLaunchKernelGGL(radix_attn, dim3(256), dim3(NTHR), LDS_BYTES, stream, a);
}
```

```cpp
#include <hip/hip_runtime.h>
#include <hip/hip_bf16.h>
#include <stdint.h>
#include <type_traits>

constexpr int NSLOT = 16;
constexpr int INFL = 20;
constexpr int CLS01 = 4;
constexpr int SHCAP = 21;
constexpr int POLLAT = 9;

constexpr int B = 16, HQ = 32, HKV = 16, D = 128, PAGE = 16, NB = 256, LK = NB * PAGE;
constexpr int NA = 4;
constexpr int NSW = 4, NTHR = (NSW + 2) * 64;
constexpr float C2 = 0.088388347648318440f * 1.4426950408889634f;
constexpr unsigned PAGE_BYTES = PAGE * HKV * D * 2;
constexpr unsigned TOK_BYTES = HKV * D * 2;

constexpr int ROW = 528;
constexpr int L_RING = 0;
constexpr int L_MRG = NSLOT * 8192;
constexpr int L_TAB = L_RING + (NSLOT - 2) * 8192;
constexpr int L_RECX = L_MRG + 32 * ROW;
constexpr int L_MISC = L_RECX + 4 * 2 * ROW;
constexpr int C_LANDED = 0, C_CSTEP = 4, C_RCNT = 8, C_RFREE = 12, C_REL = 16, C_P = 32, C_READY = 40, C_PTR = 48, C_LENS = 80;
constexpr int LDS_BYTES = L_MISC + 160;
static_assert(NSLOT % 4 == 0 && (NSLOT & (NSLOT - 1)) == 0, "ring geometry");
static_assert(LDS_BYTES <= 160 * 1024, "LDS");

constexpr size_t W_EPOCH = 0, W_FLAG = 256, FLAG_STRIDE = 128;
constexpr size_t W_PART = 65536;
constexpr unsigned PS_BYTES = 32 * 512 + 256, PX_BYTES = 2 * 512 + 128;
constexpr unsigned W_PX = 256 * PS_BYTES;
constexpr unsigned PART_BYTES = W_PX + 256 * 16 * PX_BYTES;

typedef __bf16 bf16x8 __attribute__((ext_vector_type(8)));
typedef __bf16 bf16x2 __attribute__((ext_vector_type(2)));
typedef short s16x4 __attribute__((ext_vector_type(4)));
typedef float f32x4 __attribute__((ext_vector_type(4)));
typedef float f32x2 __attribute__((ext_vector_type(2)));
typedef unsigned u32x4 __attribute__((ext_vector_type(4)));
typedef unsigned u32x2 __attribute__((ext_vector_type(2)));
typedef int i32x4 __attribute__((ext_vector_type(4)));
#define LAS __attribute__((address_space(3)))
#define GAS __attribute__((address_space(1)))
#define DI __device__ __forceinline__
typedef GAS unsigned long long gu64;
#define RLX_AGENT __ATOMIC_RELAXED, __HIP_MEMORY_SCOPE_AGENT

DI int rfl(int x) { return __builtin_amdgcn_readfirstlane(x); }
DI int rdl(int x, int l) { return __builtin_amdgcn_readlane(x, l); }
template <int CTRL> DI int dpp_i(int oldv, int x) { return __builtin_amdgcn_update_dpp(oldv, x, CTRL, 0xf, 0xf, false); }
DI int row_scan_add(int x) {
  x += dpp_i<0x111>(0, x); x += dpp_i<0x112>(0, x); x += dpp_i<0x114>(0, x); x += dpp_i<0x118>(0, x); return x;
}
DI int row_scan_max(int x) {
  x = max(x, dpp_i<0x111>(0, x)); x = max(x, dpp_i<0x112>(0, x)); x = max(x, dpp_i<0x114>(0, x)); x = max(x, dpp_i<0x118>(0, x)); return x;
}
DI float xrow16_max(float x) {
  auto s = __builtin_amdgcn_permlane16_swap(__float_as_uint(x), __float_as_uint(x), false, false);
  x = fmaxf(__uint_as_float(s[0]), __uint_as_float(s[1]));
  auto t = __builtin_amdgcn_permlane32_swap(__float_as_uint(x), __float_as_uint(x), false, false);
  return fmaxf(__uint_as_float(t[0]), __uint_as_float(t[1]));
}
DI float xrow16_sum(float x) {
  auto s = __builtin_amdgcn_permlane16_swap(__float_as_uint(x), __float_as_uint(x), false, false);
  x = __uint_as_float(s[0]) + __uint_as_float(s[1]);
  auto t = __builtin_amdgcn_permlane32_swap(__float_as_uint(x), __float_as_uint(x), false, false);
  return __uint_as_float(t[0]) + __uint_as_float(t[1]);
}
DI unsigned cvtpk(float lo, float hi) { f32x2 v = {lo, hi}; bf16x2 b = __builtin_convertvector(v, bf16x2); return __builtin_bit_cast(unsigned, b); }
DI float ex2(float x) { return __builtin_amdgcn_exp2f(x); }
DI void wg_barrier() { asm volatile("s_waitcnt lgkmcnt(0)\n\ts_barrier" ::: "memory"); }
template <int N> DI void wait_vm() { asm volatile("s_waitcnt vmcnt(%0)" :: "i"(N) : "memory"); }

DI void set_m0(unsigned m0v, unsigned so) { asm volatile("s_mov_b32 m0, %0\n\ts_nop 3" :: "s"(m0v), "s"(so) : "memory"); }
template <int X> DI void wait_dma4(__amdgpu_buffer_rsrc_t rsrc, unsigned soff, unsigned v0, unsigned v1, unsigned v2, unsigned v3) {
  asm volatile("s_waitcnt vmcnt(%6)\n\t"
               "buffer_load_dwordx4 %0, %4, %5 offen nt lds\n\t"
               "buffer_load_dwordx4 %1, %4, %5 offen offset:1024 nt lds\n\t"
               "buffer_load_dwordx4 %2, %4, %5 offen offset:2048 nt lds\n\t"
               "buffer_load_dwordx4 %3, %4, %5 offen offset:3072 nt lds"
               :: "v"(v0), "v"(v1), "v"(v2), "v"(v3), "s"(rsrc), "s"(soff), "i"(X) : "memory");
}
DI void dma4r(__amdgpu_buffer_rsrc_t r0, __amdgpu_buffer_rsrc_t r1, __amdgpu_buffer_rsrc_t r2, __amdgpu_buffer_rsrc_t r3, unsigned soff, unsigned v0, unsigned v1, unsigned v2, unsigned v3) {
  asm volatile("s_nop 0\n\t"
               "buffer_load_dwordx4 %0, %4, %8 offen nt lds\n\t"
               "buffer_load_dwordx4 %1, %5, %8 offen offset:1024 nt lds\n\t"
               "buffer_load_dwordx4 %2, %6, %8 offen offset:2048 nt lds\n\t"
               "buffer_load_dwordx4 %3, %7, %8 offen offset:3072 nt lds"
               :: "v"(v0), "v"(v1), "v"(v2), "v"(v3), "s"(r0), "s"(r1), "s"(r2), "s"(r3), "s"(soff) : "memory");
}
#define WAIT_K(X, so) wait_dma4<X>(rk, so, vk[0], vk[1], vk[2], vk[3])
#define WAIT_V(X, so) wait_dma4<X>(rv, so, vv[0], vv[1], vv[2], vv[3])

template <int X>
DI void page_asm(__amdgpu_buffer_rsrc_t rk, __amdgpu_buffer_rsrc_t rv, const unsigned (&vk)[4], const unsigned (&vv)[4], unsigned vctl, unsigned vp,
                 int& i, int& pub, unsigned& so, unsigned& mk) {
  unsigned vt;
  asm volatile("s_waitcnt vmcnt(%[X])\n\t"
               "buffer_load_dwordx4 %[k0], %[rk], %[so] offen nt lds\n\t"
               "buffer_load_dwordx4 %[k1], %[rk], %[so] offen offset:1024 nt lds\n\t"
               "buffer_load_dwordx4 %[k2], %[rk], %[so] offen offset:2048 nt lds\n\t"
               "buffer_load_dwordx4 %[k3], %[rk], %[so] offen offset:3072 nt lds\n\t"
               "v_mov_b32 %[vt], %[pub]\n\t"
               "s_add_u32 m0, m0, 0x1000\n\t"
               "ds_write_b32 %[vctl], %[vt]\n\t"
               "s_add_u32 %[pub], %[pub], 1\n\t"
               "s_waitcnt vmcnt(%[X])\n\t"
               "buffer_load_dwordx4 %[v0], %[rv], %[so] offen nt lds\n\t"
               "buffer_load_dwordx4 %[v1], %[rv], %[so] offen offset:1024 nt lds\n\t"
               "buffer_load_dwordx4 %[v2], %[rv], %[so] offen offset:2048 nt lds\n\t"
               "buffer_load_dwordx4 %[v3], %[rv], %[so] offen offset:3072 nt lds\n\t"
               "v_mov_b32 %[vt], %[pub]\n\t"
               "s_add_u32 %[i], %[i], 1\n\t"
               "ds_write_b32 %[vctl], %[vt]\n\t"
               "v_readlane_b32 %[so], %[vp], %[i]\n\t"
               "s_add_u32 %[pub], %[pub], 1\n\t"
               "s_add_u32 %[mk], %[mk], 0x2000\n\t"
               "s_and_b32 %[mk], %[mk], %[msk]\n\t"
               "s_mov_b32 m0, %[mk]"
               : [i] "+s"(i), [pub] "+s"(pub), [so] "+s"(so), [mk] "+s"(mk), [vt] "=&v"(vt)
               : [X] "i"(X), [msk] "i"(NSLOT * 8192 - 1), [k0] "v"(vk[0]), [k1] "v"(vk[1]), [k2] "v"(vk[2]), [k3] "v"(vk[3]), [v0] "v"(vv[0]), [v1] "v"(vv[1]), [v2] "v"(vv[2]), [v3] "v"(vv[3]),
                 [rk] "s"(rk), [rv] "s"(rv), [vctl] "v"(vctl), [vp] "v"(vp) : "memory", "scc");
}

struct Args { const bf16x8* q; const void* k; const void* v; const int* bt; const int* lens; unsigned* out; unsigned char* ws; unsigned kv_bytes; unsigned pad; };

DI float mfac(float m, float M) { return (m == -INFINITY) ? 0.f : ex2((m - M) * C2); }

struct Soft { float m, l; f32x4 o[8]; };
DI void soft_reset(Soft& s) { s.m = -INFINITY; s.l = 0.f;
#pragma unroll
  for (int i = 0; i < 8; ++i) s.o[i] = f32x4{0.f, 0.f, 0.f, 0.f}; }

DI s16x4 soft_step(Soft& s, f32x4 x, bool mask, int tok0, int len) {
  if (mask) {
#pragma unroll
    for (int r = 0; r < 4; ++r) x[r] = (tok0 + r < len) ? x[r] : -INFINITY;
  }
  float pm = fmaxf(fmaxf(x[0], x[1]), fmaxf(x[2], x[3]));
  pm = xrow16_max(pm);
  const float mn = fmaxf(s.m, pm);
  const float ms = (mn == -INFINITY) ? 0.f : mn * C2;
  const float alpha = ex2(s.m * C2 - ms);
  float p[4];
#pragma unroll
  for (int r = 0; r < 4; ++r) p[r] = ex2(fmaf(x[r], C2, -ms));
  s.l = fmaf(s.l, alpha, (p[0] + p[1]) + (p[2] + p[3]));
  s.m = mn;
#pragma unroll
  for (int i = 0; i < 8; ++i) s.o[i] *= alpha;
  u32x2 pk = {cvtpk(p[0], p[1]), cvtpk(p[2], p[3])};
  return __builtin_bit_cast(s16x4, pk);
}


DI int lds_ld(LAS unsigned char* p) { return __hip_atomic_load((LAS int*)p, __ATOMIC_RELAXED, __HIP_MEMORY_SCOPE_WORKGROUP); }
DI void lds_st(LAS unsigned char* p, int v) { __hip_atomic_store((LAS int*)p, v, __ATOMIC_RELAXED, __HIP_MEMORY_SCOPE_WORKGROUP); }
DI int lds_wait_ge(LAS unsigned char* p, int need) {
  int v;
#pragma nounroll
  for (unsigned spins = 0;; ++spins) { v = rfl(lds_ld(p)); if (v >= need || spins > (1u << 20)) break; __builtin_amdgcn_s_sleep(1); }
  asm volatile("" ::: "memory");
  return v;
}
DI void lds_drain() { asm volatile("s_waitcnt lgkmcnt(0)" ::: "memory"); }

__global__ void __launch_bounds__(NTHR, 2) radix_attn(Args) {
  extern __shared__ __attribute__((aligned(16))) unsigned char lds_raw[];
  LAS unsigned char* lds = (LAS unsigned char*)lds_raw;
  LAS unsigned char* ctl = lds + L_MISC;
  const int tid = threadIdx.x, lane = tid & 63, wave = rfl(tid >> 6);
  const int w = blockIdx.x & 15, h = blockIdx.x >> 4;
  const int c = lane & 15, g = lane >> 4;
  int len_raw;

  struct Part { int len, Pe, S, E, spare, exc, lo, hi, xoff, S_w, spare_w, T_w, ns, ss, hp, nsh, np_w, own_w, vA, TL; unsigned xmask; bool all_masked; };
  auto partition = [&](int P) {
    Part p;
    p.all_masked = len_raw <= 0;
    p.len = p.all_masked ? LK : min(len_raw, LK);
    const int np = (p.len + PAGE - 1) >> 4;
    p.Pe = min(P, rdl(row_scan_max(np), 15));
    const int n = max(np - p.Pe, 0);
    const int W = p.Pe + rdl(row_scan_add(n), 15);
    const int T = (W >> 4) + (c < (W & 15) ? 1 : 0);
    const int cap = max(1, ((W >> 4) * SHCAP) >> 6);
    int lg = 0;
    if (p.Pe > cap) lg = 1;
    if (p.Pe > 2 * cap) lg = 2;
    if (p.Pe > 4 * cap) lg = 3;
    if (p.Pe > 8 * cap) lg = 4;
    const int q = p.Pe >> lg, rq = p.Pe & ((1 << lg) - 1);
    const int sh = c < (1 << lg) ? q + (c < rq ? 1 : 0) : 0;
    p.nsh = min(1 << lg, p.Pe);
    p.ns = w < (1 << lg) ? q + (w < rq ? 1 : 0) : 0;
    p.ss = w * q + min(w, rq);
    const int own = min(n, T - sh);
    p.exc = n - own; p.spare = T - sh - own;
    p.S = row_scan_add(p.spare) - p.spare;
    p.E = row_scan_add(p.exc) - p.exc;
    p.S_w = rdl(p.S, w); p.spare_w = rdl(p.spare, w); p.T_w = rdl(T, w);
    p.hp = p.ns + p.spare_w;
    p.lo = max(p.S_w, p.E); p.hi = min(p.S_w + p.spare_w, p.E + p.exc);
    p.xmask = (unsigned)(__ballot(p.hi > p.lo) & 0xFFFFull);
    p.xoff = p.Pe - p.E;
    p.np_w = rdl(np, w); p.own_w = rdl(own, w);
    p.vA = min(NA, p.own_w);
    p.TL = NA + p.hp + p.own_w - p.vA;
    return p;
  };

  if (__builtin_expect(wave == 0, 1)) {
    __builtin_amdgcn_s_setprio(3);
    typedef unsigned u32x16 __attribute__((ext_vector_type(16)));
    u32x16 ka = *(const __attribute__((address_space(4))) u32x16*)__builtin_amdgcn_kernarg_segment_ptr();
    asm volatile("" : "+s"(ka));
    Args a;
    a.q = (const bf16x8*)(((uintptr_t)ka[1] << 32) | ka[0]); a.k = (const void*)(((uintptr_t)ka[3] << 32) | ka[2]); a.v = (const void*)(((uintptr_t)ka[5] << 32) | ka[4]);
    a.bt = (const int*)(((uintptr_t)ka[7] << 32) | ka[6]); a.lens = (const int*)(((uintptr_t)ka[9] << 32) | ka[8]); a.out = (unsigned*)(((uintptr_t)ka[11] << 32) | ka[10]);
    a.ws = (unsigned char*)(((uintptr_t)ka[13] << 32) | ka[12]); a.kv_bytes = ka[14];
    len_raw = *(const GAS int*)((uintptr_t)a.lens + 4 * c);
    const GAS i32x4* bt4 = (const GAS i32x4*)(uintptr_t)a.bt;
    i32x4 rk16[16];
#pragma unroll
    for (int k = 0; k < 16; ++k) rk16[k] = bt4[k * (NB / 4) + lane];
    asm volatile("s_barrier" ::: "memory");
    if (lane == 0) { *(LAS u32x4*)(ctl + C_PTR) = u32x4{(unsigned)(uintptr_t)a.q, (unsigned)((uintptr_t)a.q >> 32), (unsigned)(uintptr_t)a.out, (unsigned)((uintptr_t)a.out >> 32)};
                     *(LAS u32x2*)(ctl + C_PTR + 16) = u32x2{(unsigned)(uintptr_t)a.ws, (unsigned)((uintptr_t)a.ws >> 32)}; }
    unsigned vk[4], vv[4];
#pragma unroll
    for (int i = 0; i < 4; ++i) { const int t = 4 * i + g;
      vk[i] = t * TOK_BYTES + ((c ^ t) << 4) - 1024 * i;
      vv[i] = t * TOK_BYTES + ((c ^ ((t & 7) << 1)) << 4) - 1024 * i; }
    asm volatile("" : "+v"(vk[0]), "+v"(vk[1]), "+v"(vk[2]), "+v"(vk[3]), "+v"(vv[0]), "+v"(vv[1]), "+v"(vv[2]), "+v"(vv[3]));
    const unsigned ring = (unsigned)(uintptr_t)(lds + L_RING);
    const __amdgpu_buffer_rsrc_t rk = __builtin_amdgcn_make_buffer_rsrc((void*)a.k, 0, a.kv_bytes, 0x00020000);
    const __amdgpu_buffer_rsrc_t rv = __builtin_amdgcn_make_buffer_rsrc((void*)a.v, 0, a.kv_bytes, 0x00020000);
#pragma unroll
    for (int k = 0; k < 16; ++k) *(LAS i32x4*)(lds + L_TAB + (k * NB + 4 * lane) * 4) = rk16[k];
    unsigned vp0, vp1 = 0, vp2 = 0, vp3 = 0;
    int nq0;
    {
      const int lw = rdl(len_raw, w);
      const int npw = ((lw <= 0 ? LK : min(lw, LK)) + PAGE - 1) >> 4;
      vp0 = (unsigned)*(LAS int*)(lds + L_TAB + 4 * (w * NB + max(npw - 1 - (lane & 3), 0))) * PAGE_BYTES + (unsigned)h * (D * 2);
      nq0 = ((lw <= 0 ? LK : min(lw, LK)) - (npw - 1) * PAGE + 3) >> 2;
    }
    unsigned so = (unsigned)rdl((int)vp0, 0), m0k = ring;
    {
      const __amdgpu_buffer_rsrc_t k1 = __builtin_amdgcn_make_buffer_rsrc((void*)a.k, 0, nq0 > 1 ? a.kv_bytes : 0u, 0x00020000), k2 = __builtin_amdgcn_make_buffer_rsrc((void*)a.k, 0, nq0 > 2 ? a.kv_bytes : 0u, 0x00020000),
                                   k3 = __builtin_amdgcn_make_buffer_rsrc((void*)a.k, 0, nq0 > 3 ? a.kv_bytes : 0u, 0x00020000);
      set_m0(m0k, so);
      dma4r(rk, k1, k2, k3, so, vk[0], vk[1], vk[2], vk[3]);
      const __amdgpu_buffer_rsrc_t v1 = __builtin_amdgcn_make_buffer_rsrc((void*)a.v, 0, nq0 > 1 ? a.kv_bytes : 0u, 0x00020000), v2 = __builtin_amdgcn_make_buffer_rsrc((void*)a.v, 0, nq0 > 2 ? a.kv_bytes : 0u, 0x00020000),
                                   v3 = __builtin_amdgcn_make_buffer_rsrc((void*)a.v, 0, nq0 > 3 ? a.kv_bytes : 0u, 0x00020000);
      set_m0(m0k + 4096, so);
      dma4r(rv, v1, v2, v3, so, vv[0], vv[1], vv[2], vv[3]);
      so = (unsigned)rdl((int)vp0, 1); m0k = ring + 8192;
      set_m0(m0k, so);
    }
#pragma nounroll
    for (int i = 1; i < 3; ++i) {
      WAIT_K(63, so);
      set_m0(m0k + 4096, so);
      WAIT_V(63, so);
      so = (unsigned)rdl((int)vp0, i + 1);
      m0k = ring + (i + 1) * 8192;
      set_m0(m0k, so);
    }
    i32x4 acc = rk16[1] ^ rk16[0];
#pragma unroll
    for (int k = 2; k < 16; ++k) acc |= rk16[k] ^ rk16[0];
    int P;
    {
      const unsigned long long b0 = __ballot(acc[0] != 0), b1 = __ballot(acc[1] != 0), b2 = __ballot(acc[2] != 0), b3 = __ballot(acc[3] != 0);
      const unsigned long long any = b0 | b1 | b2 | b3;
      const int l = any ? (int)__builtin_ctzll(any) : 64;
      const unsigned long long bit = any & (0ull - any);
      P = 4 * l + ((b0 & bit) ? 0 : (b1 & bit) ? 1 : (b2 & bit) ? 2 : (b3 & bit) ? 3 : 0);
    }
    if (lane == 0) *(LAS int*)(ctl + C_P) = P;
    if (lane < 16) *(LAS int*)(ctl + C_LENS + 4 * lane) = len_raw;
    lds_drain();
    lds_st(ctl + C_READY, 1);
    const Part p = partition(P);
    const int S_w = p.S_w - p.ns - NA, hp = p.hp, ns = p.ns, ss = p.ss, T_w = p.TL, lo = p.lo, hi = p.hi, xoff = p.xoff; const unsigned xmask = p.xmask;
    const int lpA = p.np_w - 1, lpB = p.np_w - p.own_w;
    auto page_src = [&](int r) {
      const int i = 64 * r + lane;
      int idx = (i < NA) ? w * NB + max(lpA - i, 0) : (i < NA + ns) ? ss + (i - NA) : w * NB + lpB + (i - NA - hp);
      for (unsigned m = xmask; m; m &= m - 1) {
        const int b = __builtin_ctz(m);
        const int lo_b = rdl(lo, b) - S_w, hi_b = rdl(hi, b) - S_w, off_b = rdl(xoff, b) + b * NB + S_w;
        if (i >= lo_b && i < hi_b) idx = i + off_b;
      }
      return (unsigned)*(LAS int*)(lds + L_TAB + 4 * (i < T_w ? idx : 0)) * PAGE_BYTES + (unsigned)h * (D * 2);
    };
    vp0 = page_src(0); vp1 = page_src(1); vp2 = page_src(2); vp3 = page_src(3);
    asm volatile("" : "+v"(vp0), "+v"(vp1), "+v"(vp2), "+v"(vp3));
    static_assert(L_RING == 0 && C_LANDED == 0 && C_REL == 16, "page_asm addresses the ring from LDS offset 0 and the control words by these offsets");
    const unsigned vctl = (unsigned)(uintptr_t)ctl;
    auto publish = [&](int nl) { lds_st(ctl + C_LANDED, nl); };
    const bool wide = ((((unsigned)(uintptr_t)a.k >> 8) + h) & 3) == 1;
    const int TM = T_w >= 8 ? T_w - 4 : T_w;
    int i = 3, credit = NSLOT;
    unsigned mk = m0k;
    for (;;) {
      const int iend = min(min(TM, credit), (i & ~63) + 64);
      if (wide) { int pub = 2 * i - (INFL + CLS01 + 3) / 4;
#pragma nounroll
        while (i < iend) page_asm<INFL + CLS01>(rk, rv, vk, vv, vctl, vp0, i, pub, so, mk);
      } else { int pub = 2 * i - (INFL + 3) / 4;
#pragma nounroll
        while (i < iend) page_asm<INFL>(rk, rv, vk, vv, vctl, vp0, i, pub, so, mk);
      }
      if (i >= T_w) break;
      const bool endgroup = i >= TM;
      const u32x4 rel = *(volatile LAS u32x4*)(ctl + C_REL);
      credit = min(min(4 * rfl((int)rel[0]), 4 * rfl((int)rel[1]) + 1), min(4 * rfl((int)rel[2]) + 2, 4 * rfl((int)rel[3]) + 3)) + NSLOT;
      if ((i & 63) == 0) { vp0 = vp1; vp1 = vp2; vp2 = vp3; so = (unsigned)rdl((int)vp0, 0); asm volatile("s_nop 4" :: "s"(so)); }
      if (credit <= i) {
        LAS unsigned char* relp = ctl + C_REL + 4 * (i & 3);
        const int need = (i >> 2) - NSLOT / 4 + 1;
        if (rfl(lds_ld(relp)) < need) { wait_vm<12>(); publish(2 * i - 3);
          if (rfl(lds_ld(relp)) < need) { wait_vm<4>(); publish(2 * i - 1);
            if (rfl(lds_ld(relp)) < need) { wait_vm<0>(); publish(2 * i); lds_wait_ge(relp, need); } } }
        credit = i + 1;
      }
      if (endgroup) {
        if (credit < T_w) { wait_vm<0>(); publish(2 * i);
          for (int e = i; e < T_w; ++e) if (e >= NSLOT) lds_wait_ge(ctl + C_REL + 4 * (e & 3), (e >> 2) - NSLOT / 4 + 1); }
        unsigned se[4];
        int pub = 2 * i - (INFL + 3) / 4;
#pragma unroll
        for (int r = 0; r < 4; ++r) {
          if (r > 0) { if (((i + r) & 63) == 0) { vp0 = vp1; vp1 = vp2; } so = (unsigned)rdl((int)vp0, (i + r) & 63); }
          se[r] = so;
          set_m0(((i + r) & (NSLOT - 1)) * 8192, so);
          WAIT_K(INFL, so);
          publish(pub + r);
        }
#pragma unroll
        for (int r = 0; r < 4; ++r) {
          set_m0(((i + r) & (NSLOT - 1)) * 8192 + 4096, se[r]);
          WAIT_V(INFL, se[r]);
          publish(pub + 4 + r);
        }
        break;
      }
    }
    wait_vm<24>(); publish(2 * T_w - 6);
    wait_vm<20>(); publish(2 * T_w - 5);
    wait_vm<16>(); publish(2 * T_w - 4);
    wait_vm<12>(); publish(2 * T_w - 3);
    wait_vm<8>(); publish(2 * T_w - 2);
    wait_vm<4>(); publish(2 * T_w - 1);
    wait_vm<0>(); publish(2 * T_w);
  } else if (wave <= NSW) {
    const int cw = wave - 1;
    wg_barrier();
    const unsigned ka0 = c * 256 + ((g ^ c) << 4);
    const int tv = 4 * g + (c >> 2);
    const unsigned va0 = 4096 + tv * 256 + ((tv & 7) << 5) + (c & 3) * 8;
    lds_wait_ge(ctl + C_READY, 1);
    bf16x8 qf[2][4];
    {
      const u32x2 pq = *(LAS u32x2*)(ctl + C_PTR);
      const GAS bf16x8* qp = (const GAS bf16x8*)(((uintptr_t)(unsigned)rfl((int)pq[1]) << 32) | (unsigned)rfl((int)pq[0]));
#pragma unroll
      for (int cg = 0; cg < 2; ++cg)
#pragma unroll
        for (int s = 0; s < 4; ++s) { const int r = 16 * cg + c; qf[cg][s] = qp[(((r >> 1) * HQ + 2 * h + (r & 1)) * D + 32 * s + 8 * g) / 8]; }
    }
    len_raw = *(LAS int*)(ctl + C_LENS + 4 * c);
    const Part p = partition(rfl(*(LAS int*)(ctl + C_P)));
    const int len = p.len, S_w = p.S_w - p.ns - NA, hp = p.hp, ss = p.ss, T_w = p.TL, lo = p.lo, hi = p.hi, xoff = p.xoff, ns = p.ns, lpA = p.np_w - 1, lpB = p.np_w - p.own_w, vA = p.vA;
    const int minlen = LK - rdl(row_scan_max(LK - len), 15);
    const int len0 = __shfl(len, c >> 1), len1 = __shfl(len, 8 + (c >> 1));
    {
      const bool z0 = __shfl((int)p.all_masked, c >> 1) != 0, z1 = __shfl((int)p.all_masked, 8 + (c >> 1)) != 0;
#pragma unroll
      for (int s = 0; s < 4; ++s) {
        u32x4 t0 = __builtin_bit_cast(u32x4, qf[0][s]), t1 = __builtin_bit_cast(u32x4, qf[1][s]);
#pragma unroll
        for (int e = 0; e < 4; ++e) { t0[e] = z0 ? 0u : t0[e]; t1[e] = z1 ? 0u : t1[e]; }
        qf[0][s] = __builtin_bit_cast(bf16x8, t0); qf[1][s] = __builtin_bit_cast(bf16x8, t1);
      }
    }
    const int TM = T_w >= 8 ? T_w - 4 : T_w + 4;
    int j = 0, seen = 0;
    Soft s0, s1;
    auto page = [&](auto SHc, const bf16x8 (&qa)[4], const bf16x8 (&qb)[4], int la, int lb, int t0, bool mask) {
      constexpr bool SH = decltype(SHc)::value;
      const int i = 4 * j + cw;
      LAS unsigned char* sl = lds + L_RING + (i & (NSLOT - 1)) * 8192;
      const int re = i - TM;
      const int needk = re >= 0 ? 2 * TM + re + 1 : 2 * i + 1, needv = re >= 0 ? 2 * TM + re + 5 : 2 * i + 2;
      if (seen < needk) seen = lds_wait_ge(ctl + C_LANDED, needk);
      bf16x8 kf[4];
#pragma unroll
      for (int s = 0; s < 4; ++s) kf[s] = *(LAS bf16x8*)(sl + (ka0 ^ (s << 6)));
      f32x4 xa = {0.f, 0.f, 0.f, 0.f}, xb = {0.f, 0.f, 0.f, 0.f};
#pragma unroll
      for (int s = 0; s < 4; ++s) { xa = __builtin_amdgcn_mfma_f32_16x16x32_bf16(kf[s], qa[s], xa, 0, 0, 0);
        if (SH) xb = __builtin_amdgcn_mfma_f32_16x16x32_bf16(kf[s], qb[s], xb, 0, 0, 0); }
      const s16x4 pa = soft_step(s0, xa, mask, t0 + 4 * g, la);
      s16x4 pb; if (SH) pb = soft_step(s1, xb, mask, t0 + 4 * g, lb);
      if (seen < needv) seen = lds_wait_ge(ctl + C_LANDED, needv);
      s16x4 vf[8];
#pragma unroll
      for (int blk = 0; blk < 8; ++blk) vf[blk] = __builtin_amdgcn_ds_read_tr16_b64_v4i16((LAS s16x4*)(sl + (va0 ^ (blk << 5))));
      ++j;
      asm volatile("s_waitcnt lgkmcnt(0)" : "+v"(vf[0]), "+v"(vf[1]), "+v"(vf[2]), "+v"(vf[3]), "+v"(vf[4]), "+v"(vf[5]), "+v"(vf[6]), "+v"(vf[7]) :: "memory");
      lds_st(ctl + C_REL + 4 * cw, j);
#pragma unroll
      for (int blk = 0; blk < 8; ++blk) { s0.o[blk] = __builtin_amdgcn_mfma_f32_16x16x16bf16_1k(vf[blk], pa, s0.o[blk], 0, 0, 0);
        if (SH) s1.o[blk] = __builtin_amdgcn_mfma_f32_16x16x16bf16_1k(vf[blk], pb, s1.o[blk], 0, 0, 0); }
    };
    unsigned m = p.xmask;
    for (int kx = 0;; ++kx) {
      const bool early = kx == 0, last = !early && m == 0;
      const int b = (early || last) ? w : __builtin_ctz(m);
      const int i0 = early ? 0 : last ? NA + hp : rdl(lo, b) - S_w;
      const int i1 = early ? NA : last ? T_w : rdl(hi, b) - S_w;
      const int lp0 = early ? lpA - 2 * cw : last ? lpB : rdl(lo, b) + rdl(xoff, b);
      bf16x8 qa[4];
      if (b < 8) {
#pragma unroll
        for (int s = 0; s < 4; ++s) qa[s] = qf[0][s];
      } else {
#pragma unroll
        for (int s = 0; s < 4; ++s) qa[s] = qf[1][s];
      }
      const int lb = rdl(len, b);
      soft_reset(s0);
      if (last) lds_wait_ge(ctl + C_RFREE, kx);
      if (early && cw >= vA) {
        lds_wait_ge(ctl + C_LANDED, 2 * cw + 2); ++j; lds_st(ctl + C_REL + 4 * cw, j);
      }
      for (int i = 4 * j + cw; i < i1; i += 4) { const int t0 = (lp0 + i - i0) * PAGE; page(std::false_type{}, qa, qa, lb, lb, t0, t0 + PAGE > lb); }
      if (kx > 0 && !last) lds_wait_ge(ctl + C_RFREE, kx);
      const float lt = xrow16_sum(s0.l);
      if ((c >> 1) == (b & 7)) {
        LAS unsigned char* r = lds + L_RECX + (cw * 2 + (c & 1)) * ROW;
#pragma unroll
        for (int blk = 0; blk < 8; ++blk) *(LAS f32x4*)(r + blk * 64 + g * 16) = s0.o[blk];
        if (g == 0) *(LAS f32x2*)(r + 512) = f32x2{s0.m, lt};
      }
      lds_drain();
      if (lane == 0) __hip_atomic_fetch_add((LAS int*)(ctl + C_RCNT), 1, __ATOMIC_RELAXED, __HIP_MEMORY_SCOPE_WORKGROUP);
      if (last) break;
      if (early) {
        if (ns > 0) {
          soft_reset(s0); soft_reset(s1);
          for (int i = cw; i < ns; i += 4) { const int t0 = (ss + i) * PAGE; page(std::true_type{}, qf[0], qf[1], len0, len1, t0, t0 + PAGE > minlen); }
          if (cw < ns) {
            s0.l = xrow16_sum(s0.l); s1.l = xrow16_sum(s1.l);
            if (cw > 0) lds_wait_ge(ctl + C_CSTEP, cw);
#pragma unroll
            for (int cg = 0; cg < 2; ++cg) { Soft& s = cg ? s1 : s0;
              LAS unsigned char* r = lds + L_MRG + (16 * cg + c) * ROW;
              if (cw > 0) {
                const f32x2 ml = *(LAS f32x2*)(r + 512);
                const float M = fmaxf(ml[0], s.m), f0 = mfac(ml[0], M), f1 = mfac(s.m, M);
#pragma unroll
                for (int blk = 0; blk < 8; ++blk) s.o[blk] = *(LAS f32x4*)(r + blk * 64 + g * 16) * f0 + s.o[blk] * f1;
                s.l = ml[1] * f0 + s.l * f1; s.m = M;
              }
#pragma unroll
              for (int blk = 0; blk < 8; ++blk) *(LAS f32x4*)(r + blk * 64 + g * 16) = s.o[blk];
              lds_drain();
              if (g == 0) *(LAS f32x2*)(r + 512) = f32x2{s.m, s.l};
            }
            lds_drain();
            lds_st(ctl + C_CSTEP, cw + 1);
          }
        }
      } else m &= m - 1;
    }
  } else {
    if (lane < 12) *(LAS int*)(ctl + 4 * lane) = 0;
    wg_barrier();
    lds_wait_ge(ctl + C_READY, 1);
    const u32x4 pq = *(LAS u32x4*)(ctl + C_PTR);
    const u32x2 pws = *(LAS u32x2*)(ctl + C_PTR + 16);
    GAS u32x2* outp = (GAS u32x2*)(((uintptr_t)(unsigned)rfl((int)pq[3]) << 32) | (unsigned)rfl((int)pq[2]));
    unsigned char* ws = (unsigned char*)(((uintptr_t)(unsigned)rfl((int)pws[1]) << 32) | (unsigned)rfl((int)pws[0]));
    const unsigned long long epoch = __hip_atomic_load((gu64*)(ws + W_EPOCH), RLX_AGENT);
    const __amdgpu_buffer_rsrc_t prsrc = __builtin_amdgcn_make_buffer_rsrc(ws + W_PART, 0, PART_BYTES, 0x00020000);
    len_raw = *(LAS int*)(ctl + C_LENS + 4 * c);
    const Part p = partition(rfl(*(LAS int*)(ctl + C_P)));
    const unsigned long long tag = epoch + 1ull;
    const int ns = p.ns;
    const unsigned blk = w * 16 + h;
    const int r = lane >> 5, ch = lane & 31;
    float M = -INFINITY, L = 0.f; f32x4 o = {0.f, 0.f, 0.f, 0.f};
    unsigned m = p.xmask;
#pragma nounroll
    for (int kx = 0;; ++kx) {
      const bool early = kx == 0, last = !early && m == 0;
      if (kx == 1 && ns > 0) {
        lds_wait_ge(ctl + C_CSTEP, min(ns, NSW));
#pragma unroll 4
        for (int k = 0; k < 16; ++k) {
          const u32x4 v = *(LAS u32x4*)(lds + L_MRG + (2 * k + r) * ROW + ch * 16);
          __builtin_amdgcn_raw_buffer_store_b128(v, prsrc, k * 1024 + lane * 16, blk * PS_BYTES, 16); }
        if (lane < 32) { const u32x2 v = *(LAS u32x2*)(lds + L_MRG + lane * ROW + 512);
          __builtin_amdgcn_raw_buffer_store_b64(v, prsrc, 32 * 512 + lane * 8, blk * PS_BYTES, 16); }
      }
      if (last) {
        asm volatile("s_waitcnt vmcnt(0)" ::: "memory");
        if (lane == 0) __hip_atomic_store((gu64*)(ws + W_FLAG + blk * FLAG_STRIDE), tag, RLX_AGENT);
        const int E_w = rdl(p.E, w), exc_w = rdl(p.exc, w);
        const bool sh_ov = c < p.nsh;
        const bool ex_ov = min(p.S + p.spare, E_w + exc_w) > max(p.S, E_w);
        unsigned cmask = (unsigned)(__ballot(sh_ov) & 0xFFFFull) | ((unsigned)(__ballot(ex_ov) & 0xFFFFull) << 16);
        if (cmask) lds_wait_ge(ctl + C_LANDED, (p.TL * POLLAT) >> 3);
        {
          const bool need = lane < 16 && (sh_ov || ex_ov);
          gu64* f = (gu64*)(ws + W_FLAG + (size_t)(c * 16 + h) * FLAG_STRIDE);
#pragma nounroll
          for (unsigned spins = 0; spins < (1u << 16); ++spins) {
            const bool ok = !need || __hip_atomic_load(f, RLX_AGENT) == tag;
            if (__all(ok)) break;
            __builtin_amdgcn_s_sleep(32);
          }
          asm volatile("" ::: "memory");
        }
        const unsigned vso = lane < 16 ? (unsigned)(c * 16 + h) * PS_BYTES + (unsigned)(2 * w) * 512 : W_PX + ((unsigned)(c * 16 + h) * 16 + w) * PX_BYTES;
        const unsigned vsm = lane < 16 ? (unsigned)(c * 16 + h) * PS_BYTES + 32 * 512 + (unsigned)(2 * w) * 8 : vso + 1024;
        constexpr int GB = 6;
        while (cmask) {
          u32x4 po[GB]; u32x2 pml[GB]; bool pv[GB];
#pragma unroll
          for (int k = 0; k < GB; ++k) {
            pv[k] = cmask != 0;
            const int id = pv[k] ? __builtin_ctz(cmask) : 0; cmask &= cmask - 1;
            const unsigned so = pv[k] ? (unsigned)rdl((int)vso, id) : PART_BYTES, sm = pv[k] ? (unsigned)rdl((int)vsm, id) : PART_BYTES;
            po[k] = __builtin_amdgcn_raw_buffer_load_b128(prsrc, lane * 16 + so, 0, 16);
            pml[k] = __builtin_amdgcn_raw_buffer_load_b64(prsrc, r * 8 + sm, 0, 16);
          }
#pragma unroll
          for (int k = 0; k < GB; ++k) {
            const float mk = pv[k] ? __uint_as_float(pml[k][0]) : -INFINITY, lk = __uint_as_float(pml[k][1]);
            const float Mn = fmaxf(M, mk), f0 = mfac(M, Mn), f1 = mfac(mk, Mn);
            o = o * f0 + __builtin_bit_cast(f32x4, po[k]) * f1; L = L * f0 + lk * f1; M = Mn;
          }
        }
        if (blk == 0) {
#pragma nounroll
          for (unsigned spins = 0; spins < (1u << 16); ++spins) {
            bool ok = true;
#pragma unroll
            for (int k = 0; k < 4; ++k) ok &= __hip_atomic_load((gu64*)(ws + W_FLAG + (size_t)(k * 64 + lane) * FLAG_STRIDE), RLX_AGENT) == tag;
            if (__all(ok)) break;
            __builtin_amdgcn_s_sleep(8);
          }
          if (lane == 0) __hip_atomic_store((gu64*)(ws + W_EPOCH), tag, RLX_AGENT);
        }
      }
      lds_wait_ge(ctl + C_RCNT, NSW * (kx + 1));
      f32x2 ml[4]; f32x4 ok[4];
#pragma unroll
      for (int k = 0; k < 4; ++k) { ml[k] = *(LAS f32x2*)(lds + L_RECX + (k * 2 + r) * ROW + 512); ok[k] = *(LAS f32x4*)(lds + L_RECX + (k * 2 + r) * ROW + ch * 16); }
      const bool mine = early || last;
      float Mx = mine ? M : -INFINITY;
#pragma unroll
      for (int k = 0; k < 4; ++k) Mx = fmaxf(Mx, ml[k][0]);
      const float fr = mine ? mfac(M, Mx) : 0.f;
      float Lx = L * fr; f32x4 ox = o * fr;
#pragma unroll
      for (int k = 0; k < 4; ++k) { const float f = mfac(ml[k][0], Mx); Lx = fmaf(f, ml[k][1], Lx); ox += ok[k] * f; }
      asm volatile("s_waitcnt lgkmcnt(0)" : "+v"(ox) :: "memory");
      if (last) {
        const float inv = __builtin_amdgcn_rcpf(Lx);
        outp[((w * HQ + 2 * h + r) * D + 4 * ch) >> 2] = u32x2{cvtpk(ox[0] * inv, ox[1] * inv), cvtpk(ox[2] * inv, ox[3] * inv)};
        break;
      }
      lds_st(ctl + C_RFREE, kx + 1);
      if (early) { M = Mx; L = Lx; o = ox; }
      else {
        const unsigned so = W_PX + (blk * 16 + __builtin_ctz(m)) * PX_BYTES;
        __builtin_amdgcn_raw_buffer_store_b128(__builtin_bit_cast(u32x4, ox), prsrc, lane * 16, so, 16);
        const f32x4 mlv = {__builtin_bit_cast(float, rdl(__builtin_bit_cast(int, Mx), 0)), __builtin_bit_cast(float, rdl(__builtin_bit_cast(int, Lx), 0)),
                           __builtin_bit_cast(float, rdl(__builtin_bit_cast(int, Mx), 32)), __builtin_bit_cast(float, rdl(__builtin_bit_cast(int, Lx), 32))};
        if (lane < 8) __builtin_amdgcn_raw_buffer_store_b128(__builtin_bit_cast(u32x4, mlv), prsrc, 1024 + lane * 16, so, 16);
        m &= m - 1;
      }
    }
  }
}

extern "C" void kernel_launch(void* const* d_in, const int* in_sizes, int n_in, void* d_out, int out_size, void* d_ws, size_t ws_size, hipStream_t stream) {
  Args a;
  a.q = (const bf16x8*)d_in[0]; a.k = d_in[1]; a.v = d_in[2]; a.bt = (const int*)d_in[3]; a.lens = (const int*)d_in[4];
  a.out = (unsigned*)d_out; a.ws = (unsigned char*)d_ws; a.kv_bytes = (unsigned)in_sizes[1] * 2u; a.pad = 0;
  (void)hipFuncSetAttribute((const void*)radix_attn, hipFuncAttributeMaxDynamicSharedMemorySize, LDS_BYTES);
  hipLaunchKernelGGL(radix_attn, dim3(256), dim3(NTHR), LDS_BYTES, stream, a);
}
```

```cpp
#define CLS01 4
#define PTEH 2
#include <hip/hip_runtime.h>
#include <hip/hip_bf16.h>
#include <stdint.h>

#define NSLOT 16
#define INFL 20
#define KV_NT 1
#define TLBPF 0
#define OUTSC1 0
#define OUT_STORE(p, v) (*(p) = (v))
#define ABL_NOCOMP 0
#define SLEEPN 1
#define ABL_NOSVC 0
#define ABL_NOSTORE 0
#define ABL_NOGATHER 0
#define ABL_NOPOLL 0
#define POLLSLEEP 32
#define SHCAP 21
#define POLLAT 9
#define STAUX 16
#define LDAUX 16
#define STPACE 0
#define PTEPF 1
#define PTESTEP (8u << 20)
#define XBIG 0
#define LATEF 12
#define FASTT 49
#define FASTN 2
#define WARM 1
#define DIAG 0
#define MAPSWAP 1

constexpr int B = 16, HQ = 32, HKV = 16, D = 128, PAGE = 16, NB = 256, LK = NB * PAGE;
constexpr int NA = 4;
constexpr int NSW = 4, NTHR = (NSW + 2) * 64;
constexpr float C2 = 0.088388347648318440f * 1.4426950408889634f;
constexpr unsigned PAGE_BYTES = PAGE * HKV * D * 2;
constexpr unsigned TOK_BYTES = HKV * D * 2;

constexpr int ROW = 528;
constexpr int L_RING = 0;
constexpr int L_MRG = NSLOT * 8192;
constexpr int L_TAB = L_RING + (NSLOT - 2) * 8192;
constexpr int L_RECX = L_MRG + 32 * ROW;
constexpr int L_MISC = L_RECX + 4 * 2 * ROW;
constexpr int C_LANDED = 0, C_CSTEP = 4, C_RCNT = 8, C_RFREE = 12, C_REL = 16, C_P = 32, C_DUMMY = 36, C_READY = 40, C_PTR = 48, C_LENS = 80;
constexpr int LDS_BYTES = L_MISC + 160;
static_assert(NSLOT % 4 == 0 && (NSLOT & (NSLOT - 1)) == 0, "ring geometry");
static_assert(LDS_BYTES <= 160 * 1024, "LDS");

constexpr size_t W_EPOCH = 0, W_FLAG = 256, FLAG_STRIDE = 128;
constexpr size_t W_PART = 65536;
constexpr unsigned PS_BYTES = 32 * 512 + 256, PX_BYTES = 2 * 512 + 128;
constexpr unsigned W_PX = 256 * PS_BYTES;
constexpr unsigned PART_BYTES = W_PX + 256 * 16 * PX_BYTES;
#define STAMP(k) do {} while (0)

typedef __bf16 bf16x8 __attribute__((ext_vector_type(8)));
typedef __bf16 bf16x2 __attribute__((ext_vector_type(2)));
typedef short s16x4 __attribute__((ext_vector_type(4)));
typedef float f32x4 __attribute__((ext_vector_type(4)));
typedef float f32x2 __attribute__((ext_vector_type(2)));
typedef unsigned u32x4 __attribute__((ext_vector_type(4)));
typedef unsigned u32x2 __attribute__((ext_vector_type(2)));
typedef int i32x4 __attribute__((ext_vector_type(4)));
#define LAS __attribute__((address_space(3)))
#define GAS __attribute__((address_space(1)))
#define DI __device__ __forceinline__
typedef GAS unsigned long long gu64;
#define RLX_AGENT __ATOMIC_RELAXED, __HIP_MEMORY_SCOPE_AGENT

DI int rfl(int x) { return __builtin_amdgcn_readfirstlane(x); }
DI int rdl(int x, int l) { return __builtin_amdgcn_readlane(x, l); }
template <int CTRL> DI int dpp_i(int oldv, int x) { return __builtin_amdgcn_update_dpp(oldv, x, CTRL, 0xf, 0xf, false); }
DI int row_scan_add(int x) {
  x += dpp_i<0x111>(0, x); x += dpp_i<0x112>(0, x); x += dpp_i<0x114>(0, x); x += dpp_i<0x118>(0, x); return x;
}
DI int row_scan_max(int x) {
  x = max(x, dpp_i<0x111>(0, x)); x = max(x, dpp_i<0x112>(0, x)); x = max(x, dpp_i<0x114>(0, x)); x = max(x, dpp_i<0x118>(0, x)); return x;
}
DI float xrow16_max(float x) {
  auto s = __builtin_amdgcn_permlane16_swap(__float_as_uint(x), __float_as_uint(x), false, false);
  x = fmaxf(__uint_as_float(s[0]), __uint_as_float(s[1]));
  auto t = __builtin_amdgcn_permlane32_swap(__float_as_uint(x), __float_as_uint(x), false, false);
  return fmaxf(__uint_as_float(t[0]), __uint_as_float(t[1]));
}
DI float xrow16_sum(float x) {
  auto s = __builtin_amdgcn_permlane16_swap(__float_as_uint(x), __float_as_uint(x), false, false);
  x = __uint_as_float(s[0]) + __uint_as_float(s[1]);
  auto t = __builtin_amdgcn_permlane32_swap(__float_as_uint(x), __float_as_uint(x), false, false);
  return __uint_as_float(t[0]) + __uint_as_float(t[1]);
}
DI unsigned cvtpk(float lo, float hi) { f32x2 v = {lo, hi}; bf16x2 b = __builtin_convertvector(v, bf16x2); return __builtin_bit_cast(unsigned, b); }
DI float ex2(float x) { return __builtin_amdgcn_exp2f(x); }
DI void wg_barrier() { asm volatile("s_waitcnt lgkmcnt(0)\n\ts_barrier" ::: "memory"); }
template <int N> DI void wait_vm() { asm volatile("s_waitcnt vmcnt(%0)" :: "i"(N) : "memory"); }

#define NTS " nt"
DI void dma4(unsigned m0v, __amdgpu_buffer_rsrc_t rsrc, unsigned soff, unsigned v0, unsigned v1, unsigned v2, unsigned v3) {
  asm volatile("s_mov_b32 m0, %0\n\t"
               "s_nop 4\n\t"
               "buffer_load_dwordx4 %1, %5, %6 offen" NTS " lds\n\t"
               "buffer_load_dwordx4 %2, %5, %6 offen offset:1024" NTS " lds\n\t"
               "buffer_load_dwordx4 %3, %5, %6 offen offset:2048" NTS " lds\n\t"
               "buffer_load_dwordx4 %4, %5, %6 offen offset:3072" NTS " lds"
               :: "s"(m0v), "v"(v0), "v"(v1), "v"(v2), "v"(v3), "s"(rsrc), "s"(soff) : "memory");
}

DI void set_m0(unsigned m0v, unsigned so) { asm volatile("s_mov_b32 m0, %0\n\ts_nop 3" :: "s"(m0v), "s"(so) : "memory"); }
template <int X, bool NT> DI void wait_dma4x(__amdgpu_buffer_rsrc_t rsrc, unsigned soff, unsigned v0, unsigned v1, unsigned v2, unsigned v3) {
  if (NT)
  asm volatile("s_waitcnt vmcnt(%6)\n\t"
               "buffer_load_dwordx4 %0, %4, %5 offen nt lds\n\t"
               "buffer_load_dwordx4 %1, %4, %5 offen offset:1024 nt lds\n\t"
               "buffer_load_dwordx4 %2, %4, %5 offen offset:2048 nt lds\n\t"
               "buffer_load_dwordx4 %3, %4, %5 offen offset:3072 nt lds"
               :: "v"(v0), "v"(v1), "v"(v2), "v"(v3), "s"(rsrc), "s"(soff), "i"(X) : "memory");
  else
  asm volatile("s_waitcnt vmcnt(%6)\n\t"
               "buffer_load_dwordx4 %0, %4, %5 offen lds\n\t"
               "buffer_load_dwordx4 %1, %4, %5 offen offset:1024 lds\n\t"
               "buffer_load_dwordx4 %2, %4, %5 offen offset:2048 lds\n\t"
               "buffer_load_dwordx4 %3, %4, %5 offen offset:3072 lds"
               :: "v"(v0), "v"(v1), "v"(v2), "v"(v3), "s"(rsrc), "s"(soff), "i"(X) : "memory");
}
template <bool NT> DI void dma4r(__amdgpu_buffer_rsrc_t r0, __amdgpu_buffer_rsrc_t r1, __amdgpu_buffer_rsrc_t r2, __amdgpu_buffer_rsrc_t r3, unsigned soff, unsigned v0, unsigned v1, unsigned v2, unsigned v3) {
  if (NT)
  asm volatile("s_nop 0\n\t"
               "buffer_load_dwordx4 %0, %4, %8 offen nt lds\n\t"
               "buffer_load_dwordx4 %1, %5, %8 offen offset:1024 nt lds\n\t"
               "buffer_load_dwordx4 %2, %6, %8 offen offset:2048 nt lds\n\t"
               "buffer_load_dwordx4 %3, %7, %8 offen offset:3072 nt lds"
               :: "v"(v0), "v"(v1), "v"(v2), "v"(v3), "s"(r0), "s"(r1), "s"(r2), "s"(r3), "s"(soff) : "memory");
  else
  asm volatile("s_nop 0\n\t"
               "buffer_load_dwordx4 %0, %4, %8 offen lds\n\t"
               "buffer_load_dwordx4 %1, %5, %8 offen offset:1024 lds\n\t"
               "buffer_load_dwordx4 %2, %6, %8 offen offset:2048 lds\n\t"
               "buffer_load_dwordx4 %3, %7, %8 offen offset:3072 lds"
               :: "v"(v0), "v"(v1), "v"(v2), "v"(v3), "s"(r0), "s"(r1), "s"(r2), "s"(r3), "s"(soff) : "memory");
}
#define K_NT 1
#define V_NT 1
#define WAIT_K(X, so) wait_dma4x<X, K_NT>(rk, so, vk[0], vk[1], vk[2], vk[3])
#define WAIT_V(X, so) wait_dma4x<X, V_NT>(rv, so, vv[0], vv[1], vv[2], vv[3])

template <int X, bool KNT, bool VNT>
DI void page_asm(__amdgpu_buffer_rsrc_t rk, __amdgpu_buffer_rsrc_t rv, const unsigned (&vk)[4], const unsigned (&vv)[4], unsigned vctl, unsigned vp,
                 int& i, int& pub, unsigned& so, unsigned& mk) {
  unsigned vt;
#define PAGE_ASM(KS, VS) \
  asm volatile("s_waitcnt vmcnt(%[X])\n\t" \
               "buffer_load_dwordx4 %[k0], %[rk], %[so] offen" KS " lds\n\t" \
               "buffer_load_dwordx4 %[k1], %[rk], %[so] offen offset:1024" KS " lds\n\t" \
               "buffer_load_dwordx4 %[k2], %[rk], %[so] offen offset:2048" KS " lds\n\t" \
               "buffer_load_dwordx4 %[k3], %[rk], %[so] offen offset:3072" KS " lds\n\t" \
               "v_mov_b32 %[vt], %[pub]\n\t" \
               "s_add_u32 m0, m0, 0x1000\n\t" \
               "ds_write_b32 %[vctl], %[vt]\n\t" \
               "s_add_u32 %[pub], %[pub], 1\n\t" \
               "s_waitcnt vmcnt(%[X])\n\t" \
               "buffer_load_dwordx4 %[v0], %[rv], %[so] offen" VS " lds\n\t" \
               "buffer_load_dwordx4 %[v1], %[rv], %[so] offen offset:1024" VS " lds\n\t" \
               "buffer_load_dwordx4 %[v2], %[rv], %[so] offen offset:2048" VS " lds\n\t" \
               "buffer_load_dwordx4 %[v3], %[rv], %[so] offen offset:3072" VS " lds\n\t" \
               "v_mov_b32 %[vt], %[pub]\n\t" \
               "s_add_u32 %[i], %[i], 1\n\t" \
               "ds_write_b32 %[vctl], %[vt]\n\t" \
               "v_readlane_b32 %[so], %[vp], %[i]\n\t" \
               "s_add_u32 %[pub], %[pub], 1\n\t" \
               "s_add_u32 %[mk], %[mk], 0x2000\n\t" \
               "s_and_b32 %[mk], %[mk], %[msk]\n\t" \
               "s_mov_b32 m0, %[mk]" \
               : [i] "+s"(i), [pub] "+s"(pub), [so] "+s"(so), [mk] "+s"(mk), [vt] "=&v"(vt) \
               : [X] "i"(X), [msk] "i"(NSLOT * 8192 - 1), [k0] "v"(vk[0]), [k1] "v"(vk[1]), [k2] "v"(vk[2]), [k3] "v"(vk[3]), [v0] "v"(vv[0]), [v1] "v"(vv[1]), [v2] "v"(vv[2]), [v3] "v"(vv[3]), \
                 [rk] "s"(rk), [rv] "s"(rv), [vctl] "v"(vctl), [vp] "v"(vp) : "memory", "scc")
  if (KNT && VNT) PAGE_ASM(" nt", " nt"); else if (KNT) PAGE_ASM(" nt", ""); else if (VNT) PAGE_ASM("", " nt"); else PAGE_ASM("", "");
#undef PAGE_ASM
}

struct Args { const bf16x8* q; const void* k; const void* v; const int* bt; const int* lens; unsigned* out; unsigned char* ws; unsigned kv_bytes; unsigned pad; };

DI float mfac(float m, float M) { return (m == -INFINITY) ? 0.f : ex2((m - M) * C2); }

struct Soft { float m, l; f32x4 o[8]; };
DI void soft_reset(Soft& s) { s.m = -INFINITY; s.l = 0.f;
#pragma unroll
  for (int i = 0; i < 8; ++i) s.o[i] = f32x4{0.f, 0.f, 0.f, 0.f}; }

DI s16x4 soft_step(Soft& s, f32x4 x, bool mask, int tok0, int len) {
  if (mask) {
#pragma unroll
    for (int r = 0; r < 4; ++r) x[r] = (tok0 + r < len) ? x[r] : -INFINITY;
  }
  float pm = fmaxf(fmaxf(x[0], x[1]), fmaxf(x[2], x[3]));
  pm = xrow16_max(pm);
  const float mn = fmaxf(s.m, pm);
  const float ms = (mn == -INFINITY) ? 0.f : mn * C2;
  const float alpha = ex2(s.m * C2 - ms);
  float p[4];
#pragma unroll
  for (int r = 0; r < 4; ++r) p[r] = ex2(fmaf(x[r], C2, -ms));
  s.l = fmaf(s.l, alpha, (p[0] + p[1]) + (p[2] + p[3]));
  s.m = mn;
#pragma unroll
  for (int i = 0; i < 8; ++i) s.o[i] *= alpha;
  u32x2 pk = {cvtpk(p[0], p[1]), cvtpk(p[2], p[3])};
  return __builtin_bit_cast(s16x4, pk);
}

DI int lds_ld(LAS unsigned char* p) { return __hip_atomic_load((LAS int*)p, __ATOMIC_RELAXED, __HIP_MEMORY_SCOPE_WORKGROUP); }
DI void lds_st(LAS unsigned char* p, int v) { __hip_atomic_store((LAS int*)p, v, __ATOMIC_RELAXED, __HIP_MEMORY_SCOPE_WORKGROUP); }
DI int lds_wait_ge(LAS unsigned char* p, int need) {
  int v;
#pragma nounroll
  for (unsigned spins = 0;; ++spins) { v = rfl(lds_ld(p)); if (v >= need || spins > (1u << 24)) break; __builtin_amdgcn_s_sleep(SLEEPN); }
  asm volatile("" ::: "memory");
  return v;
}
DI void lds_drain() { asm volatile("s_waitcnt lgkmcnt(0)" ::: "memory"); }

__global__ void __launch_bounds__(NTHR, 2) radix_attn(Args a_) {
  extern __shared__ __attribute__((aligned(16))) unsigned char lds_raw[];
  LAS unsigned char* lds = (LAS unsigned char*)lds_raw;
  LAS unsigned char* ctl = lds + L_MISC;
  const int tid = threadIdx.x, lane = tid & 63, wave = rfl(tid >> 6);
  const int w = blockIdx.x & 15, h = blockIdx.x >> 4;
  const int c = lane & 15, g = lane >> 4;
  int len_raw;

  struct Part { int len, Pe, S, E, spare, exc, lo, hi, xoff, S_w, spare_w, T_w, ns, ss, hp, nsh, np_w, own_w, vA, TL; unsigned xmask; bool all_masked; };
  auto partition = [&](int P) {
    Part p;
    p.all_masked = len_raw <= 0;
    p.len = p.all_masked ? LK : min(len_raw, LK);
    const int np = (p.len + PAGE - 1) >> 4;
    p.Pe = min(P, rdl(row_scan_max(np), 15));
    const int n = max(np - p.Pe, 0);
    const int W = p.Pe + rdl(row_scan_add(n), 15);
    const int T = (W >> 4) + (c < (W & 15) ? 1 : 0);
    const int cap = max(1, ((W >> 4) * SHCAP) >> 6);
    int lg = 0;
    if (p.Pe > cap) lg = 1;
    if (p.Pe > 2 * cap) lg = 2;
    if (p.Pe > 4 * cap) lg = 3;
    if (p.Pe > 8 * cap) lg = 4;
    const int q = p.Pe >> lg, rq = p.Pe & ((1 << lg) - 1);
    const int sh = c < (1 << lg) ? q + (c < rq ? 1 : 0) : 0;
    p.nsh = min(1 << lg, p.Pe);
    p.ns = w < (1 << lg) ? q + (w < rq ? 1 : 0) : 0;
    p.ss = w * q + min(w, rq);
    const int own = min(n, T - sh);
    p.exc = n - own; p.spare = T - sh - own;
    p.S = row_scan_add(p.spare) - p.spare;
    p.E = row_scan_add(p.exc) - p.exc;
    p.S_w = rdl(p.S, w); p.spare_w = rdl(p.spare, w); p.T_w = rdl(T, w);
    p.hp = p.ns + p.spare_w;
    p.lo = max(p.S_w, p.E); p.hi = min(p.S_w + p.spare_w, p.E + p.exc);
    p.xmask = (unsigned)(__ballot(p.hi > p.lo) & 0xFFFFull);
    p.xoff = p.Pe - p.E;
    p.np_w = rdl(np, w); p.own_w = rdl(own, w);
    p.vA = min(NA, p.own_w);
    p.TL = NA + p.hp + p.own_w - p.vA;
    return p;
  };

  if (__builtin_expect(wave == 0, 1)) {
    __builtin_amdgcn_s_setprio(3);
    typedef unsigned u32x16 __attribute__((ext_vector_type(16)));
    u32x16 ka = *(const __attribute__((address_space(4))) u32x16*)__builtin_amdgcn_kernarg_segment_ptr();
    asm volatile("" : "+s"(ka));
    Args a;
    a.q = (const bf16x8*)(((uintptr_t)ka[1] << 32) | ka[0]); a.k = (const void*)(((uintptr_t)ka[3] << 32) | ka[2]); a.v = (const void*)(((uintptr_t)ka[5] << 32) | ka[4]);
    a.bt = (const int*)(((uintptr_t)ka[7] << 32) | ka[6]); a.lens = (const int*)(((uintptr_t)ka[9] << 32) | ka[8]); a.out = (unsigned*)(((uintptr_t)ka[11] << 32) | ka[10]);
    a.ws = (unsigned char*)(((uintptr_t)ka[13] << 32) | ka[12]); a.kv_bytes = ka[14];
    len_raw = *(const GAS int*)((uintptr_t)a.lens + 4 * c);
    const GAS i32x4* bt4 = (const GAS i32x4*)(uintptr_t)a.bt;
    i32x4 rk16[16];
#pragma unroll
    for (int k = 0; k < 16; ++k) rk16[k] = bt4[k * (NB / 4) + lane];
    STAMP(8);
    asm volatile("s_barrier" ::: "memory");
    if (lane == 0) { *(LAS u32x4*)(ctl + C_PTR) = u32x4{(unsigned)(uintptr_t)a.q, (unsigned)((uintptr_t)a.q >> 32), (unsigned)(uintptr_t)a.out, (unsigned)((uintptr_t)a.out >> 32)};
                     *(LAS u32x2*)(ctl + C_PTR + 16) = u32x2{(unsigned)(uintptr_t)a.ws, (unsigned)((uintptr_t)a.ws >> 32)}; }
    unsigned vk[4], vv[4];
#pragma unroll
    for (int i = 0; i < 4; ++i) { const int t = 4 * i + g;
      vk[i] = t * TOK_BYTES + ((c ^ t) << 4) - 1024 * i;
      vv[i] = t * TOK_BYTES + ((c ^ ((t & 7) << 1)) << 4) - 1024 * i; }
    asm volatile("" : "+v"(vk[0]), "+v"(vk[1]), "+v"(vk[2]), "+v"(vk[3]), "+v"(vv[0]), "+v"(vv[1]), "+v"(vv[2]), "+v"(vv[3]));
    const unsigned ring = (unsigned)(uintptr_t)(lds + L_RING);
    const __amdgpu_buffer_rsrc_t rk = __builtin_amdgcn_make_buffer_rsrc((void*)a.k, 0, a.kv_bytes, 0x00020000);
    const __amdgpu_buffer_rsrc_t rv = __builtin_amdgcn_make_buffer_rsrc((void*)a.v, 0, a.kv_bytes, 0x00020000);
#pragma unroll
    for (int k = 0; k < 16; ++k) *(LAS i32x4*)(lds + L_TAB + (k * NB + 4 * lane) * 4) = rk16[k];
    unsigned vp0, vp1 = 0, vp2 = 0, vp3 = 0;
    int nq0;
    {
      const int lw = rdl(len_raw, w);
      const int npw = ((lw <= 0 ? LK : min(lw, LK)) + PAGE - 1) >> 4;
      vp0 = (unsigned)*(LAS int*)(lds + L_TAB + 4 * (w * NB + max(npw - 1 - (lane & 3), 0))) * PAGE_BYTES + (unsigned)h * (D * 2);
      nq0 = ((lw <= 0 ? LK : min(lw, LK)) - (npw - 1) * PAGE + 3) >> 2;
    }
    unsigned so = (unsigned)rdl((int)vp0, 0), m0k = ring;
    {
      const __amdgpu_buffer_rsrc_t k1 = __builtin_amdgcn_make_buffer_rsrc((void*)a.k, 0, nq0 > 1 ? a.kv_bytes : 0u, 0x00020000), k2 = __builtin_amdgcn_make_buffer_rsrc((void*)a.k, 0, nq0 > 2 ? a.kv_bytes : 0u, 0x00020000),
                                   k3 = __builtin_amdgcn_make_buffer_rsrc((void*)a.k, 0, nq0 > 3 ? a.kv_bytes : 0u, 0x00020000);
      set_m0(m0k, so);
      dma4r<K_NT>(rk, k1, k2, k3, so, vk[0], vk[1], vk[2], vk[3]);
      const __amdgpu_buffer_rsrc_t v1 = __builtin_amdgcn_make_buffer_rsrc((void*)a.v, 0, nq0 > 1 ? a.kv_bytes : 0u, 0x00020000), v2 = __builtin_amdgcn_make_buffer_rsrc((void*)a.v, 0, nq0 > 2 ? a.kv_bytes : 0u, 0x00020000),
                                   v3 = __builtin_amdgcn_make_buffer_rsrc((void*)a.v, 0, nq0 > 3 ? a.kv_bytes : 0u, 0x00020000);
      set_m0(m0k + 4096, so);
      dma4r<V_NT>(rv, v1, v2, v3, so, vv[0], vv[1], vv[2], vv[3]);
      so = (unsigned)rdl((int)vp0, 1); m0k = ring + 8192;
      set_m0(m0k, so);
    }
#pragma nounroll
    for (int i = 1; i < 3; ++i) {
      WAIT_K(63, so);
      set_m0(m0k + 4096, so);
      WAIT_V(63, so);
      so = (unsigned)rdl((int)vp0, i + 1);
      m0k = ring + (i + 1) * 8192;
      set_m0(m0k, so);
    }
    STAMP(3);
    i32x4 acc = rk16[1] ^ rk16[0];
#pragma unroll
    for (int k = 2; k < 16; ++k) acc |= rk16[k] ^ rk16[0];
    int P;
    {
      const unsigned long long b0 = __ballot(acc[0] != 0), b1 = __ballot(acc[1] != 0), b2 = __ballot(acc[2] != 0), b3 = __ballot(acc[3] != 0);
      const unsigned long long any = b0 | b1 | b2 | b3;
      const int l = any ? (int)__builtin_ctzll(any) : 64;
      const unsigned long long bit = any & (0ull - any);
      P = 4 * l + ((b0 & bit) ? 0 : (b1 & bit) ? 1 : (b2 & bit) ? 2 : (b3 & bit) ? 3 : 0);
    }
    if (lane == 0) *(LAS int*)(ctl + C_P) = P;
    if (lane < 16) *(LAS int*)(ctl + C_LENS + 4 * lane) = len_raw;
    lds_drain();
    lds_st(ctl + C_READY, 1);
    STAMP(2);
    const Part p = partition(P);
    const int S_w = p.S_w - p.ns - NA, hp = p.hp, ns = p.ns, ss = p.ss, T_w = p.TL, lo = p.lo, hi = p.hi, xoff = p.xoff; const unsigned xmask = p.xmask;
    const int lpA = p.np_w - 1, lpB = p.np_w - p.own_w;
    auto page_src = [&](int r) {
      const int i = 64 * r + lane;
      int idx = (i < NA) ? w * NB + max(lpA - i, 0) : (i < NA + ns) ? ss + (i - NA) : w * NB + lpB + (i - NA - hp);
      for (unsigned m = xmask; m; m &= m - 1) {
        const int b = __builtin_ctz(m);
        const int lo_b = rdl(lo, b) - S_w, hi_b = rdl(hi, b) - S_w, off_b = rdl(xoff, b) + b * NB + S_w;
        if (i >= lo_b && i < hi_b) idx = i + off_b;
      }
      return (unsigned)*(LAS int*)(lds + L_TAB + 4 * (i < T_w ? idx : 0)) * PAGE_BYTES + (unsigned)h * (D * 2);
    };
    vp0 = page_src(0); vp1 = page_src(1); vp2 = page_src(2); vp3 = page_src(3);
    asm volatile("" : "+v"(vp0), "+v"(vp1), "+v"(vp2), "+v"(vp3));
    STAMP(7);
    static_assert(L_RING == 0 && C_LANDED == 0 && C_REL == 16, "page_asm addresses the ring from LDS offset 0 and the control words by these offsets");
    const unsigned vctl = (unsigned)(uintptr_t)ctl;
    auto publish = [&](int nl) { lds_st(ctl + C_LANDED, nl); };
    const bool wide = CLS01 && ((((unsigned)(uintptr_t)a.k >> 8) + h) & 3) == 1;
    const int TM = T_w >= 8 ? T_w - 4 : T_w;
    int i = 3, credit = NSLOT;
    unsigned mk = m0k;
    for (;;) {
      const int iend = min(min(TM, credit), (i & ~63) + 64);
      if (wide) { int pub = 2 * i - (INFL + CLS01 + 3) / 4;
#pragma nounroll
        while (i < iend) page_asm<INFL + CLS01, K_NT, V_NT>(rk, rv, vk, vv, vctl, vp0, i, pub, so, mk);
      } else { int pub = 2 * i - (INFL + 3) / 4;
#pragma nounroll
        while (i < iend) page_asm<INFL, K_NT, V_NT>(rk, rv, vk, vv, vctl, vp0, i, pub, so, mk);
      }
      if (i >= T_w) break;
      const bool endgroup = i >= TM;
      const u32x4 rel = *(volatile LAS u32x4*)(ctl + C_REL);
      credit = min(min(4 * rfl((int)rel[0]), 4 * rfl((int)rel[1]) + 1), min(4 * rfl((int)rel[2]) + 2, 4 * rfl((int)rel[3]) + 3)) + NSLOT;
      if ((i & 63) == 0) { vp0 = vp1; vp1 = vp2; vp2 = vp3; so = (unsigned)rdl((int)vp0, 0); asm volatile("s_nop 4" :: "s"(so)); }
      if (credit <= i) {
        LAS unsigned char* relp = ctl + C_REL + 4 * (i & 3);
        const int need = (i >> 2) - NSLOT / 4 + 1;
        if (rfl(lds_ld(relp)) < need) { wait_vm<12>(); publish(2 * i - 3);
          if (rfl(lds_ld(relp)) < need) { wait_vm<4>(); publish(2 * i - 1);
            if (rfl(lds_ld(relp)) < need) { wait_vm<0>(); publish(2 * i); lds_wait_ge(relp, need); } } }
        credit = i + 1;
      }
      if (endgroup) {
        if (credit < T_w) { wait_vm<0>(); publish(2 * i);
          for (int e = i; e < T_w; ++e) if (e >= NSLOT) lds_wait_ge(ctl + C_REL + 4 * (e & 3), (e >> 2) - NSLOT / 4 + 1); }
        unsigned se[4];
        int pub = 2 * i - (INFL + 3) / 4;
#pragma unroll
        for (int r = 0; r < 4; ++r) {
          if (r > 0) { if (((i + r) & 63) == 0) { vp0 = vp1; vp1 = vp2; } so = (unsigned)rdl((int)vp0, (i + r) & 63); }
          se[r] = so;
          set_m0(((i + r) & (NSLOT - 1)) * 8192, so);
          WAIT_K(INFL, so);
          publish(pub + r);
        }
#pragma unroll
        for (int r = 0; r < 4; ++r) {
          set_m0(((i + r) & (NSLOT - 1)) * 8192 + 4096, se[r]);
          WAIT_V(INFL, se[r]);
          publish(pub + 4 + r);
        }
        break;
      }
    }
    STAMP(5);
    if (INFL + CLS01 + 4 > 24) { wait_vm<24>(); publish(2 * T_w - 6); }
    if (INFL + CLS01 + 4 > 20) { wait_vm<20>(); publish(2 * T_w - 5); }
    wait_vm<16>(); publish(2 * T_w - 4);
    wait_vm<12>(); publish(2 * T_w - 3);
    wait_vm<8>(); publish(2 * T_w - 2);
    wait_vm<4>(); publish(2 * T_w - 1);
    wait_vm<0>(); publish(2 * T_w);
    STAMP(6);
  } else if (wave <= NSW) {
    const int cw = wave - 1;
    wg_barrier();
    const unsigned ka0 = c * 256 + ((g ^ c) << 4);
    const int tv = 4 * g + (c >> 2);
    const unsigned va0 = 4096 + tv * 256 + ((tv & 7) << 5) + (c & 3) * 8;
    lds_wait_ge(ctl + C_READY, 1);
    STAMP(2);
    bf16x8 qf[2][4];
    {
      const u32x2 pq = *(LAS u32x2*)(ctl + C_PTR);
      const GAS bf16x8* qp = (const GAS bf16x8*)(((uintptr_t)(unsigned)rfl((int)pq[1]) << 32) | (unsigned)rfl((int)pq[0]));
#pragma unroll
      for (int cg = 0; cg < 2; ++cg)
#pragma unroll
        for (int s = 0; s < 4; ++s) { const int r = 16 * cg + c; qf[cg][s] = qp[(((r >> 1) * HQ + 2 * h + (r & 1)) * D + 32 * s + 8 * g) / 8]; }
    }
    len_raw = *(LAS int*)(ctl + C_LENS + 4 * c);
    const Part p = partition(rfl(*(LAS int*)(ctl + C_P)));
    const int len = p.len, S_w = p.S_w - p.ns - NA, hp = p.hp, ss = p.ss, T_w = p.TL, lo = p.lo, hi = p.hi, xoff = p.xoff, ns = p.ns, lpA = p.np_w - 1, lpB = p.np_w - p.own_w, vA = p.vA;
    const int minlen = LK - rdl(row_scan_max(LK - len), 15);
    const int len0 = __shfl(len, c >> 1), len1 = __shfl(len, 8 + (c >> 1));
    {
      const bool z0 = __shfl((int)p.all_masked, c >> 1) != 0, z1 = __shfl((int)p.all_masked, 8 + (c >> 1)) != 0;
#pragma unroll
      for (int s = 0; s < 4; ++s) {
        u32x4 t0 = __builtin_bit_cast(u32x4, qf[0][s]), t1 = __builtin_bit_cast(u32x4, qf[1][s]);
#pragma unroll
        for (int e = 0; e < 4; ++e) { t0[e] = z0 ? 0u : t0[e]; t1[e] = z1 ? 0u : t1[e]; }
        qf[0][s] = __builtin_bit_cast(bf16x8, t0); qf[1][s] = __builtin_bit_cast(bf16x8, t1);
      }
    }
    const int TM = T_w >= 8 ? T_w - 4 : T_w + 4;
    int j = 0, seen = 0;
    Soft s0, s1;
    auto page = [&](auto SHc, const bf16x8 (&qa)[4], const bf16x8 (&qb)[4], int la, int lb, int t0, bool mask) {
      constexpr bool SH = decltype(SHc)::value;
      const int i = 4 * j + cw;
      LAS unsigned char* sl = lds + L_RING + (i & (NSLOT - 1)) * 8192;
      const int re = i - TM;
      const int needk = re >= 0 ? 2 * TM + re + 1 : 2 * i + 1, needv = re >= 0 ? 2 * TM + re + 5 : 2 * i + 2;
      if (seen < needk) seen = lds_wait_ge(ctl + C_LANDED, needk);
      bf16x8 kf[4];
#pragma unroll
      for (int s = 0; s < 4; ++s) kf[s] = *(LAS bf16x8*)(sl + (ka0 ^ (s << 6)));
      f32x4 xa = {0.f, 0.f, 0.f, 0.f}, xb = {0.f, 0.f, 0.f, 0.f};
#pragma unroll
      for (int s = 0; s < 4; ++s) { xa = __builtin_amdgcn_mfma_f32_16x16x32_bf16(kf[s], qa[s], xa, 0, 0, 0);
        if (SH) xb = __builtin_amdgcn_mfma_f32_16x16x32_bf16(kf[s], qb[s], xb, 0, 0, 0); }
      const s16x4 pa = soft_step(s0, xa, mask, t0 + 4 * g, la);
      s16x4 pb; if (SH) pb = soft_step(s1, xb, mask, t0 + 4 * g, lb);
      if (seen < needv) seen = lds_wait_ge(ctl + C_LANDED, needv);
      s16x4 vf[8];
#pragma unroll
      for (int blk = 0; blk < 8; ++blk) vf[blk] = __builtin_amdgcn_ds_read_tr16_b64_v4i16((LAS s16x4*)(sl + (va0 ^ (blk << 5))));
      ++j;
      asm volatile("s_waitcnt lgkmcnt(0)" : "+v"(vf[0]), "+v"(vf[1]), "+v"(vf[2]), "+v"(vf[3]), "+v"(vf[4]), "+v"(vf[5]), "+v"(vf[6]), "+v"(vf[7]) :: "memory");
      lds_st(ctl + C_REL + 4 * cw, j);
#pragma unroll
      for (int blk = 0; blk < 8; ++blk) { s0.o[blk] = __builtin_amdgcn_mfma_f32_16x16x16bf16_1k(vf[blk], pa, s0.o[blk], 0, 0, 0);
        if (SH) s1.o[blk] = __builtin_amdgcn_mfma_f32_16x16x16bf16_1k(vf[blk], pb, s1.o[blk], 0, 0, 0); }
    };
    unsigned m = p.xmask;
    for (int kx = 0;; ++kx) {
      const bool early = kx == 0, last = !early && m == 0;
      const int b = (early || last) ? w : __builtin_ctz(m);
      const int i0 = early ? 0 : last ? NA + hp : rdl(lo, b) - S_w;
      const int i1 = early ? NA : last ? T_w : rdl(hi, b) - S_w;
      const int lp0 = early ? lpA - 2 * cw : last ? lpB : rdl(lo, b) + rdl(xoff, b);
      bf16x8 qa[4];
      if (b < 8) {
#pragma unroll
        for (int s = 0; s < 4; ++s) qa[s] = qf[0][s];
      } else {
#pragma unroll
        for (int s = 0; s < 4; ++s) qa[s] = qf[1][s];
      }
      const int lb = rdl(len, b);
      soft_reset(s0);
      if (last) lds_wait_ge(ctl + C_RFREE, kx);
      if (early && cw >= vA) {
        lds_wait_ge(ctl + C_LANDED, 2 * cw + 2); ++j; lds_st(ctl + C_REL + 4 * cw, j);
      }
      for (int i = 4 * j + cw; i < i1; i += 4) { const int t0 = (lp0 + i - i0) * PAGE; page(std::false_type{}, qa, qa, lb, lb, t0, t0 + PAGE > lb); }
      if (kx > 0 && !last) lds_wait_ge(ctl + C_RFREE, kx);
      const float lt = xrow16_sum(s0.l);
      if ((c >> 1) == (b & 7)) {
        LAS unsigned char* r = lds + L_RECX + (cw * 2 + (c & 1)) * ROW;
#pragma unroll
        for (int blk = 0; blk < 8; ++blk) *(LAS f32x4*)(r + blk * 64 + g * 16) = s0.o[blk];
        if (g == 0) *(LAS f32x2*)(r + 512) = f32x2{s0.m, lt};
      }
      lds_drain();
      if (lane == 0) __hip_atomic_fetch_add((LAS int*)(ctl + C_RCNT), 1, __ATOMIC_RELAXED, __HIP_MEMORY_SCOPE_WORKGROUP);
      if (last) break;
      if (early) {
        if (ns > 0) {
          soft_reset(s0); soft_reset(s1);
          for (int i = cw; i < ns; i += 4) { const int t0 = (ss + i) * PAGE; page(std::true_type{}, qf[0], qf[1], len0, len1, t0, t0 + PAGE > minlen); }
          if (cw < ns) {
            s0.l = xrow16_sum(s0.l); s1.l = xrow16_sum(s1.l);
            if (cw > 0) lds_wait_ge(ctl + C_CSTEP, cw);
#pragma unroll
            for (int cg = 0; cg < 2; ++cg) { Soft& s = cg ? s1 : s0;
              LAS unsigned char* r = lds + L_MRG + (16 * cg + c) * ROW;
              if (cw > 0) {
                const f32x2 ml = *(LAS f32x2*)(r + 512);
                const float M = fmaxf(ml[0], s.m), f0 = mfac(ml[0], M), f1 = mfac(s.m, M);
#pragma unroll
                for (int blk = 0; blk < 8; ++blk) s.o[blk] = *(LAS f32x4*)(r + blk * 64 + g * 16) * f0 + s.o[blk] * f1;
                s.l = ml[1] * f0 + s.l * f1; s.m = M;
              }
#pragma unroll
              for (int blk = 0; blk < 8; ++blk) *(LAS f32x4*)(r + blk * 64 + g * 16) = s.o[blk];
              lds_drain();
              if (g == 0) *(LAS f32x2*)(r + 512) = f32x2{s.m, s.l};
            }
            lds_drain();
            lds_st(ctl + C_CSTEP, cw + 1);
          }
        }
      } else m &= m - 1;
    }
  } else {
    if (lane < 12) *(LAS int*)(ctl + 4 * lane) = 0;
    unsigned touch = 0, touch2 = 0;
    if (h < PTEH) {
      typedef unsigned u32x16 __attribute__((ext_vector_type(16)));
      const u32x16 ka = *(const __attribute__((address_space(4))) u32x16*)__builtin_amdgcn_kernarg_segment_ptr();
      const uintptr_t kb = ((uintptr_t)ka[3] << 32) | ka[2], vb = ((uintptr_t)ka[5] << 32) | ka[4], wb = ((uintptr_t)ka[13] << 32) | ka[12];
      const unsigned off = (unsigned)(lane & 31) * PTESTEP;
      if (off < ka[14]) touch = *(const GAS unsigned*)((lane < 32 ? kb : vb) + off);
      if (lane < 2) touch2 = *(const GAS unsigned*)(wb + W_PART + lane * (PART_BYTES / 2));
    }
    wg_barrier();
    lds_wait_ge(ctl + C_READY, 1);
    asm volatile("" :: "v"(touch), "v"(touch2));
    STAMP(2);
    const u32x4 pq = *(LAS u32x4*)(ctl + C_PTR);
    const u32x2 pws = *(LAS u32x2*)(ctl + C_PTR + 16);
    GAS u32x2* outp = (GAS u32x2*)(((uintptr_t)(unsigned)rfl((int)pq[3]) << 32) | (unsigned)rfl((int)pq[2]));
    unsigned char* ws = (unsigned char*)(((uintptr_t)(unsigned)rfl((int)pws[1]) << 32) | (unsigned)rfl((int)pws[0]));
    const unsigned long long epoch = __hip_atomic_load((gu64*)(ws + W_EPOCH), RLX_AGENT);
    const __amdgpu_buffer_rsrc_t prsrc = __builtin_amdgcn_make_buffer_rsrc(ws + W_PART, 0, PART_BYTES, 0x00020000);
    len_raw = *(LAS int*)(ctl + C_LENS + 4 * c);
    const Part p = partition(rfl(*(LAS int*)(ctl + C_P)));
    const unsigned long long tag = epoch + 1ull;
    const int ns = p.ns;
    const unsigned blk = w * 16 + h;
    const int r = lane >> 5, ch = lane & 31;
    float M = -INFINITY, L = 0.f; f32x4 o = {0.f, 0.f, 0.f, 0.f};
    unsigned m = p.xmask;
#pragma nounroll
    for (int kx = 0;; ++kx) {
      const bool early = kx == 0, last = !early && m == 0;
      if (kx == 1 && ns > 0 && !ABL_NOSVC && !ABL_NOSTORE) {
        lds_wait_ge(ctl + C_CSTEP, min(ns, NSW));
#pragma unroll 4
        for (int k = 0; k < 16; ++k) {
          const u32x4 v = *(LAS u32x4*)(lds + L_MRG + (2 * k + r) * ROW + ch * 16);
          __builtin_amdgcn_raw_buffer_store_b128(v, prsrc, k * 1024 + lane * 16, blk * PS_BYTES, 16); }
        if (lane < 32) { const u32x2 v = *(LAS u32x2*)(lds + L_MRG + lane * ROW + 512);
          __builtin_amdgcn_raw_buffer_store_b64(v, prsrc, 32 * 512 + lane * 8, blk * PS_BYTES, 16); }
      }
      if (last) {
        asm volatile("s_waitcnt vmcnt(0)" ::: "memory");
        if (lane == 0) __hip_atomic_store((gu64*)(ws + W_FLAG + blk * FLAG_STRIDE), tag, RLX_AGENT);
        STAMP(3);
        const int E_w = rdl(p.E, w), exc_w = rdl(p.exc, w);
        const bool sh_ov = c < p.nsh;
        const bool ex_ov = min(p.S + p.spare, E_w + exc_w) > max(p.S, E_w);
        unsigned cmask = (ABL_NOSVC || ABL_NOGATHER) ? 0u : (unsigned)(__ballot(sh_ov) & 0xFFFFull) | ((unsigned)(__ballot(ex_ov) & 0xFFFFull) << 16);
        if (cmask) lds_wait_ge(ctl + C_LANDED, (p.TL * POLLAT) >> 3);
        {
          const bool need = !ABL_NOSVC && !ABL_NOPOLL && lane < 16 && (sh_ov || ex_ov);
          gu64* f = (gu64*)(ws + W_FLAG + (size_t)(c * 16 + h) * FLAG_STRIDE);
#pragma nounroll
          for (unsigned spins = 0; spins < (1u << 20); ++spins) {
            const bool ok = !need || __hip_atomic_load(f, RLX_AGENT) == tag;
            if (__all(ok)) break;
            __builtin_amdgcn_s_sleep(POLLSLEEP);
          }
          asm volatile("" ::: "memory");
        }
        STAMP(4);
        const unsigned vso = lane < 16 ? (unsigned)(c * 16 + h) * PS_BYTES + (unsigned)(2 * w) * 512 : W_PX + ((unsigned)(c * 16 + h) * 16 + w) * PX_BYTES;
        const unsigned vsm = lane < 16 ? (unsigned)(c * 16 + h) * PS_BYTES + 32 * 512 + (unsigned)(2 * w) * 8 : vso + 1024;
        constexpr int GB = 6;
        while (cmask) {
          u32x4 po[GB]; u32x2 pml[GB]; bool pv[GB];
#pragma unroll
          for (int k = 0; k < GB; ++k) {
            pv[k] = cmask != 0;
            const int id = pv[k] ? __builtin_ctz(cmask) : 0; cmask &= cmask - 1;
            const unsigned so = pv[k] ? (unsigned)rdl((int)vso, id) : PART_BYTES, sm = pv[k] ? (unsigned)rdl((int)vsm, id) : PART_BYTES;
            po[k] = __builtin_amdgcn_raw_buffer_load_b128(prsrc, lane * 16 + so, 0, 16);
            pml[k] = __builtin_amdgcn_raw_buffer_load_b64(prsrc, r * 8 + sm, 0, 16);
          }
#pragma unroll
          for (int k = 0; k < GB; ++k) {
            const float mk = pv[k] ? __uint_as_float(pml[k][0]) : -INFINITY, lk = __uint_as_float(pml[k][1]);
            const float Mn = fmaxf(M, mk), f0 = mfac(M, Mn), f1 = mfac(mk, Mn);
            o = o * f0 + __builtin_bit_cast(f32x4, po[k]) * f1; L = L * f0 + lk * f1; M = Mn;
          }
        }
        if (blk == 0) {
#pragma nounroll
          for (unsigned spins = 0; spins < (1u << 20); ++spins) {
            bool ok = true;
#pragma unroll
            for (int k = 0; k < 4; ++k) ok &= __hip_atomic_load((gu64*)(ws + W_FLAG + (size_t)(k * 64 + lane) * FLAG_STRIDE), RLX_AGENT) == tag;
            if (__all(ok)) break;
            __builtin_amdgcn_s_sleep(8);
          }
          if (lane == 0) __hip_atomic_store((gu64*)(ws + W_EPOCH), tag, RLX_AGENT);
        }
        STAMP(5);
      }
      lds_wait_ge(ctl + C_RCNT, NSW * (kx + 1));
      f32x2 ml[4]; f32x4 ok[4];
#pragma unroll
      for (int k = 0; k < 4; ++k) { ml[k] = *(LAS f32x2*)(lds + L_RECX + (k * 2 + r) * ROW + 512); ok[k] = *(LAS f32x4*)(lds + L_RECX + (k * 2 + r) * ROW + ch * 16); }
      const bool mine = early || last;
      float Mx = mine ? M : -INFINITY;
#pragma unroll
      for (int k = 0; k < 4; ++k) Mx = fmaxf(Mx, ml[k][0]);
      const float fr = mine ? mfac(M, Mx) : 0.f;
      float Lx = L * fr; f32x4 ox = o * fr;
#pragma unroll
      for (int k = 0; k < 4; ++k) { const float f = mfac(ml[k][0], Mx); Lx = fmaf(f, ml[k][1], Lx); ox += ok[k] * f; }
      asm volatile("s_waitcnt lgkmcnt(0)" : "+v"(ox) :: "memory");
      if (last) {
        const float inv = __builtin_amdgcn_rcpf(Lx);
        outp[((w * HQ + 2 * h + r) * D + 4 * ch) >> 2] = u32x2{cvtpk(ox[0] * inv, ox[1] * inv), cvtpk(ox[2] * inv, ox[3] * inv)};
        break;
      }
      lds_st(ctl + C_RFREE, kx + 1);
      if (early) { M = Mx; L = Lx; o = ox; }
      else {
        const unsigned so = W_PX + (blk * 16 + __builtin_ctz(m)) * PX_BYTES;
        __builtin_amdgcn_raw_buffer_store_b128(__builtin_bit_cast(u32x4, ox), prsrc, lane * 16, so, 16);
        const f32x4 mlv = {__builtin_bit_cast(float, rdl(__builtin_bit_cast(int, Mx), 0)), __builtin_bit_cast(float, rdl(__builtin_bit_cast(int, Lx), 0)),
                           __builtin_bit_cast(float, rdl(__builtin_bit_cast(int, Mx), 32)), __builtin_bit_cast(float, rdl(__builtin_bit_cast(int, Lx), 32))};
        if (lane < 8) __builtin_amdgcn_raw_buffer_store_b128(__builtin_bit_cast(u32x4, mlv), prsrc, 1024 + lane * 16, so, 16);
        m &= m - 1;
      }
    }
    STAMP(6);
  }
}

extern "C" void kernel_launch(void* const* d_in, const int* in_sizes, int n_in, void* d_out, int out_size, void* d_ws, size_t ws_size, hipStream_t stream) {
  Args a;
  a.q = (const bf16x8*)d_in[0]; a.k = d_in[1]; a.v = d_in[2]; a.bt = (const int*)d_in[3]; a.lens = (const int*)d_in[4];
  a.out = (unsigned*)d_out; a.ws = (unsigned char*)d_ws; a.kv_bytes = (unsigned)in_sizes[1] * 2u; a.pad = 0;
  hipFuncSetAttribute((const void*)radix_attn, hipFuncAttributeMaxDynamicSharedMemorySize, LDS_BYTES);
  hipLaunchKernelGGL(radix_attn, dim3(256), dim3(NTHR), LDS_BYTES, stream, a);
}
```

```cpp
#include <hip/hip_runtime.h>
#include <hip/hip_bf16.h>
#include <stdint.h>
#include <type_traits>

constexpr int NSLOT = 16;
constexpr int INFL = 20;
constexpr int CLS01 = 4;
constexpr int SHCAP = 21;
constexpr int POLLAT = 9;
constexpr int NA = 8;
constexpr unsigned PTESTEP = 8u << 20;
constexpr int PTEH = 2;

constexpr int B = 16, HQ = 32, HKV = 16, D = 128, PAGE = 16, NB = 256, LK = NB * PAGE;
constexpr int NSW = 4, NTHR = (NSW + 2) * 64;
constexpr float C2 = 0.088388347648318440f * 1.4426950408889634f;
constexpr unsigned PAGE_BYTES = PAGE * HKV * D * 2;
constexpr unsigned TOK_BYTES = HKV * D * 2;

constexpr int ROW = 528;
constexpr int L_RING = 0;
constexpr int L_MRG = NSLOT * 8192;
constexpr int L_TAB = L_RING + (NSLOT - 2) * 8192;
constexpr int L_RECX = L_MRG + 32 * ROW;
constexpr int L_LIST = L_RECX + 4 * 2 * ROW;
constexpr int L_MISC = L_LIST + 1024;
constexpr int C_LANDED = 0, C_CSTEP = 4, C_RCNT = 8, C_RFREE = 12, C_REL = 16, C_P = 32, C_LIST = 36, C_TL = 44, C_READY = 40, C_PTR = 48, C_LENS = 80;
constexpr int LDS_BYTES = L_MISC + 160;
static_assert(NSLOT % 4 == 0 && (NSLOT & (NSLOT - 1)) == 0, "ring geometry");
static_assert(LDS_BYTES <= 160 * 1024, "LDS");
static_assert(NA % 4 == 0 && NA <= NSLOT - 2, "the table image lives in the last two ring slots until the list is built");
static_assert(INFL + CLS01 + 4 <= 28, "closing waits");

constexpr size_t W_EPOCH = 0, W_FLAG = 256, FLAG_STRIDE = 128;
constexpr size_t W_PART = 65536;
constexpr unsigned PS_BYTES = 32 * 512 + 256, PX_BYTES = 2 * 512 + 128;
constexpr unsigned W_PX = 256 * PS_BYTES;
constexpr unsigned PART_BYTES = W_PX + 256 * 16 * PX_BYTES;

typedef __bf16 bf16x8 __attribute__((ext_vector_type(8)));
typedef __bf16 bf16x2 __attribute__((ext_vector_type(2)));
typedef short s16x4 __attribute__((ext_vector_type(4)));
typedef float f32x4 __attribute__((ext_vector_type(4)));
typedef float f32x2 __attribute__((ext_vector_type(2)));
typedef unsigned u32x4 __attribute__((ext_vector_type(4)));
typedef unsigned u32x2 __attribute__((ext_vector_type(2)));
typedef int i32x4 __attribute__((ext_vector_type(4)));
#define LAS __attribute__((address_space(3)))
#define GAS __attribute__((address_space(1)))
#define DI __device__ __forceinline__
typedef GAS unsigned long long gu64;
#define RLX_AGENT __ATOMIC_RELAXED, __HIP_MEMORY_SCOPE_AGENT

DI int rfl(int x) { return __builtin_amdgcn_readfirstlane(x); }
DI int rdl(int x, int l) { return __builtin_amdgcn_readlane(x, l); }
template <int CTRL> DI int dpp_i(int oldv, int x) { return __builtin_amdgcn_update_dpp(oldv, x, CTRL, 0xf, 0xf, false); }
DI int row_scan_add(int x) {
  x += dpp_i<0x111>(0, x); x += dpp_i<0x112>(0, x); x += dpp_i<0x114>(0, x); x += dpp_i<0x118>(0, x); return x;
}
DI int row_scan_max(int x) {
  x = max(x, dpp_i<0x111>(0, x)); x = max(x, dpp_i<0x112>(0, x)); x = max(x, dpp_i<0x114>(0, x)); x = max(x, dpp_i<0x118>(0, x)); return x;
}
DI float xrow16_max(float x) {
  auto s = __builtin_amdgcn_permlane16_swap(__float_as_uint(x), __float_as_uint(x), false, false);
  x = fmaxf(__uint_as_float(s[0]), __uint_as_float(s[1]));
  auto t = __builtin_amdgcn_permlane32_swap(__float_as_uint(x), __float_as_uint(x), false, false);
  return fmaxf(__uint_as_float(t[0]), __uint_as_float(t[1]));
}
DI float xrow16_sum(float x) {
  auto s = __builtin_amdgcn_permlane16_swap(__float_as_uint(x), __float_as_uint(x), false, false);
  x = __uint_as_float(s[0]) + __uint_as_float(s[1]);
  auto t = __builtin_amdgcn_permlane32_swap(__float_as_uint(x), __float_as_uint(x), false, false);
  return __uint_as_float(t[0]) + __uint_as_float(t[1]);
}
DI unsigned cvtpk(float lo, float hi) { f32x2 v = {lo, hi}; bf16x2 b = __builtin_convertvector(v, bf16x2); return __builtin_bit_cast(unsigned, b); }
DI float ex2(float x) { return __builtin_amdgcn_exp2f(x); }
DI void wg_barrier() { asm volatile("s_waitcnt lgkmcnt(0)\n\ts_barrier" ::: "memory"); }
template <int N> DI void wait_vm() { asm volatile("s_waitcnt vmcnt(%0)" :: "i"(N) : "memory"); }

DI void set_m0(unsigned m0v, unsigned so) { asm volatile("s_mov_b32 m0, %0\n\ts_nop 3" :: "s"(m0v), "s"(so) : "memory"); }
template <int X> DI void wait_dma4(__amdgpu_buffer_rsrc_t rsrc, unsigned soff, unsigned v0, unsigned v1, unsigned v2, unsigned v3) {
  asm volatile("s_waitcnt vmcnt(%6)\n\t"
               "buffer_load_dwordx4 %0, %4, %5 offen nt lds\n\t"
               "buffer_load_dwordx4 %1, %4, %5 offen offset:1024 nt lds\n\t"
               "buffer_load_dwordx4 %2, %4, %5 offen offset:2048 nt lds\n\t"
               "buffer_load_dwordx4 %3, %4, %5 offen offset:3072 nt lds"
               :: "v"(v0), "v"(v1), "v"(v2), "v"(v3), "s"(rsrc), "s"(soff), "i"(X) : "memory");
}
DI void dma4r(__amdgpu_buffer_rsrc_t r0, __amdgpu_buffer_rsrc_t r1, __amdgpu_buffer_rsrc_t r2, __amdgpu_buffer_rsrc_t r3, unsigned soff, unsigned v0, unsigned v1, unsigned v2, unsigned v3) {
  asm volatile("s_nop 0\n\t"
               "buffer_load_dwordx4 %0, %4, %8 offen nt lds\n\t"
               "buffer_load_dwordx4 %1, %5, %8 offen offset:1024 nt lds\n\t"
               "buffer_load_dwordx4 %2, %6, %8 offen offset:2048 nt lds\n\t"
               "buffer_load_dwordx4 %3, %7, %8 offen offset:3072 nt lds"
               :: "v"(v0), "v"(v1), "v"(v2), "v"(v3), "s"(r0), "s"(r1), "s"(r2), "s"(r3), "s"(soff) : "memory");
}
#define WAIT_K(X, so) wait_dma4<X>(rk, so, vk[0], vk[1], vk[2], vk[3])
#define WAIT_V(X, so) wait_dma4<X>(rv, so, vv[0], vv[1], vv[2], vv[3])

template <int X>
DI void page_asm(__amdgpu_buffer_rsrc_t rk, __amdgpu_buffer_rsrc_t rv, const unsigned (&vk)[4], const unsigned (&vv)[4], unsigned vctl, unsigned vp,
                 int& i, int& pub, unsigned& so, unsigned& mk) {
  unsigned vt;
  asm volatile("s_waitcnt vmcnt(%[X])\n\t"
               "buffer_load_dwordx4 %[k0], %[rk], %[so] offen nt lds\n\t"
               "buffer_load_dwordx4 %[k1], %[rk], %[so] offen offset:1024 nt lds\n\t"
               "buffer_load_dwordx4 %[k2], %[rk], %[so] offen offset:2048 nt lds\n\t"
               "buffer_load_dwordx4 %[k3], %[rk], %[so] offen offset:3072 nt lds\n\t"
               "v_mov_b32 %[vt], %[pub]\n\t"
               "s_add_u32 m0, m0, 0x1000\n\t"
               "ds_write_b32 %[vctl], %[vt]\n\t"
               "s_add_u32 %[pub], %[pub], 1\n\t"
               "s_waitcnt vmcnt(%[X])\n\t"
               "buffer_load_dwordx4 %[v0], %[rv], %[so] offen nt lds\n\t"
               "buffer_load_dwordx4 %[v1], %[rv], %[so] offen offset:1024 nt lds\n\t"
               "buffer_load_dwordx4 %[v2], %[rv], %[so] offen offset:2048 nt lds\n\t"
               "buffer_load_dwordx4 %[v3], %[rv], %[so] offen offset:3072 nt lds\n\t"
               "v_mov_b32 %[vt], %[pub]\n\t"
               "s_add_u32 %[i], %[i], 1\n\t"
               "ds_write_b32 %[vctl], %[vt]\n\t"
               "v_readlane_b32 %[so], %[vp], %[i]\n\t"
               "s_add_u32 %[pub], %[pub], 1\n\t"
               "s_add_u32 %[mk], %[mk], 0x2000\n\t"
               "s_and_b32 %[mk], %[mk], %[msk]\n\t"
               "s_mov_b32 m0, %[mk]"
               : [i] "+s"(i), [pub] "+s"(pub), [so] "+s"(so), [mk] "+s"(mk), [vt] "=&v"(vt)
               : [X] "i"(X), [msk] "i"(NSLOT * 8192 - 1), [k0] "v"(vk[0]), [k1] "v"(vk[1]), [k2] "v"(vk[2]), [k3] "v"(vk[3]), [v0] "v"(vv[0]), [v1] "v"(vv[1]), [v2] "v"(vv[2]), [v3] "v"(vv[3]),
                 [rk] "s"(rk), [rv] "s"(rv), [vctl] "v"(vctl), [vp] "v"(vp) : "memory", "scc");
}

struct Args { const bf16x8* q; const void* k; const void* v; const int* bt; const int* lens; unsigned* out; unsigned char* ws; unsigned kv_bytes; unsigned pad; };

DI float mfac(float m, float M) { return (m == -INFINITY) ? 0.f : ex2((m - M) * C2); }

struct Soft { float m, l; f32x4 o[8]; };
DI void soft_reset(Soft& s) { s.m = -INFINITY; s.l = 0.f;
#pragma unroll
  for (int i = 0; i < 8; ++i) s.o[i] = f32x4{0.f, 0.f, 0.f, 0.f}; }

DI s16x4 soft_step(Soft& s, f32x4 x, bool mask, int tok0, int len) {
  if (mask) {
#pragma unroll
    for (int r = 0; r < 4; ++r) x[r] = (tok0 + r < len) ? x[r] : -INFINITY;
  }
  float pm = fmaxf(fmaxf(x[0], x[1]), fmaxf(x[2], x[3]));
  pm = xrow16_max(pm);
  const float mn = fmaxf(s.m, pm);
  const float ms = (mn == -INFINITY) ? 0.f : mn * C2;
  const float alpha = ex2(s.m * C2 - ms);
  float p[4];
#pragma unroll
  for (int r = 0; r < 4; ++r) p[r] = ex2(fmaf(x[r], C2, -ms));
  s.l = fmaf(s.l, alpha, (p[0] + p[1]) + (p[2] + p[3]));
  s.m = mn;
#pragma unroll
  for (int i = 0; i < 8; ++i) s.o[i] *= alpha;
  u32x2 pk = {cvtpk(p[0], p[1]), cvtpk(p[2], p[3])};
  return __builtin_bit_cast(s16x4, pk);
}


DI int lds_ld(LAS unsigned char* p) { return __hip_atomic_load((LAS int*)p, __ATOMIC_RELAXED, __HIP_MEMORY_SCOPE_WORKGROUP); }
DI void lds_st(LAS unsigned char* p, int v) { __hip_atomic_store((LAS int*)p, v, __ATOMIC_RELAXED, __HIP_MEMORY_SCOPE_WORKGROUP); }
DI int lds_wait_ge(LAS unsigned char* p, int need) {
  int v;
#pragma nounroll
  for (unsigned spins = 0;; ++spins) { v = rfl(lds_ld(p)); if (v >= need || spins > (1u << 20)) break; __builtin_amdgcn_s_sleep(1); }
  asm volatile("" ::: "memory");
  return v;
}
DI void lds_drain() { asm volatile("s_waitcnt lgkmcnt(0)" ::: "memory"); }

__global__ void __launch_bounds__(NTHR, 2) radix_attn(Args) {
  extern __shared__ __attribute__((aligned(16))) unsigned char lds_raw[];
  LAS unsigned char* lds = (LAS unsigned char*)lds_raw;
  LAS unsigned char* ctl = lds + L_MISC;
  const int tid = threadIdx.x, lane = tid & 63, wave = rfl(tid >> 6);
  const int w = blockIdx.x & 15, h = blockIdx.x >> 4;
  const int c = lane & 15, g = lane >> 4;
  int len_raw;

  struct Part { int len, Pe, S, E, spare, exc, lo, hi, xoff, S_w, spare_w, T_w, ns, ss, hp, nsh, np_w, own_w, vA, TL; unsigned xmask; bool all_masked; };
  auto partition = [&](int P) {
    Part p;
    p.all_masked = len_raw <= 0;
    p.len = p.all_masked ? LK : min(len_raw, LK);
    const int np = (p.len + PAGE - 1) >> 4;
    p.Pe = min(P, rdl(row_scan_max(np), 15));
    const int n = max(np - p.Pe, 0);
    const int W = p.Pe + rdl(row_scan_add(n), 15);
    const int T = (W >> 4) + (c < (W & 15) ? 1 : 0);
    const int cap = max(1, ((W >> 4) * SHCAP) >> 6);
    int lg = 0;
    if (p.Pe > cap) lg = 1;
    if (p.Pe > 2 * cap) lg = 2;
    if (p.Pe > 4 * cap) lg = 3;
    if (p.Pe > 8 * cap) lg = 4;
    const int q = p.Pe >> lg, rq = p.Pe & ((1 << lg) - 1);
    const int sh = c < (1 << lg) ? q + (c < rq ? 1 : 0) : 0;
    p.nsh = min(1 << lg, p.Pe);
    p.ns = w < (1 << lg) ? q + (w < rq ? 1 : 0) : 0;
    p.ss = w * q + min(w, rq);
    const int own = min(n, T - sh);
    p.exc = n - own; p.spare = T - sh - own;
    p.S = row_scan_add(p.spare) - p.spare;
    p.E = row_scan_add(p.exc) - p.exc;
    p.S_w = rdl(p.S, w); p.spare_w = rdl(p.spare, w); p.T_w = rdl(T, w);
    p.hp = p.ns + p.spare_w;
    p.lo = max(p.S_w, p.E); p.hi = min(p.S_w + p.spare_w, p.E + p.exc);
    p.xmask = (unsigned)(__ballot(p.hi > p.lo) & 0xFFFFull);
    p.xoff = p.Pe - p.E;
    p.np_w = rdl(np, w); p.own_w = rdl(own, w);
    p.vA = min(NA, p.own_w);
    p.TL = NA + p.hp + p.own_w - p.vA;
    return p;
  };

  if (__builtin_expect(wave == 0, 1)) {
    __builtin_amdgcn_s_setprio(3);
    typedef unsigned u32x16 __attribute__((ext_vector_type(16)));
    u32x16 ka = *(const __attribute__((address_space(4))) u32x16*)__builtin_amdgcn_kernarg_segment_ptr();
    asm volatile("" : "+s"(ka));
    const uintptr_t kptr = ((uintptr_t)ka[3] << 32) | ka[2], vptr = ((uintptr_t)ka[5] << 32) | ka[4], btptr = ((uintptr_t)ka[7] << 32) | ka[6], lensptr = ((uintptr_t)ka[9] << 32) | ka[8];
    const unsigned kv_bytes = ka[14];
    len_raw = *(const GAS int*)(lensptr + 4 * c);
    const i32x4 rw = *(const GAS i32x4*)(btptr + w * (NB * 4) + 16 * lane);
    asm volatile("s_barrier" ::: "memory");
    unsigned vk[4], vv[4];
#pragma unroll
    for (int i = 0; i < 4; ++i) { const int t = 4 * i + g;
      vk[i] = t * TOK_BYTES + ((c ^ t) << 4) - 1024 * i;
      vv[i] = t * TOK_BYTES + ((c ^ ((t & 7) << 1)) << 4) - 1024 * i; }
    asm volatile("" : "+v"(vk[0]), "+v"(vk[1]), "+v"(vk[2]), "+v"(vk[3]), "+v"(vv[0]), "+v"(vv[1]), "+v"(vv[2]), "+v"(vv[3]));
    const unsigned ring = (unsigned)(uintptr_t)(lds + L_RING);
    const __amdgpu_buffer_rsrc_t rk = __builtin_amdgcn_make_buffer_rsrc((void*)kptr, 0, kv_bytes, 0x00020000);
    const __amdgpu_buffer_rsrc_t rv = __builtin_amdgcn_make_buffer_rsrc((void*)vptr, 0, kv_bytes, 0x00020000);
    *(LAS i32x4*)(lds + L_MRG + 16 * lane) = rw;
    unsigned vp0, vp1 = 0, vp2 = 0, vp3 = 0;
    int nq0;
    {
      const int lw = rdl(len_raw, w);
      const int npw = ((lw <= 0 ? LK : min(lw, LK)) + PAGE - 1) >> 4;
      vp0 = (unsigned)*(LAS int*)(lds + L_MRG + 4 * max(npw - 1 - lane, 0)) * PAGE_BYTES + (unsigned)h * (D * 2);
      nq0 = ((lw <= 0 ? LK : min(lw, LK)) - (npw - 1) * PAGE + 3) >> 2;
    }
    unsigned so = (unsigned)rdl((int)vp0, 0), m0k = ring;
    {
      const __amdgpu_buffer_rsrc_t k1 = __builtin_amdgcn_make_buffer_rsrc((void*)kptr, 0, nq0 > 1 ? kv_bytes : 0u, 0x00020000), k2 = __builtin_amdgcn_make_buffer_rsrc((void*)kptr, 0, nq0 > 2 ? kv_bytes : 0u, 0x00020000),
                                   k3 = __builtin_amdgcn_make_buffer_rsrc((void*)kptr, 0, nq0 > 3 ? kv_bytes : 0u, 0x00020000);
      set_m0(m0k, so);
      dma4r(rk, k1, k2, k3, so, vk[0], vk[1], vk[2], vk[3]);
      const __amdgpu_buffer_rsrc_t v1 = __builtin_amdgcn_make_buffer_rsrc((void*)vptr, 0, nq0 > 1 ? kv_bytes : 0u, 0x00020000), v2 = __builtin_amdgcn_make_buffer_rsrc((void*)vptr, 0, nq0 > 2 ? kv_bytes : 0u, 0x00020000),
                                   v3 = __builtin_amdgcn_make_buffer_rsrc((void*)vptr, 0, nq0 > 3 ? kv_bytes : 0u, 0x00020000);
      set_m0(m0k + 4096, so);
      dma4r(rv, v1, v2, v3, so, vv[0], vv[1], vv[2], vv[3]);
      so = (unsigned)rdl((int)vp0, 1); m0k = ring + 8192;
      set_m0(m0k, so);
    }
    static_assert(L_RING == 0 && C_LANDED == 0 && C_REL == 16, "page_asm addresses the ring from LDS offset 0 and the control words by these offsets");
    const unsigned vctl = (unsigned)(uintptr_t)ctl;
    auto publish = [&](int nl) { lds_st(ctl + C_LANDED, nl); };
    const bool wide = ((((unsigned)kptr >> 8) + h) & 3) == 1;
    int i = 1, credit = NSLOT;
    int T_w = NA, TM = NA;
    bool listed = false;
    unsigned mk = m0k;
    for (;;) {
      i = rfl(i); so = (unsigned)rfl((int)so); mk = (unsigned)rfl((int)mk);
      const int iend = rfl(min(min(TM, credit), (i & ~63) + 64));
      if (wide) { int pub = 2 * i - (INFL + CLS01 + 3) / 4;
#pragma nounroll
        while (i < iend) page_asm<INFL + CLS01>(rk, rv, vk, vv, vctl, vp0, i, pub, so, mk);
      } else { int pub = 2 * i - (INFL + 3) / 4;
#pragma nounroll
        while (i < iend) page_asm<INFL>(rk, rv, vk, vv, vctl, vp0, i, pub, so, mk);
      }
      if (!listed) {
        listed = true;
        lds_wait_ge(ctl + C_LIST, 1);
        T_w = rfl(*(LAS int*)(ctl + C_TL));
        TM = T_w >= NA + 4 ? T_w - 4 : T_w;
        vp0 = *(LAS unsigned*)(lds + L_LIST + 4 * lane); vp1 = *(LAS unsigned*)(lds + L_LIST + 256 + 4 * lane);
        vp2 = *(LAS unsigned*)(lds + L_LIST + 512 + 4 * lane); vp3 = *(LAS unsigned*)(lds + L_LIST + 768 + 4 * lane);
        so = (unsigned)rdl((int)vp0, NA);
        asm volatile("s_nop 4" :: "s"(so));
        if (i < min(TM, credit)) continue;
      }
      if (i >= T_w) break;
      const bool endgroup = i >= TM;
      const u32x4 rel = *(volatile LAS u32x4*)(ctl + C_REL);
      credit = min(min(4 * rfl((int)rel[0]), 4 * rfl((int)rel[1]) + 1), min(4 * rfl((int)rel[2]) + 2, 4 * rfl((int)rel[3]) + 3)) + NSLOT;
      if ((i & 63) == 0) { vp0 = vp1; vp1 = vp2; vp2 = vp3; so = (unsigned)rdl((int)vp0, 0); asm volatile("s_nop 4" :: "s"(so)); }
      if (credit <= i) {
        LAS unsigned char* relp = ctl + C_REL + 4 * (i & 3);
        const int need = (i >> 2) - NSLOT / 4 + 1;
        if (rfl(lds_ld(relp)) < need) { wait_vm<12>(); publish(2 * i - 3);
          if (rfl(lds_ld(relp)) < need) { wait_vm<4>(); publish(2 * i - 1);
            if (rfl(lds_ld(relp)) < need) { wait_vm<0>(); publish(2 * i); lds_wait_ge(relp, need); } } }
        credit = i + 1;
      }
      if (endgroup) {
        if (credit < T_w) { wait_vm<0>(); publish(2 * i);
          for (int e = i; e < T_w; ++e) if (e >= NSLOT) lds_wait_ge(ctl + C_REL + 4 * (e & 3), (e >> 2) - NSLOT / 4 + 1); }
        unsigned se[4];
        int pub = 2 * i - (INFL + 3) / 4;
#pragma unroll
        for (int r = 0; r < 4; ++r) {
          if (r > 0) { if (((i + r) & 63) == 0) { vp0 = vp1; vp1 = vp2; } so = (unsigned)rdl((int)vp0, (i + r) & 63); }
          se[r] = so;
          set_m0(((i + r) & (NSLOT - 1)) * 8192, so);
          WAIT_K(INFL, so);
          publish(pub + r);
        }
#pragma unroll
        for (int r = 0; r < 4; ++r) {
          set_m0(((i + r) & (NSLOT - 1)) * 8192 + 4096, se[r]);
          WAIT_V(INFL, se[r]);
          publish(pub + 4 + r);
        }
        break;
      }
    }
    wait_vm<24>(); publish(2 * T_w - 6);
    wait_vm<20>(); publish(2 * T_w - 5);
    wait_vm<16>(); publish(2 * T_w - 4);
    wait_vm<12>(); publish(2 * T_w - 3);
    wait_vm<8>(); publish(2 * T_w - 2);
    wait_vm<4>(); publish(2 * T_w - 1);
    wait_vm<0>(); publish(2 * T_w);
  } else if (wave <= NSW) {
    const int cw = wave - 1;
    wg_barrier();
    const unsigned ka0 = c * 256 + ((g ^ c) << 4);
    const int tv = 4 * g + (c >> 2);
    const unsigned va0 = 4096 + tv * 256 + ((tv & 7) << 5) + (c & 3) * 8;
    lds_wait_ge(ctl + C_READY, 1);
    bf16x8 qf[2][4];
    {
      const u32x2 pq = *(LAS u32x2*)(ctl + C_PTR);
      const GAS bf16x8* qp = (const GAS bf16x8*)(((uintptr_t)(unsigned)rfl((int)pq[1]) << 32) | (unsigned)rfl((int)pq[0]));
#pragma unroll
      for (int cg = 0; cg < 2; ++cg)
#pragma unroll
        for (int s = 0; s < 4; ++s) { const int r = 16 * cg + c; qf[cg][s] = qp[(((r >> 1) * HQ + 2 * h + (r & 1)) * D + 32 * s + 8 * g) / 8]; }
    }
    len_raw = *(LAS int*)(ctl + C_LENS + 4 * c);
    const Part p = partition(rfl(*(LAS int*)(ctl + C_P)));
    const int len = p.len, S_w = p.S_w - p.ns - NA, hp = p.hp, ss = p.ss, T_w = p.TL, lo = p.lo, hi = p.hi, xoff = p.xoff, ns = p.ns, lpA = p.np_w - 1, lpB = p.np_w - p.own_w, vA = p.vA;
    const int minlen = LK - rdl(row_scan_max(LK - len), 15);
    const int len0 = __shfl(len, c >> 1), len1 = __shfl(len, 8 + (c >> 1));
    {
      const bool z0 = __shfl((int)p.all_masked, c >> 1) != 0, z1 = __shfl((int)p.all_masked, 8 + (c >> 1)) != 0;
#pragma unroll
      for (int s = 0; s < 4; ++s) {
        u32x4 t0 = __builtin_bit_cast(u32x4, qf[0][s]), t1 = __builtin_bit_cast(u32x4, qf[1][s]);
#pragma unroll
        for (int e = 0; e < 4; ++e) { t0[e] = z0 ? 0u : t0[e]; t1[e] = z1 ? 0u : t1[e]; }
        qf[0][s] = __builtin_bit_cast(bf16x8, t0); qf[1][s] = __builtin_bit_cast(bf16x8, t1);
      }
    }
    const int TM = T_w >= NA + 4 ? T_w - 4 : T_w + 4;
    int j = 0, seen = 0;
    Soft s0, s1;
    auto page = [&](auto SHc, const bf16x8 (&qa)[4], const bf16x8 (&qb)[4], int la, int lb, int t0, bool mask) {
      constexpr bool SH = decltype(SHc)::value;
      const int i = 4 * j + cw;
      LAS unsigned char* sl = lds + L_RING + (i & (NSLOT - 1)) * 8192;
      const int re = i - TM;
      const int needk = re >= 0 ? 2 * TM + re + 1 : 2 * i + 1, needv = re >= 0 ? 2 * TM + re + 5 : 2 * i + 2;
      if (seen < needk) seen = lds_wait_ge(ctl + C_LANDED, needk);
      bf16x8 kf[4];
#pragma unroll
      for (int s = 0; s < 4; ++s) kf[s] = *(LAS bf16x8*)(sl + (ka0 ^ (s << 6)));
      f32x4 xa = {0.f, 0.f, 0.f, 0.f}, xb = {0.f, 0.f, 0.f, 0.f};
#pragma unroll
      for (int s = 0; s < 4; ++s) { xa = __builtin_amdgcn_mfma_f32_16x16x32_bf16(kf[s], qa[s], xa, 0, 0, 0);
        if (SH) xb = __builtin_amdgcn_mfma_f32_16x16x32_bf16(kf[s], qb[s], xb, 0, 0, 0); }
      const s16x4 pa = soft_step(s0, xa, mask, t0 + 4 * g, la);
      s16x4 pb; if (SH) pb = soft_step(s1, xb, mask, t0 + 4 * g, lb);
      if (seen < needv) seen = lds_wait_ge(ctl + C_LANDED, needv);
      s16x4 vf[8];
#pragma unroll
      for (int blk = 0; blk < 8; ++blk) vf[blk] = __builtin_amdgcn_ds_read_tr16_b64_v4i16((LAS s16x4*)(sl + (va0 ^ (blk << 5))));
      ++j;
      asm volatile("s_waitcnt lgkmcnt(0)" : "+v"(vf[0]), "+v"(vf[1]), "+v"(vf[2]), "+v"(vf[3]), "+v"(vf[4]), "+v"(vf[5]), "+v"(vf[6]), "+v"(vf[7]) :: "memory");
      lds_st(ctl + C_REL + 4 * cw, j);
#pragma unroll
      for (int blk = 0; blk < 8; ++blk) { s0.o[blk] = __builtin_amdgcn_mfma_f32_16x16x16bf16_1k(vf[blk], pa, s0.o[blk], 0, 0, 0);
        if (SH) s1.o[blk] = __builtin_amdgcn_mfma_f32_16x16x16bf16_1k(vf[blk], pb, s1.o[blk], 0, 0, 0); }
    };
    unsigned m = p.xmask;
    for (int kx = 0;; ++kx) {
      const bool early = kx == 0, last = !early && m == 0;
      const int b = (early || last) ? w : __builtin_ctz(m);
      const int i0 = early ? 0 : last ? NA + hp : rdl(lo, b) - S_w;
      const int i1 = early ? NA : last ? T_w : rdl(hi, b) - S_w;
      const int lp0 = last ? lpB : rdl(lo, b) + rdl(xoff, b);
      bf16x8 qa[4];
      if (b < 8) {
#pragma unroll
        for (int s = 0; s < 4; ++s) qa[s] = qf[0][s];
      } else {
#pragma unroll
        for (int s = 0; s < 4; ++s) qa[s] = qf[1][s];
      }
      const int lb = rdl(len, b);
      soft_reset(s0);
      if (last) lds_wait_ge(ctl + C_RFREE, kx);
      for (int i = 4 * j + cw; i < i1; i += 4) {
        if (early && i >= vA) {
          lds_wait_ge(ctl + C_LANDED, 2 * i + 2); ++j; lds_st(ctl + C_REL + 4 * cw, j);
        } else {
          const int t0 = (early ? lpA - i : lp0 + i - i0) * PAGE;
          page(std::false_type{}, qa, qa, lb, lb, t0, t0 + PAGE > lb);
        }
      }
      if (kx > 0 && !last) lds_wait_ge(ctl + C_RFREE, kx);
      const float lt = xrow16_sum(s0.l);
      if ((c >> 1) == (b & 7)) {
        LAS unsigned char* r = lds + L_RECX + (cw * 2 + (c & 1)) * ROW;
#pragma unroll
        for (int blk = 0; blk < 8; ++blk) *(LAS f32x4*)(r + blk * 64 + g * 16) = s0.o[blk];
        if (g == 0) *(LAS f32x2*)(r + 512) = f32x2{s0.m, lt};
      }
      lds_drain();
      if (lane == 0) __hip_atomic_fetch_add((LAS int*)(ctl + C_RCNT), 1, __ATOMIC_RELAXED, __HIP_MEMORY_SCOPE_WORKGROUP);
      if (last) break;
      if (early) {
        if (ns > 0) {
          soft_reset(s0); soft_reset(s1);
          for (int i = cw; i < ns; i += 4) { const int t0 = (ss + i) * PAGE; page(std::true_type{}, qf[0], qf[1], len0, len1, t0, t0 + PAGE > minlen); }
          if (cw < ns) {
            s0.l = xrow16_sum(s0.l); s1.l = xrow16_sum(s1.l);
            if (cw > 0) lds_wait_ge(ctl + C_CSTEP, cw);
#pragma unroll
            for (int cg = 0; cg < 2; ++cg) { Soft& s = cg ? s1 : s0;
              LAS unsigned char* r = lds + L_MRG + (16 * cg + c) * ROW;
              if (cw > 0) {
                const f32x2 ml = *(LAS f32x2*)(r + 512);
                const float M = fmaxf(ml[0], s.m), f0 = mfac(ml[0], M), f1 = mfac(s.m, M);
#pragma unroll
                for (int blk = 0; blk < 8; ++blk) s.o[blk] = *(LAS f32x4*)(r + blk * 64 + g * 16) * f0 + s.o[blk] * f1;
                s.l = ml[1] * f0 + s.l * f1; s.m = M;
              }
#pragma unroll
              for (int blk = 0; blk < 8; ++blk) *(LAS f32x4*)(r + blk * 64 + g * 16) = s.o[blk];
              lds_drain();
              if (g == 0) *(LAS f32x2*)(r + 512) = f32x2{s.m, s.l};
            }
            lds_drain();
            lds_st(ctl + C_CSTEP, cw + 1);
          }
        }
      } else m &= m - 1;
    }
  } else {
    typedef unsigned u32x16 __attribute__((ext_vector_type(16)));
    u32x16 ka = *(const __attribute__((address_space(4))) u32x16*)__builtin_amdgcn_kernarg_segment_ptr();
    if (lane < 12) *(LAS int*)(ctl + 4 * lane) = 0;
    wg_barrier();
    asm volatile("" : "+s"(ka));
    const uintptr_t btptr = ((uintptr_t)ka[7] << 32) | ka[6], lensptr = ((uintptr_t)ka[9] << 32) | ka[8];
    GAS u32x2* outp = (GAS u32x2*)(((uintptr_t)ka[11] << 32) | ka[10]);
    unsigned char* ws = (unsigned char*)(((uintptr_t)ka[13] << 32) | ka[12]);
    len_raw = *(const GAS int*)(lensptr + 4 * c);
    i32x4 rk16[16];
#pragma unroll
    for (int k = 0; k < 16; ++k) rk16[k] = *(const GAS i32x4*)(btptr + k * (NB * 4) + 16 * lane);
    unsigned touch = 0, touch2 = 0;
    if (h < PTEH) {
      const uintptr_t kb = ((uintptr_t)ka[3] << 32) | ka[2], vb = ((uintptr_t)ka[5] << 32) | ka[4];
      const unsigned off = (unsigned)(lane & 31) * PTESTEP;
      if (off < ka[14]) touch = *(const GAS unsigned*)((lane < 32 ? kb : vb) + off);
      if (lane < 2) touch2 = *(const GAS unsigned*)((uintptr_t)ws + W_PART + lane * (PART_BYTES / 2));
    }
    const unsigned long long epoch = __hip_atomic_load((gu64*)(ws + W_EPOCH), RLX_AGENT);
#pragma unroll
    for (int k = 0; k < 16; ++k) *(LAS i32x4*)(lds + L_TAB + (k * NB + 4 * lane) * 4) = rk16[k];
    i32x4 acc = rk16[1] ^ rk16[0];
#pragma unroll
    for (int k = 2; k < 16; ++k) acc |= rk16[k] ^ rk16[0];
    int P;
    {
      const unsigned long long b0 = __ballot(acc[0] != 0), b1 = __ballot(acc[1] != 0), b2 = __ballot(acc[2] != 0), b3 = __ballot(acc[3] != 0);
      const unsigned long long any = b0 | b1 | b2 | b3;
      const int l = any ? (int)__builtin_ctzll(any) : 64;
      const unsigned long long bit = any & (0ull - any);
      P = 4 * l + ((b0 & bit) ? 0 : (b1 & bit) ? 1 : (b2 & bit) ? 2 : (b3 & bit) ? 3 : 0);
    }
    if (lane == 0) { *(LAS int*)(ctl + C_P) = P; *(LAS u32x2*)(ctl + C_PTR) = u32x2{ka[0], ka[1]}; }
    if (lane < 16) *(LAS int*)(ctl + C_LENS + 4 * lane) = len_raw;
    lds_drain();
    lds_st(ctl + C_READY, 1);
    const Part p = partition(P);
    {
      const int S_w = p.S_w - p.ns - NA, lpA = p.np_w - 1, lpB = p.np_w - p.own_w;
#pragma unroll
      for (int r = 0; r < 4; ++r) {
        const int i = 64 * r + lane;
        int idx = (i < NA) ? w * NB + max(lpA - i, 0) : (i < NA + p.ns) ? p.ss + (i - NA) : w * NB + lpB + (i - NA - p.hp);
        for (unsigned mm = p.xmask; mm; mm &= mm - 1) {
          const int b = __builtin_ctz(mm);
          const int lo_b = rdl(p.lo, b) - S_w, hi_b = rdl(p.hi, b) - S_w, off_b = rdl(p.xoff, b) + b * NB + S_w;
          if (i >= lo_b && i < hi_b) idx = i + off_b;
        }
        *(LAS unsigned*)(lds + L_LIST + 4 * i) = (unsigned)*(LAS int*)(lds + L_TAB + 4 * (i < p.TL ? idx : 0)) * PAGE_BYTES + (unsigned)h * (D * 2);
      }
      if (lane == 0) *(LAS int*)(ctl + C_TL) = p.TL;
      lds_drain();
      lds_st(ctl + C_LIST, 1);
    }
    asm volatile("" :: "v"(touch), "v"(touch2));
    const __amdgpu_buffer_rsrc_t prsrc = __builtin_amdgcn_make_buffer_rsrc(ws + W_PART, 0, PART_BYTES, 0x00020000);
    const unsigned long long tag = epoch + 1ull;
    const int ns = p.ns;
    const unsigned blk = w * 16 + h;
    const int r = lane >> 5, ch = lane & 31;
    float M = -INFINITY, L = 0.f; f32x4 o = {0.f, 0.f, 0.f, 0.f};
    unsigned m = p.xmask;
#pragma nounroll
    for (int kx = 0;; ++kx) {
      const bool early = kx == 0, last = !early && m == 0;
      if (kx == 1 && ns > 0) {
        lds_wait_ge(ctl + C_CSTEP, min(ns, NSW));
#pragma unroll 4
        for (int k = 0; k < 16; ++k) {
          const u32x4 v = *(LAS u32x4*)(lds + L_MRG + (2 * k + r) * ROW + ch * 16);
          __builtin_amdgcn_raw_buffer_store_b128(v, prsrc, k * 1024 + lane * 16, blk * PS_BYTES, 16); }
        if (lane < 32) { const u32x2 v = *(LAS u32x2*)(lds + L_MRG + lane * ROW + 512);
          __builtin_amdgcn_raw_buffer_store_b64(v, prsrc, 32 * 512 + lane * 8, blk * PS_BYTES, 16); }
      }
      if (last) {
        asm volatile("s_waitcnt vmcnt(0)" ::: "memory");
        if (lane == 0) __hip_atomic_store((gu64*)(ws + W_FLAG + blk * FLAG_STRIDE), tag, RLX_AGENT);
        const int E_w = rdl(p.E, w), exc_w = rdl(p.exc, w);
        const bool sh_ov = c < p.nsh;
        const bool ex_ov = min(p.S + p.spare, E_w + exc_w) > max(p.S, E_w);
        unsigned cmask = (unsigned)(__ballot(sh_ov) & 0xFFFFull) | ((unsigned)(__ballot(ex_ov) & 0xFFFFull) << 16);
        if (cmask) lds_wait_ge(ctl + C_LANDED, (p.TL * POLLAT) >> 3);
        {
          const bool need = lane < 16 && (sh_ov || ex_ov);
          gu64* f = (gu64*)(ws + W_FLAG + (size_t)(c * 16 + h) * FLAG_STRIDE);
#pragma nounroll
          for (unsigned spins = 0; spins < (1u << 16); ++spins) {
            const bool ok = !need || __hip_atomic_load(f, RLX_AGENT) == tag;
            if (__all(ok)) break;
            __builtin_amdgcn_s_sleep(32);
          }
          asm volatile("" ::: "memory");
        }
        const unsigned vso = lane < 16 ? (unsigned)(c * 16 + h) * PS_BYTES + (unsigned)(2 * w) * 512 : W_PX + ((unsigned)(c * 16 + h) * 16 + w) * PX_BYTES;
        const unsigned vsm = lane < 16 ? (unsigned)(c * 16 + h) * PS_BYTES + 32 * 512 + (unsigned)(2 * w) * 8 : vso + 1024;
        constexpr int GB = 6;
        while (cmask) {
          u32x4 po[GB]; u32x2 pml[GB]; bool pv[GB];
#pragma unroll
          for (int k = 0; k < GB; ++k) {
            pv[k] = cmask != 0;
            const int id = pv[k] ? __builtin_ctz(cmask) : 0; cmask &= cmask - 1;
            const unsigned so = pv[k] ? (unsigned)rdl((int)vso, id) : PART_BYTES, sm = pv[k] ? (unsigned)rdl((int)vsm, id) : PART_BYTES;
            po[k] = __builtin_amdgcn_raw_buffer_load_b128(prsrc, lane * 16 + so, 0, 16);
            pml[k] = __builtin_amdgcn_raw_buffer_load_b64(prsrc, r * 8 + sm, 0, 16);
          }
#pragma unroll
          for (int k = 0; k < GB; ++k) {
            const float mk = pv[k] ? __uint_as_float(pml[k][0]) : -INFINITY, lk = __uint_as_float(pml[k][1]);
            const float Mn = fmaxf(M, mk), f0 = mfac(M, Mn), f1 = mfac(mk, Mn);
            o = o * f0 + __builtin_bit_cast(f32x4, po[k]) * f1; L = L * f0 + lk * f1; M = Mn;
          }
        }
        if (blk == 0) {
#pragma nounroll
          for (unsigned spins = 0; spins < (1u << 16); ++spins) {
            bool ok = true;
#pragma unroll
            for (int k = 0; k < 4; ++k) ok &= __hip_atomic_load((gu64*)(ws + W_FLAG + (size_t)(k * 64 + lane) * FLAG_STRIDE), RLX_AGENT) == tag;
            if (__all(ok)) break;
            __builtin_amdgcn_s_sleep(8);
          }
          if (lane == 0) __hip_atomic_store((gu64*)(ws + W_EPOCH), tag, RLX_AGENT);
        }
      }
      lds_wait_ge(ctl + C_RCNT, NSW * (kx + 1));
      f32x2 ml[4]; f32x4 ok[4];
#pragma unroll
      for (int k = 0; k < 4; ++k) { ml[k] = *(LAS f32x2*)(lds + L_RECX + (k * 2 + r) * ROW + 512); ok[k] = *(LAS f32x4*)(lds + L_RECX + (k * 2 + r) * ROW + ch * 16); }
      const bool mine = early || last;
      float Mx = mine ? M : -INFINITY;
#pragma unroll
      for (int k = 0; k < 4; ++k) Mx = fmaxf(Mx, ml[k][0]);
      const float fr = mine ? mfac(M, Mx) : 0.f;
      float Lx = L * fr; f32x4 ox = o * fr;
#pragma unroll
      for (int k = 0; k < 4; ++k) { const float f = mfac(ml[k][0], Mx); Lx = fmaf(f, ml[k][1], Lx); ox += ok[k] * f; }
      asm volatile("s_waitcnt lgkmcnt(0)" : "+v"(ox) :: "memory");
      if (last) {
        const float inv = __builtin_amdgcn_rcpf(Lx);
        outp[((w * HQ + 2 * h + r) * D + 4 * ch) >> 2] = u32x2{cvtpk(ox[0] * inv, ox[1] * inv), cvtpk(ox[2] * inv, ox[3] * inv)};
        break;
      }
      lds_st(ctl + C_RFREE, kx + 1);
      if (early) { M = Mx; L = Lx; o = ox; }
      else {
        const unsigned so = W_PX + (blk * 16 + __builtin_ctz(m)) * PX_BYTES;
        __builtin_amdgcn_raw_buffer_store_b128(__builtin_bit_cast(u32x4, ox), prsrc, lane * 16, so, 16);
        const f32x4 mlv = {__builtin_bit_cast(float, rdl(__builtin_bit_cast(int, Mx), 0)), __builtin_bit_cast(float, rdl(__builtin_bit_cast(int, Lx), 0)),
                           __builtin_bit_cast(float, rdl(__builtin_bit_cast(int, Mx), 32)), __builtin_bit_cast(float, rdl(__builtin_bit_cast(int, Lx), 32))};
        if (lane < 8) __builtin_amdgcn_raw_buffer_store_b128(__builtin_bit_cast(u32x4, mlv), prsrc, 1024 + lane * 16, so, 16);
        m &= m - 1;
      }
    }
  }
}

extern "C" void kernel_launch(void* const* d_in, const int* in_sizes, int n_in, void* d_out, int out_size, void* d_ws, size_t ws_size, hipStream_t stream) {
  Args a;
  a.q = (const bf16x8*)d_in[0]; a.k = d_in[1]; a.v = d_in[2]; a.bt = (const int*)d_in[3]; a.lens = (const int*)d_in[4];
  a.out = (unsigned*)d_out; a.ws = (unsigned char*)d_ws; a.kv_bytes = (unsigned)in_sizes[1] * 2u; a.pad = 0;
  (void)hipFuncSetAttribute((const void*)radix_attn, hipFuncAttributeMaxDynamicSharedMemorySize, LDS_BYTES);
  hipLaunchKernelGGL(radix_attn, dim3(256), dim3(NTHR), LDS_BYTES, stream, a);
}
```

```cpp
#include <hip/hip_runtime.h>
#include <hip/hip_bf16.h>
#include <stdint.h>
#include <type_traits>

constexpr int NSLOT = 16;
constexpr int INFL = 20;
constexpr int CLS01 = 4;
constexpr int SHCAP = 21;
constexpr int POLLAT = 9;
constexpr int NA = 8;
constexpr unsigned PTESTEP = 8u << 20;
constexpr int PTEH = 2;

constexpr int B = 16, HQ = 32, HKV = 16, D = 128, PAGE = 16, NB = 256, LK = NB * PAGE;
constexpr int NSW = 4, NTHR = (NSW + 2) * 64;
constexpr float C2 = 0.088388347648318440f * 1.4426950408889634f;
constexpr unsigned PAGE_BYTES = PAGE * HKV * D * 2;
constexpr unsigned TOK_BYTES = HKV * D * 2;

constexpr int ROW = 528;
constexpr int L_RING = 0;
constexpr int L_MRG = NSLOT * 8192;
constexpr int L_TAB = L_RING + (NSLOT - 2) * 8192;
constexpr int L_RECX = L_MRG + 32 * ROW;
constexpr int L_LIST = L_RECX + 4 * 2 * ROW;
constexpr int L_MISC = L_LIST + 1024;
constexpr int C_LANDED = 0, C_CSTEP = 4, C_RCNT = 8, C_RFREE = 12, C_REL = 16, C_P = 32, C_LIST = 36, C_TL = 44, C_READY = 40, C_PTR = 48, C_LENS = 80;
constexpr int LDS_BYTES = L_MISC + 160;
static_assert(NSLOT % 4 == 0 && (NSLOT & (NSLOT - 1)) == 0, "ring geometry");
static_assert(LDS_BYTES <= 160 * 1024, "LDS");
static_assert(NA % 4 == 0 && NA <= NSLOT - 2, "the table image lives in the last two ring slots until the list is built");
static_assert(INFL + CLS01 + 4 <= 28, "closing waits");

constexpr size_t W_EPOCH = 0, W_FLAG = 256, FLAG_STRIDE = 128;
constexpr size_t W_PART = 65536;
constexpr unsigned PS_BYTES = 32 * 512 + 256, PX_BYTES = 2 * 512 + 128;
constexpr unsigned W_PX = 256 * PS_BYTES;
constexpr unsigned PART_BYTES = W_PX + 256 * 16 * PX_BYTES;

typedef __bf16 bf16x8 __attribute__((ext_vector_type(8)));
typedef __bf16 bf16x2 __attribute__((ext_vector_type(2)));
typedef short s16x4 __attribute__((ext_vector_type(4)));
typedef float f32x4 __attribute__((ext_vector_type(4)));
typedef float f32x2 __attribute__((ext_vector_type(2)));
typedef unsigned u32x4 __attribute__((ext_vector_type(4)));
typedef unsigned u32x2 __attribute__((ext_vector_type(2)));
typedef int i32x4 __attribute__((ext_vector_type(4)));
#define LAS __attribute__((address_space(3)))
#define GAS __attribute__((address_space(1)))
#define DI __device__ __forceinline__
typedef GAS unsigned long long gu64;
#define RLX_AGENT __ATOMIC_RELAXED, __HIP_MEMORY_SCOPE_AGENT

DI int rfl(int x) { return __builtin_amdgcn_readfirstlane(x); }
DI int rdl(int x, int l) { return __builtin_amdgcn_readlane(x, l); }
template <int CTRL> DI int dpp_i(int oldv, int x) { return __builtin_amdgcn_update_dpp(oldv, x, CTRL, 0xf, 0xf, false); }
DI int row_scan_add(int x) {
  x += dpp_i<0x111>(0, x); x += dpp_i<0x112>(0, x); x += dpp_i<0x114>(0, x); x += dpp_i<0x118>(0, x); return x;
}
DI int row_scan_max(int x) {
  x = max(x, dpp_i<0x111>(0, x)); x = max(x, dpp_i<0x112>(0, x)); x = max(x, dpp_i<0x114>(0, x)); x = max(x, dpp_i<0x118>(0, x)); return x;
}
DI float xrow16_max(float x) {
  auto s = __builtin_amdgcn_permlane16_swap(__float_as_uint(x), __float_as_uint(x), false, false);
  x = fmaxf(__uint_as_float(s[0]), __uint_as_float(s[1]));
  auto t = __builtin_amdgcn_permlane32_swap(__float_as_uint(x), __float_as_uint(x), false, false);
  return fmaxf(__uint_as_float(t[0]), __uint_as_float(t[1]));
}
DI float xrow16_sum(float x) {
  auto s = __builtin_amdgcn_permlane16_swap(__float_as_uint(x), __float_as_uint(x), false, false);
  x = __uint_as_float(s[0]) + __uint_as_float(s[1]);
  auto t = __builtin_amdgcn_permlane32_swap(__float_as_uint(x), __float_as_uint(x), false, false);
  return __uint_as_float(t[0]) + __uint_as_float(t[1]);
}
DI unsigned cvtpk(float lo, float hi) { f32x2 v = {lo, hi}; bf16x2 b = __builtin_convertvector(v, bf16x2); return __builtin_bit_cast(unsigned, b); }
DI float ex2(float x) { return __builtin_amdgcn_exp2f(x); }
DI void wg_barrier() { asm volatile("s_waitcnt lgkmcnt(0)\n\ts_barrier" ::: "memory"); }
template <int N> DI void wait_vm() { asm volatile("s_waitcnt vmcnt(%0)" :: "i"(N) : "memory"); }

DI void set_m0(unsigned m0v, unsigned so) { asm volatile("s_mov_b32 m0, %0\n\ts_nop 3" :: "s"(m0v), "s"(so) : "memory"); }
template <int X> DI void wait_dma4(__amdgpu_buffer_rsrc_t rsrc, unsigned soff, unsigned v0, unsigned v1, unsigned v2, unsigned v3) {
  asm volatile("s_waitcnt vmcnt(%6)\n\t"
               "buffer_load_dwordx4 %0, %4, %5 offen nt lds\n\t"
               "buffer_load_dwordx4 %1, %4, %5 offen offset:1024 nt lds\n\t"
               "buffer_load_dwordx4 %2, %4, %5 offen offset:2048 nt lds\n\t"
               "buffer_load_dwordx4 %3, %4, %5 offen offset:3072 nt lds"
               :: "v"(v0), "v"(v1), "v"(v2), "v"(v3), "s"(rsrc), "s"(soff), "i"(X) : "memory");
}
DI void dma4r(__amdgpu_buffer_rsrc_t r0, __amdgpu_buffer_rsrc_t r1, __amdgpu_buffer_rsrc_t r2, __amdgpu_buffer_rsrc_t r3, unsigned soff, unsigned v0, unsigned v1, unsigned v2, unsigned v3) {
  asm volatile("s_nop 0\n\t"
               "buffer_load_dwordx4 %0, %4, %8 offen nt lds\n\t"
               "buffer_load_dwordx4 %1, %5, %8 offen offset:1024 nt lds\n\t"
               "buffer_load_dwordx4 %2, %6, %8 offen offset:2048 nt lds\n\t"
               "buffer_load_dwordx4 %3, %7, %8 offen offset:3072 nt lds"
               :: "v"(v0), "v"(v1), "v"(v2), "v"(v3), "s"(r0), "s"(r1), "s"(r2), "s"(r3), "s"(soff) : "memory");
}
#define WAIT_K(X, so) wait_dma4<X>(rk, so, vk[0], vk[1], vk[2], vk[3])
#define WAIT_V(X, so) wait_dma4<X>(rv, so, vv[0], vv[1], vv[2], vv[3])

template <int X>
DI void page_asm(__amdgpu_buffer_rsrc_t rk, __amdgpu_buffer_rsrc_t rv, const unsigned (&vk)[4], const unsigned (&vv)[4], unsigned vctl, unsigned vp,
                 int& i, int& pub, unsigned& so, unsigned& mk) {
  unsigned vt;
  asm volatile("s_waitcnt vmcnt(%[X])\n\t"
               "buffer_load_dwordx4 %[k0], %[rk], %[so] offen nt lds\n\t"
               "buffer_load_dwordx4 %[k1], %[rk], %[so] offen offset:1024 nt lds\n\t"
               "buffer_load_dwordx4 %[k2], %[rk], %[so] offen offset:2048 nt lds\n\t"
               "buffer_load_dwordx4 %[k3], %[rk], %[so] offen offset:3072 nt lds\n\t"
               "v_mov_b32 %[vt], %[pub]\n\t"
               "s_add_u32 m0, m0, 0x1000\n\t"
               "ds_write_b32 %[vctl], %[vt]\n\t"
               "s_add_u32 %[pub], %[pub], 1\n\t"
               "s_waitcnt vmcnt(%[X])\n\t"
               "buffer_load_dwordx4 %[v0], %[rv], %[so] offen nt lds\n\t"
               "buffer_load_dwordx4 %[v1], %[rv], %[so] offen offset:1024 nt lds\n\t"
               "buffer_load_dwordx4 %[v2], %[rv], %[so] offen offset:2048 nt lds\n\t"
               "buffer_load_dwordx4 %[v3], %[rv], %[so] offen offset:3072 nt lds\n\t"
               "v_mov_b32 %[vt], %[pub]\n\t"
               "s_add_u32 %[i], %[i], 1\n\t"
               "ds_write_b32 %[vctl], %[vt]\n\t"
               "v_readlane_b32 %[so], %[vp], %[i]\n\t"
               "s_add_u32 %[pub], %[pub], 1\n\t"
               "s_add_u32 %[mk], %[mk], 0x2000\n\t"
               "s_and_b32 %[mk], %[mk], %[msk]\n\t"
               "s_mov_b32 m0, %[mk]"
               : [i] "+s"(i), [pub] "+s"(pub), [so] "+s"(so), [mk] "+s"(mk), [vt] "=&v"(vt)
               : [X] "i"(X), [msk] "i"(NSLOT * 8192 - 1), [k0] "v"(vk[0]), [k1] "v"(vk[1]), [k2] "v"(vk[2]), [k3] "v"(vk[3]), [v0] "v"(vv[0]), [v1] "v"(vv[1]), [v2] "v"(vv[2]), [v3] "v"(vv[3]),
                 [rk] "s"(rk), [rv] "s"(rv), [vctl] "v"(vctl), [vp] "v"(vp) : "memory", "scc");
}

struct Args { const bf16x8* q; const void* k; const void* v; const int* bt; const int* lens; unsigned* out; unsigned char* ws; unsigned kv_bytes; unsigned pad; };

DI float mfac(float m, float M) { return (m == -INFINITY) ? 0.f : ex2((m - M) * C2); }

struct Soft { float m, l; f32x4 o[8]; };
DI void soft_reset(Soft& s) { s.m = -INFINITY; s.l = 0.f;
#pragma unroll
  for (int i = 0; i < 8; ++i) s.o[i] = f32x4{0.f, 0.f, 0.f, 0.f}; }

DI s16x4 soft_step(Soft& s, f32x4 x, bool mask, int tok0, int len) {
  if (mask) {
#pragma unroll
    for (int r = 0; r < 4; ++r) x[r] = (tok0 + r < len) ? x[r] : -INFINITY;
  }
  float pm = fmaxf(fmaxf(x[0], x[1]), fmaxf(x[2], x[3]));
  pm = xrow16_max(pm);
  const float mn = fmaxf(s.m, pm);
  const float ms = (mn == -INFINITY) ? 0.f : mn * C2;
  const float alpha = ex2(s.m * C2 - ms);
  float p[4];
#pragma unroll
  for (int r = 0; r < 4; ++r) p[r] = ex2(fmaf(x[r], C2, -ms));
  s.l = fmaf(s.l, alpha, (p[0] + p[1]) + (p[2] + p[3]));
  s.m = mn;
#pragma unroll
  for (int i = 0; i < 8; ++i) s.o[i] *= alpha;
  u32x2 pk = {cvtpk(p[0], p[1]), cvtpk(p[2], p[3])};
  return __builtin_bit_cast(s16x4, pk);
}


DI int lds_ld(LAS unsigned char* p) { return __hip_atomic_load((LAS int*)p, __ATOMIC_RELAXED, __HIP_MEMORY_SCOPE_WORKGROUP); }
DI void lds_st(LAS unsigned char* p, int v) { __hip_atomic_store((LAS int*)p, v, __ATOMIC_RELAXED, __HIP_MEMORY_SCOPE_WORKGROUP); }
DI int lds_wait_ge(LAS unsigned char* p, int need) {
  int v;
#pragma nounroll
  for (unsigned spins = 0;; ++spins) { v = rfl(lds_ld(p)); if (v >= need || spins > (1u << 20)) break; __builtin_amdgcn_s_sleep(1); }
  asm volatile("" ::: "memory");
  return v;
}
DI void lds_drain() { asm volatile("s_waitcnt lgkmcnt(0)" ::: "memory"); }

__global__ void __launch_bounds__(NTHR, 2) radix_attn(Args) {
  extern __shared__ __attribute__((aligned(16))) unsigned char lds_raw[];
  LAS unsigned char* lds = (LAS unsigned char*)lds_raw;
  LAS unsigned char* ctl = lds + L_MISC;
  const int tid = threadIdx.x, lane = tid & 63, wave = rfl(tid >> 6);
  const int w = blockIdx.x & 15, h = blockIdx.x >> 4;
  const int c = lane & 15, g = lane >> 4;
  int len_raw;

  struct Part { int len, Pe, S, E, spare, exc, lo, hi, xoff, S_w, spare_w, T_w, ns, ss, hp, nsh, np_w, own_w, vA, TL; unsigned xmask; bool all_masked; };
  auto partition = [&](int P) {
    Part p;
    p.all_masked = len_raw <= 0;
    p.len = p.all_masked ? LK : min(len_raw, LK);
    const int np = (p.len + PAGE - 1) >> 4;
    p.Pe = min(P, rdl(row_scan_max(np), 15));
    const int n = max(np - p.Pe, 0);
    const int W = p.Pe + rdl(row_scan_add(n), 15);
    const int T = (W >> 4) + (c < (W & 15) ? 1 : 0);
    const int cap = max(1, ((W >> 4) * SHCAP) >> 6);
    int lg = 0;
    if (p.Pe > cap) lg = 1;
    if (p.Pe > 2 * cap) lg = 2;
    if (p.Pe > 4 * cap) lg = 3;
    if (p.Pe > 8 * cap) lg = 4;
    const int q = p.Pe >> lg, rq = p.Pe & ((1 << lg) - 1);
    const int sh = c < (1 << lg) ? q + (c < rq ? 1 : 0) : 0;
    p.nsh = min(1 << lg, p.Pe);
    p.ns = w < (1 << lg) ? q + (w < rq ? 1 : 0) : 0;
    p.ss = w * q + min(w, rq);
    const int own = min(n, T - sh);
    p.exc = n - own; p.spare = T - sh - own;
    p.S = row_scan_add(p.spare) - p.spare;
    p.E = row_scan_add(p.exc) - p.exc;
    p.S_w = rdl(p.S, w); p.spare_w = rdl(p.spare, w); p.T_w = rdl(T, w);
    p.hp = p.ns + p.spare_w;
    p.lo = max(p.S_w, p.E); p.hi = min(p.S_w + p.spare_w, p.E + p.exc);
    p.xmask = (unsigned)(__ballot(p.hi > p.lo) & 0xFFFFull);
    p.xoff = p.Pe - p.E;
    p.np_w = rdl(np, w); p.own_w = rdl(own, w);
    p.vA = min(NA, p.own_w);
    p.TL = NA + p.hp + p.own_w - p.vA;
    return p;
  };

  if (__builtin_expect(wave == 0, 1)) {
    __builtin_amdgcn_s_setprio(3);
    typedef unsigned u32x16 __attribute__((ext_vector_type(16)));
    u32x16 ka = *(const __attribute__((address_space(4))) u32x16*)__builtin_amdgcn_kernarg_segment_ptr();
    asm volatile("" : "+s"(ka));
    const uintptr_t kptr = ((uintptr_t)ka[3] << 32) | ka[2], vptr = ((uintptr_t)ka[5] << 32) | ka[4], btptr = ((uintptr_t)ka[7] << 32) | ka[6], lensptr = ((uintptr_t)ka[9] << 32) | ka[8];
    const unsigned kv_bytes = ka[14];
    len_raw = *(const GAS int*)(lensptr + 4 * c);
    const i32x4 rw = *(const GAS i32x4*)(btptr + w * (NB * 4) + 16 * lane);
    asm volatile("s_barrier" ::: "memory");
    unsigned vk[4], vv[4];
#pragma unroll
    for (int i = 0; i < 4; ++i) { const int t = 4 * i + g;
      vk[i] = t * TOK_BYTES + ((c ^ t) << 4) - 1024 * i;
      vv[i] = t * TOK_BYTES + ((c ^ ((t & 7) << 1)) << 4) - 1024 * i; }
    asm volatile("" : "+v"(vk[0]), "+v"(vk[1]), "+v"(vk[2]), "+v"(vk[3]), "+v"(vv[0]), "+v"(vv[1]), "+v"(vv[2]), "+v"(vv[3]));
    const unsigned ring = (unsigned)(uintptr_t)(lds + L_RING);
    const __amdgpu_buffer_rsrc_t rk = __builtin_amdgcn_make_buffer_rsrc((void*)kptr, 0, kv_bytes, 0x00020000);
    const __amdgpu_buffer_rsrc_t rv = __builtin_amdgcn_make_buffer_rsrc((void*)vptr, 0, kv_bytes, 0x00020000);
    *(LAS i32x4*)(lds + L_MRG + 16 * lane) = rw;
    unsigned vp0, vp1 = 0, vp2 = 0, vp3 = 0;
    int nq0;
    {
      const int lw = rdl(len_raw, w);
      const int npw = ((lw <= 0 ? LK : min(lw, LK)) + PAGE - 1) >> 4;
      vp0 = (unsigned)*(LAS int*)(lds + L_MRG + 4 * max(npw - 1 - lane, 0)) * PAGE_BYTES + (unsigned)h * (D * 2);
      nq0 = ((lw <= 0 ? LK : min(lw, LK)) - (npw - 1) * PAGE + 3) >> 2;
    }
    unsigned so = (unsigned)rdl((int)vp0, 0), m0k = ring;
    {
      const __amdgpu_buffer_rsrc_t k1 = __builtin_amdgcn_make_buffer_rsrc((void*)kptr, 0, nq0 > 1 ? kv_bytes : 0u, 0x00020000), k2 = __builtin_amdgcn_make_buffer_rsrc((void*)kptr, 0, nq0 > 2 ? kv_bytes : 0u, 0x00020000),
                                   k3 = __builtin_amdgcn_make_buffer_rsrc((void*)kptr, 0, nq0 > 3 ? kv_bytes : 0u, 0x00020000);
      set_m0(m0k, so);
      dma4r(rk, k1, k2, k3, so, vk[0], vk[1], vk[2], vk[3]);
      const __amdgpu_buffer_rsrc_t v1 = __builtin_amdgcn_make_buffer_rsrc((void*)vptr, 0, nq0 > 1 ? kv_bytes : 0u, 0x00020000), v2 = __builtin_amdgcn_make_buffer_rsrc((void*)vptr, 0, nq0 > 2 ? kv_bytes : 0u, 0x00020000),
                                   v3 = __builtin_amdgcn_make_buffer_rsrc((void*)vptr, 0, nq0 > 3 ? kv_bytes : 0u, 0x00020000);
      set_m0(m0k + 4096, so);
      dma4r(rv, v1, v2, v3, so, vv[0], vv[1], vv[2], vv[3]);
      so = (unsigned)rdl((int)vp0, 1); m0k = ring + 8192;
      set_m0(m0k, so);
    }
    static_assert(L_RING == 0 && C_LANDED == 0 && C_REL == 16, "page_asm addresses the ring from LDS offset 0 and the control words by these offsets");
    const unsigned vctl = (unsigned)(uintptr_t)ctl;
    auto publish = [&](int nl) { lds_st(ctl + C_LANDED, nl); };
    const bool wide = ((((unsigned)kptr >> 8) + h) & 3) == 1;
    int i = 1, credit = NSLOT;
    int T_w = NA, TM = NA;
    bool listed = false;
    unsigned mk = m0k;
    for (;;) {
      i = rfl(i); so = (unsigned)rfl((int)so); mk = (unsigned)rfl((int)mk);
      const int iend = rfl(min(min(TM, credit), (i & ~63) + 64));
      if (wide) { int pub = 2 * i - (INFL + CLS01 + 3) / 4;
#pragma nounroll
        while (i < iend) page_asm<INFL + CLS01>(rk, rv, vk, vv, vctl, vp0, i, pub, so, mk);
      } else { int pub = 2 * i - (INFL + 3) / 4;
#pragma nounroll
        while (i < iend) page_asm<INFL>(rk, rv, vk, vv, vctl, vp0, i, pub, so, mk);
      }
      if (!listed) {
        listed = true;
        lds_wait_ge(ctl + C_LIST, 1);
        T_w = rfl(*(LAS int*)(ctl + C_TL));
        TM = T_w >= NA + 4 ? T_w - 4 : T_w;
        vp0 = *(LAS unsigned*)(lds + L_LIST + 4 * lane); vp1 = *(LAS unsigned*)(lds + L_LIST + 256 + 4 * lane);
        vp2 = *(LAS unsigned*)(lds + L_LIST + 512 + 4 * lane); vp3 = *(LAS unsigned*)(lds + L_LIST + 768 + 4 * lane);
        so = (unsigned)rdl((int)vp0, NA);
        asm volatile("s_nop 4" :: "s"(so));
        if (i < min(TM, credit)) continue;
      }
      if (i >= T_w) break;
      const bool endgroup = i >= TM;
      const u32x4 rel = *(volatile LAS u32x4*)(ctl + C_REL);
      credit = min(min(4 * rfl((int)rel[0]), 4 * rfl((int)rel[1]) + 1), min(4 * rfl((int)rel[2]) + 2, 4 * rfl((int)rel[3]) + 3)) + NSLOT;
      if ((i & 63) == 0) { vp0 = vp1; vp1 = vp2; vp2 = vp3; so = (unsigned)rdl((int)vp0, 0); asm volatile("s_nop 4" :: "s"(so)); }
      if (credit <= i) {
        LAS unsigned char* relp = ctl + C_REL + 4 * (i & 3);
        const int need = (i >> 2) - NSLOT / 4 + 1;
        if (rfl(lds_ld(relp)) < need) { wait_vm<12>(); publish(2 * i - 3);
          if (rfl(lds_ld(relp)) < need) { wait_vm<4>(); publish(2 * i - 1);
            if (rfl(lds_ld(relp)) < need) { wait_vm<0>(); publish(2 * i); lds_wait_ge(relp, need); } } }
        credit = i + 1;
      }
      if (endgroup) {
        if (credit < T_w) { wait_vm<0>(); publish(2 * i);
          for (int e = i; e < T_w; ++e) if (e >= NSLOT) lds_wait_ge(ctl + C_REL + 4 * (e & 3), (e >> 2) - NSLOT / 4 + 1); }
        unsigned se[4];
        int pub = 2 * i - (INFL + 3) / 4;
#pragma unroll
        for (int r = 0; r < 4; ++r) {
          if (r > 0) { if (((i + r) & 63) == 0) { vp0 = vp1; vp1 = vp2; } so = (unsigned)rdl((int)vp0, (i + r) & 63); }
          se[r] = so;
          set_m0(((i + r) & (NSLOT - 1)) * 8192, so);
          WAIT_K(INFL, so);
          publish(pub + r);
        }
#pragma unroll
        for (int r = 0; r < 4; ++r) {
          set_m0(((i + r) & (NSLOT - 1)) * 8192 + 4096, se[r]);
          WAIT_V(INFL, se[r]);
          publish(pub + 4 + r);
        }
        break;
      }
    }
    wait_vm<24>(); publish(2 * T_w - 6);
    wait_vm<20>(); publish(2 * T_w - 5);
    wait_vm<16>(); publish(2 * T_w - 4);
    wait_vm<12>(); publish(2 * T_w - 3);
    wait_vm<8>(); publish(2 * T_w - 2);
    wait_vm<4>(); publish(2 * T_w - 1);
    wait_vm<0>(); publish(2 * T_w);
  } else if (wave <= NSW) {
    const int cw = wave - 1;
    wg_barrier();
    const unsigned ka0 = c * 256 + ((g ^ c) << 4);
    const int tv = 4 * g + (c >> 2);
    const unsigned va0 = 4096 + tv * 256 + ((tv & 7) << 5) + (c & 3) * 8;
    unsigned touch = 0, touch2 = 0;
    if (h < PTEH && cw == 0) {
      typedef unsigned u32x16 __attribute__((ext_vector_type(16)));
      const u32x16 ka = *(const __attribute__((address_space(4))) u32x16*)__builtin_amdgcn_kernarg_segment_ptr();
      const uintptr_t kb = ((uintptr_t)ka[3] << 32) | ka[2], vb = ((uintptr_t)ka[5] << 32) | ka[4], wb = ((uintptr_t)ka[13] << 32) | ka[12];
      const unsigned off = (unsigned)(lane & 31) * PTESTEP;
      if (off < ka[14]) touch = *(const GAS unsigned*)((lane < 32 ? kb : vb) + off);
      if (lane < 2) touch2 = *(const GAS unsigned*)(wb + W_PART + lane * (PART_BYTES / 2));
    }
    lds_wait_ge(ctl + C_READY, 1);
    bf16x8 qf[2][4];
    {
      const u32x2 pq = *(LAS u32x2*)(ctl + C_PTR);
      const GAS bf16x8* qp = (const GAS bf16x8*)(((uintptr_t)(unsigned)rfl((int)pq[1]) << 32) | (unsigned)rfl((int)pq[0]));
#pragma unroll
      for (int cg = 0; cg < 2; ++cg)
#pragma unroll
        for (int s = 0; s < 4; ++s) { const int r = 16 * cg + c; qf[cg][s] = qp[(((r >> 1) * HQ + 2 * h + (r & 1)) * D + 32 * s + 8 * g) / 8]; }
    }
    len_raw = *(LAS int*)(ctl + C_LENS + 4 * c);
    const Part p = partition(rfl(*(LAS int*)(ctl + C_P)));
    const int len = p.len, S_w = p.S_w - p.ns - NA, hp = p.hp, ss = p.ss, T_w = p.TL, lo = p.lo, hi = p.hi, xoff = p.xoff, ns = p.ns, lpA = p.np_w - 1, lpB = p.np_w - p.own_w, vA = p.vA;
    const int minlen = LK - rdl(row_scan_max(LK - len), 15);
    const int len0 = __shfl(len, c >> 1), len1 = __shfl(len, 8 + (c >> 1));
    {
      const bool z0 = __shfl((int)p.all_masked, c >> 1) != 0, z1 = __shfl((int)p.all_masked, 8 + (c >> 1)) != 0;
#pragma unroll
      for (int s = 0; s < 4; ++s) {
        u32x4 t0 = __builtin_bit_cast(u32x4, qf[0][s]), t1 = __builtin_bit_cast(u32x4, qf[1][s]);
#pragma unroll
        for (int e = 0; e < 4; ++e) { t0[e] = z0 ? 0u : t0[e]; t1[e] = z1 ? 0u : t1[e]; }
        qf[0][s] = __builtin_bit_cast(bf16x8, t0); qf[1][s] = __builtin_bit_cast(bf16x8, t1);
      }
    }
    asm volatile("" :: "v"(touch), "v"(touch2));
    const int TM = T_w >= NA + 4 ? T_w - 4 : T_w + 4;
    int j = 0, seen = 0;
    Soft s0, s1;
    auto page = [&](auto SHc, const bf16x8 (&qa)[4], const bf16x8 (&qb)[4], int la, int lb, int t0, bool mask) {
      constexpr bool SH = decltype(SHc)::value;
      const int i = 4 * j + cw;
      LAS unsigned char* sl = lds + L_RING + (i & (NSLOT - 1)) * 8192;
      const int re = i - TM;
      const int needk = re >= 0 ? 2 * TM + re + 1 : 2 * i + 1, needv = re >= 0 ? 2 * TM + re + 5 : 2 * i + 2;
      if (seen < needk) seen = lds_wait_ge(ctl + C_LANDED, needk);
      bf16x8 kf[4];
#pragma unroll
      for (int s = 0; s < 4; ++s) kf[s] = *(LAS bf16x8*)(sl + (ka0 ^ (s << 6)));
      f32x4 xa = {0.f, 0.f, 0.f, 0.f}, xb = {0.f, 0.f, 0.f, 0.f};
#pragma unroll
      for (int s = 0; s < 4; ++s) { xa = __builtin_amdgcn_mfma_f32_16x16x32_bf16(kf[s], qa[s], xa, 0, 0, 0);
        if (SH) xb = __builtin_amdgcn_mfma_f32_16x16x32_bf16(kf[s], qb[s], xb, 0, 0, 0); }
      const s16x4 pa = soft_step(s0, xa, mask, t0 + 4 * g, la);
      s16x4 pb; if (SH) pb = soft_step(s1, xb, mask, t0 + 4 * g, lb);
      if (seen < needv) seen = lds_wait_ge(ctl + C_LANDED, needv);
      s16x4 vf[8];
#pragma unroll
      for (int blk = 0; blk < 8; ++blk) vf[blk] = __builtin_amdgcn_ds_read_tr16_b64_v4i16((LAS s16x4*)(sl + (va0 ^ (blk << 5))));
      ++j;
      asm volatile("s_waitcnt lgkmcnt(0)" : "+v"(vf[0]), "+v"(vf[1]), "+v"(vf[2]), "+v"(vf[3]), "+v"(vf[4]), "+v"(vf[5]), "+v"(vf[6]), "+v"(vf[7]) :: "memory");
      lds_st(ctl + C_REL + 4 * cw, j);
#pragma unroll
      for (int blk = 0; blk < 8; ++blk) { s0.o[blk] = __builtin_amdgcn_mfma_f32_16x16x16bf16_1k(vf[blk], pa, s0.o[blk], 0, 0, 0);
        if (SH) s1.o[blk] = __builtin_amdgcn_mfma_f32_16x16x16bf16_1k(vf[blk], pb, s1.o[blk], 0, 0, 0); }
    };
    unsigned m = p.xmask;
    for (int kx = 0;; ++kx) {
      const bool early = kx == 0, last = !early && m == 0;
      const int b = (early || last) ? w : __builtin_ctz(m);
      const int i0 = early ? 0 : last ? NA + hp : rdl(lo, b) - S_w;
      const int i1 = early ? NA : last ? T_w : rdl(hi, b) - S_w;
      const int lp0 = last ? lpB : rdl(lo, b) + rdl(xoff, b);
      bf16x8 qa[4];
      if (b < 8) {
#pragma unroll
        for (int s = 0; s < 4; ++s) qa[s] = qf[0][s];
      } else {
#pragma unroll
        for (int s = 0; s < 4; ++s) qa[s] = qf[1][s];
      }
      const int lb = rdl(len, b);
      soft_reset(s0);
      if (last) lds_wait_ge(ctl + C_RFREE, kx);
      for (int i = 4 * j + cw; i < i1; i += 4) {
        if (early && i >= vA) {
          lds_wait_ge(ctl + C_LANDED, 2 * i + 2); ++j; lds_st(ctl + C_REL + 4 * cw, j);
        } else {
          const int t0 = (early ? lpA - i : lp0 + i - i0) * PAGE;
          page(std::false_type{}, qa, qa, lb, lb, t0, t0 + PAGE > lb);
        }
      }
      if (kx > 0 && !last) lds_wait_ge(ctl + C_RFREE, kx);
      const float lt = xrow16_sum(s0.l);
      if ((c >> 1) == (b & 7)) {
        LAS unsigned char* r = lds + L_RECX + (cw * 2 + (c & 1)) * ROW;
#pragma unroll
        for (int blk = 0; blk < 8; ++blk) *(LAS f32x4*)(r + blk * 64 + g * 16) = s0.o[blk];
        if (g == 0) *(LAS f32x2*)(r + 512) = f32x2{s0.m, lt};
      }
      lds_drain();
      if (lane == 0) __hip_atomic_fetch_add((LAS int*)(ctl + C_RCNT), 1, __ATOMIC_RELAXED, __HIP_MEMORY_SCOPE_WORKGROUP);
      if (last) break;
      if (early) {
        if (ns > 0) {
          soft_reset(s0); soft_reset(s1);
          for (int i = cw; i < ns; i += 4) { const int t0 = (ss + i) * PAGE; page(std::true_type{}, qf[0], qf[1], len0, len1, t0, t0 + PAGE > minlen); }
          if (cw < ns) {
            s0.l = xrow16_sum(s0.l); s1.l = xrow16_sum(s1.l);
            if (cw > 0) lds_wait_ge(ctl + C_CSTEP, cw);
#pragma unroll
            for (int cg = 0; cg < 2; ++cg) { Soft& s = cg ? s1 : s0;
              LAS unsigned char* r = lds + L_MRG + (16 * cg + c) * ROW;
              if (cw > 0) {
                const f32x2 ml = *(LAS f32x2*)(r + 512);
                const float M = fmaxf(ml[0], s.m), f0 = mfac(ml[0], M), f1 = mfac(s.m, M);
#pragma unroll
                for (int blk = 0; blk < 8; ++blk) s.o[blk] = *(LAS f32x4*)(r + blk * 64 + g * 16) * f0 + s.o[blk] * f1;
                s.l = ml[1] * f0 + s.l * f1; s.m = M;
              }
#pragma unroll
              for (int blk = 0; blk < 8; ++blk) *(LAS f32x4*)(r + blk * 64 + g * 16) = s.o[blk];
              lds_drain();
              if (g == 0) *(LAS f32x2*)(r + 512) = f32x2{s.m, s.l};
            }
            lds_drain();
            lds_st(ctl + C_CSTEP, cw + 1);
          }
        }
      } else m &= m - 1;
    }
  } else {
    typedef unsigned u32x16 __attribute__((ext_vector_type(16)));
    u32x16 ka = *(const __attribute__((address_space(4))) u32x16*)__builtin_amdgcn_kernarg_segment_ptr();
    if (lane < 12) *(LAS int*)(ctl + 4 * lane) = 0;
    wg_barrier();
    asm volatile("" : "+s"(ka));
    const uintptr_t btptr = ((uintptr_t)ka[7] << 32) | ka[6], lensptr = ((uintptr_t)ka[9] << 32) | ka[8];
    GAS u32x2* outp = (GAS u32x2*)(((uintptr_t)ka[11] << 32) | ka[10]);
    unsigned char* ws = (unsigned char*)(((uintptr_t)ka[13] << 32) | ka[12]);
    len_raw = *(const GAS int*)(lensptr + 4 * c);
    i32x4 rk16[16];
#pragma unroll
    for (int k = 0; k < 16; ++k) rk16[k] = *(const GAS i32x4*)(btptr + k * (NB * 4) + 16 * lane);
    const unsigned long long epoch = __hip_atomic_load((gu64*)(ws + W_EPOCH), RLX_AGENT);
#pragma unroll
    for (int k = 0; k < 16; ++k) *(LAS i32x4*)(lds + L_TAB + (k * NB + 4 * lane) * 4) = rk16[k];
    i32x4 acc = rk16[1] ^ rk16[0];
#pragma unroll
    for (int k = 2; k < 16; ++k) acc |= rk16[k] ^ rk16[0];
    int P;
    {
      const unsigned long long b0 = __ballot(acc[0] != 0), b1 = __ballot(acc[1] != 0), b2 = __ballot(acc[2] != 0), b3 = __ballot(acc[3] != 0);
      const unsigned long long any = b0 | b1 | b2 | b3;
      const int l = any ? (int)__builtin_ctzll(any) : 64;
      const unsigned long long bit = any & (0ull - any);
      P = 4 * l + ((b0 & bit) ? 0 : (b1 & bit) ? 1 : (b2 & bit) ? 2 : (b3 & bit) ? 3 : 0);
    }
    if (lane == 0) { *(LAS int*)(ctl + C_P) = P; *(LAS u32x2*)(ctl + C_PTR) = u32x2{ka[0], ka[1]}; }
    if (lane < 16) *(LAS int*)(ctl + C_LENS + 4 * lane) = len_raw;
    lds_drain();
    lds_st(ctl + C_READY, 1);
    const Part p = partition(P);
    {
      const int S_w = p.S_w - p.ns - NA, lpA = p.np_w - 1, lpB = p.np_w - p.own_w;
#pragma unroll
      for (int r = 0; r < 4; ++r) {
        const int i = 64 * r + lane;
        int idx = (i < NA) ? w * NB + max(lpA - i, 0) : (i < NA + p.ns) ? p.ss + (i - NA) : w * NB + lpB + (i - NA - p.hp);
        for (unsigned mm = p.xmask; mm; mm &= mm - 1) {
          const int b = __builtin_ctz(mm);
          const int lo_b = rdl(p.lo, b) - S_w, hi_b = rdl(p.hi, b) - S_w, off_b = rdl(p.xoff, b) + b * NB + S_w;
          if (i >= lo_b && i < hi_b) idx = i + off_b;
        }
        *(LAS unsigned*)(lds + L_LIST + 4 * i) = (unsigned)*(LAS int*)(lds + L_TAB + 4 * (i < p.TL ? idx : 0)) * PAGE_BYTES + (unsigned)h * (D * 2);
      }
      if (lane == 0) *(LAS int*)(ctl + C_TL) = p.TL;
      lds_drain();
      lds_st(ctl + C_LIST, 1);
    }
    const __amdgpu_buffer_rsrc_t prsrc = __builtin_amdgcn_make_buffer_rsrc(ws + W_PART, 0, PART_BYTES, 0x00020000);
    const unsigned long long tag = epoch + 1ull;
    const int ns = p.ns;
    const unsigned blk = w * 16 + h;
    const int r = lane >> 5, ch = lane & 31;
    float M = -INFINITY, L = 0.f; f32x4 o = {0.f, 0.f, 0.f, 0.f};
    unsigned m = p.xmask;
#pragma nounroll
    for (int kx = 0;; ++kx) {
      const bool early = kx == 0, last = !early && m == 0;
      if (kx == 1 && ns > 0) {
        lds_wait_ge(ctl + C_CSTEP, min(ns, NSW));
#pragma unroll 4
        for (int k = 0; k < 16; ++k) {
          const u32x4 v = *(LAS u32x4*)(lds + L_MRG + (2 * k + r) * ROW + ch * 16);
          __builtin_amdgcn_raw_buffer_store_b128(v, prsrc, k * 1024 + lane * 16, blk * PS_BYTES, 16); }
        if (lane < 32) { const u32x2 v = *(LAS u32x2*)(lds + L_MRG + lane * ROW + 512);
          __builtin_amdgcn_raw_buffer_store_b64(v, prsrc, 32 * 512 + lane * 8, blk * PS_BYTES, 16); }
      }
      if (last) {
        asm volatile("s_waitcnt vmcnt(0)" ::: "memory");
        if (lane == 0) __hip_atomic_store((gu64*)(ws + W_FLAG + blk * FLAG_STRIDE), tag, RLX_AGENT);
        const int E_w = rdl(p.E, w), exc_w = rdl(p.exc, w);
        const bool sh_ov = c < p.nsh;
        const bool ex_ov = min(p.S + p.spare, E_w + exc_w) > max(p.S, E_w);
        unsigned cmask = (unsigned)(__ballot(sh_ov) & 0xFFFFull) | ((unsigned)(__ballot(ex_ov) & 0xFFFFull) << 16);
        if (cmask) lds_wait_ge(ctl + C_LANDED, (p.TL * POLLAT) >> 3);
        {
          const bool need = lane < 16 && (sh_ov || ex_ov);
          gu64* f = (gu64*)(ws + W_FLAG + (size_t)(c * 16 + h) * FLAG_STRIDE);
#pragma nounroll
          for (unsigned spins = 0; spins < (1u << 16); ++spins) {
            const bool ok = !need || __hip_atomic_load(f, RLX_AGENT) == tag;
            if (__all(ok)) break;
            __builtin_amdgcn_s_sleep(32);
          }
          asm volatile("" ::: "memory");
        }
        const unsigned vso = lane < 16 ? (unsigned)(c * 16 + h) * PS_BYTES + (unsigned)(2 * w) * 512 : W_PX + ((unsigned)(c * 16 + h) * 16 + w) * PX_BYTES;
        const unsigned vsm = lane < 16 ? (unsigned)(c * 16 + h) * PS_BYTES + 32 * 512 + (unsigned)(2 * w) * 8 : vso + 1024;
        constexpr int GB = 6;
        while (cmask) {
          u32x4 po[GB]; u32x2 pml[GB]; bool pv[GB];
#pragma unroll
          for (int k = 0; k < GB; ++k) {
            pv[k] = cmask != 0;
            const int id = pv[k] ? __builtin_ctz(cmask) : 0; cmask &= cmask - 1;
            const unsigned so = pv[k] ? (unsigned)rdl((int)vso, id) : PART_BYTES, sm = pv[k] ? (unsigned)rdl((int)vsm, id) : PART_BYTES;
            po[k] = __builtin_amdgcn_raw_buffer_load_b128(prsrc, lane * 16 + so, 0, 16);
            pml[k] = __builtin_amdgcn_raw_buffer_load_b64(prsrc, r * 8 + sm, 0, 16);
          }
#pragma unroll
          for (int k = 0; k < GB; ++k) {
            const float mk = pv[k] ? __uint_as_float(pml[k][0]) : -INFINITY, lk = __uint_as_float(pml[k][1]);
            const float Mn = fmaxf(M, mk), f0 = mfac(M, Mn), f1 = mfac(mk, Mn);
            o = o * f0 + __builtin_bit_cast(f32x4, po[k]) * f1; L = L * f0 + lk * f1; M = Mn;
          }
        }
        if (blk == 0) {
#pragma nounroll
          for (unsigned spins = 0; spins < (1u << 16); ++spins) {
            bool ok = true;
#pragma unroll
            for (int k = 0; k < 4; ++k) ok &= __hip_atomic_load((gu64*)(ws + W_FLAG + (size_t)(k * 64 + lane) * FLAG_STRIDE), RLX_AGENT) == tag;
            if (__all(ok)) break;
            __builtin_amdgcn_s_sleep(8);
          }
          if (lane == 0) __hip_atomic_store((gu64*)(ws + W_EPOCH), tag, RLX_AGENT);
        }
      }
      lds_wait_ge(ctl + C_RCNT, NSW * (kx + 1));
      f32x2 ml[4]; f32x4 ok[4];
#pragma unroll
      for (int k = 0; k < 4; ++k) { ml[k] = *(LAS f32x2*)(lds + L_RECX + (k * 2 + r) * ROW + 512); ok[k] = *(LAS f32x4*)(lds + L_RECX + (k * 2 + r) * ROW + ch * 16); }
      const bool mine = early || last;
      float Mx = mine ? M : -INFINITY;
#pragma unroll
      for (int k = 0; k < 4; ++k) Mx = fmaxf(Mx, ml[k][0]);
      const float fr = mine ? mfac(M, Mx) : 0.f;
      float Lx = L * fr; f32x4 ox = o * fr;
#pragma unroll
      for (int k = 0; k < 4; ++k) { const float f = mfac(ml[k][0], Mx); Lx = fmaf(f, ml[k][1], Lx); ox += ok[k] * f; }
      asm volatile("s_waitcnt lgkmcnt(0)" : "+v"(ox) :: "memory");
      if (last) {
        const float inv = __builtin_amdgcn_rcpf(Lx);
        outp[((w * HQ + 2 * h + r) * D + 4 * ch) >> 2] = u32x2{cvtpk(ox[0] * inv, ox[1] * inv), cvtpk(ox[2] * inv, ox[3] * inv)};
        break;
      }
      lds_st(ctl + C_RFREE, kx + 1);
      if (early) { M = Mx; L = Lx; o = ox; }
      else {
        const unsigned so = W_PX + (blk * 16 + __builtin_ctz(m)) * PX_BYTES;
        __builtin_amdgcn_raw_buffer_store_b128(__builtin_bit_cast(u32x4, ox), prsrc, lane * 16, so, 16);
        const f32x4 mlv = {__builtin_bit_cast(float, rdl(__builtin_bit_cast(int, Mx), 0)), __builtin_bit_cast(float, rdl(__builtin_bit_cast(int, Lx), 0)),
                           __builtin_bit_cast(float, rdl(__builtin_bit_cast(int, Mx), 32)), __builtin_bit_cast(float, rdl(__builtin_bit_cast(int, Lx), 32))};
        if (lane < 8) __builtin_amdgcn_raw_buffer_store_b128(__builtin_bit_cast(u32x4, mlv), prsrc, 1024 + lane * 16, so, 16);
        m &= m - 1;
      }
    }
  }
}

extern "C" void kernel_launch(void* const* d_in, const int* in_sizes, int n_in, void* d_out, int out_size, void* d_ws, size_t ws_size, hipStream_t stream) {
  Args a;
  a.q = (const bf16x8*)d_in[0]; a.k = d_in[1]; a.v = d_in[2]; a.bt = (const int*)d_in[3]; a.lens = (const int*)d_in[4];
  a.out = (unsigned*)d_out; a.ws = (unsigned char*)d_ws; a.kv_bytes = (unsigned)in_sizes[1] * 2u; a.pad = 0;
  (void)hipFuncSetAttribute((const void*)radix_attn, hipFuncAttributeMaxDynamicSharedMemorySize, LDS_BYTES);
  hipLaunchKernelGGL(radix_attn, dim3(256), dim3(NTHR), LDS_BYTES, stream, a);
}
```

```cpp
#include <hip/hip_runtime.h>
#include <hip/hip_bf16.h>
#include <stdint.h>
#include <type_traits>

constexpr int NSLOT = 16;
constexpr int INFL = 20;
constexpr int CLS01 = 4;
constexpr int SHCAP = 21;
constexpr int POLLAT = 9;
constexpr int NA = 12;
constexpr unsigned PTESTEP = 8u << 20;
constexpr int PTEH = 2;

constexpr int B = 16, HQ = 32, HKV = 16, D = 128, PAGE = 16, NB = 256, LK = NB * PAGE;
constexpr int NSW = 4, NTHR = (NSW + 2) * 64;
constexpr float C2 = 0.088388347648318440f * 1.4426950408889634f;
constexpr unsigned PAGE_BYTES = PAGE * HKV * D * 2;
constexpr unsigned TOK_BYTES = HKV * D * 2;

constexpr int ROW = 528;
constexpr int L_RING = 0;
constexpr int L_MRG = NSLOT * 8192;
constexpr int L_TAB = L_RING + (NSLOT - 2) * 8192;
constexpr int L_RECX = L_MRG + 32 * ROW;
constexpr int L_LIST = L_RECX + 4 * 2 * ROW;
constexpr int L_MISC = L_LIST + 1024;
constexpr int C_LANDED = 0, C_CSTEP = 4, C_RCNT = 8, C_RFREE = 12, C_REL = 16, C_P = 32, C_LIST = 36, C_TL = 44, C_READY = 40, C_PTR = 48, C_LENS = 80;
constexpr int LDS_BYTES = L_MISC + 160;
static_assert(NSLOT % 4 == 0 && (NSLOT & (NSLOT - 1)) == 0, "ring geometry");
static_assert(LDS_BYTES <= 160 * 1024, "LDS");
static_assert(NA % 4 == 0 && NA <= NSLOT - 2, "the table image lives in the last two ring slots until the list is built");
static_assert(INFL + CLS01 + 4 <= 28, "closing waits");

constexpr size_t W_EPOCH = 0, W_FLAG = 256, FLAG_STRIDE = 128;
constexpr size_t W_PART = 65536;
constexpr unsigned PS_BYTES = 32 * 512 + 256, PX_BYTES = 2 * 512 + 128;
constexpr unsigned W_PX = 256 * PS_BYTES;
constexpr unsigned PART_BYTES = W_PX + 256 * 16 * PX_BYTES;

typedef __bf16 bf16x8 __attribute__((ext_vector_type(8)));
typedef __bf16 bf16x2 __attribute__((ext_vector_type(2)));
typedef short s16x4 __attribute__((ext_vector_type(4)));
typedef float f32x4 __attribute__((ext_vector_type(4)));
typedef float f32x2 __attribute__((ext_vector_type(2)));
typedef unsigned u32x4 __attribute__((ext_vector_type(4)));
typedef unsigned u32x2 __attribute__((ext_vector_type(2)));
typedef int i32x4 __attribute__((ext_vector_type(4)));
#define LAS __attribute__((address_space(3)))
#define GAS __attribute__((address_space(1)))
#define DI __device__ __forceinline__
typedef GAS unsigned long long gu64;
#define RLX_AGENT __ATOMIC_RELAXED, __HIP_MEMORY_SCOPE_AGENT

DI int rfl(int x) { return __builtin_amdgcn_readfirstlane(x); }
DI int rdl(int x, int l) { return __builtin_amdgcn_readlane(x, l); }
template <int CTRL> DI int dpp_i(int oldv, int x) { return __builtin_amdgcn_update_dpp(oldv, x, CTRL, 0xf, 0xf, false); }
DI int row_scan_add(int x) {
  x += dpp_i<0x111>(0, x); x += dpp_i<0x112>(0, x); x += dpp_i<0x114>(0, x); x += dpp_i<0x118>(0, x); return x;
}
DI int row_scan_max(int x) {
  x = max(x, dpp_i<0x111>(0, x)); x = max(x, dpp_i<0x112>(0, x)); x = max(x, dpp_i<0x114>(0, x)); x = max(x, dpp_i<0x118>(0, x)); return x;
}
DI float xrow16_max(float x) {
  auto s = __builtin_amdgcn_permlane16_swap(__float_as_uint(x), __float_as_uint(x), false, false);
  x = fmaxf(__uint_as_float(s[0]), __uint_as_float(s[1]));
  auto t = __builtin_amdgcn_permlane32_swap(__float_as_uint(x), __float_as_uint(x), false, false);
  return fmaxf(__uint_as_float(t[0]), __uint_as_float(t[1]));
}
DI float xrow16_sum(float x) {
  auto s = __builtin_amdgcn_permlane16_swap(__float_as_uint(x), __float_as_uint(x), false, false);
  x = __uint_as_float(s[0]) + __uint_as_float(s[1]);
  auto t = __builtin_amdgcn_permlane32_swap(__float_as_uint(x), __float_as_uint(x), false, false);
  return __uint_as_float(t[0]) + __uint_as_float(t[1]);
}
DI unsigned cvtpk(float lo, float hi) { f32x2 v = {lo, hi}; bf16x2 b = __builtin_convertvector(v, bf16x2); return __builtin_bit_cast(unsigned, b); }
DI float ex2(float x) { return __builtin_amdgcn_exp2f(x); }
DI void wg_barrier() { asm volatile("s_waitcnt lgkmcnt(0)\n\ts_barrier" ::: "memory"); }
template <int N> DI void wait_vm() { asm volatile("s_waitcnt vmcnt(%0)" :: "i"(N) : "memory"); }

DI void set_m0(unsigned m0v, unsigned so) { asm volatile("s_mov_b32 m0, %0\n\ts_nop 3" :: "s"(m0v), "s"(so) : "memory"); }
template <int X> DI void wait_dma4(__amdgpu_buffer_rsrc_t rsrc, unsigned soff, unsigned v0, unsigned v1, unsigned v2, unsigned v3) {
  asm volatile("s_waitcnt vmcnt(%6)\n\t"
               "buffer_load_dwordx4 %0, %4, %5 offen nt lds\n\t"
               "buffer_load_dwordx4 %1, %4, %5 offen offset:1024 nt lds\n\t"
               "buffer_load_dwordx4 %2, %4, %5 offen offset:2048 nt lds\n\t"
               "buffer_load_dwordx4 %3, %4, %5 offen offset:3072 nt lds"
               :: "v"(v0), "v"(v1), "v"(v2), "v"(v3), "s"(rsrc), "s"(soff), "i"(X) : "memory");
}
DI void dma4r(__amdgpu_buffer_rsrc_t r0, __amdgpu_buffer_rsrc_t r1, __amdgpu_buffer_rsrc_t r2, __amdgpu_buffer_rsrc_t r3, unsigned soff, unsigned v0, unsigned v1, unsigned v2, unsigned v3) {
  asm volatile("s_nop 0\n\t"
               "buffer_load_dwordx4 %0, %4, %8 offen nt lds\n\t"
               "buffer_load_dwordx4 %1, %5, %8 offen offset:1024 nt lds\n\t"
               "buffer_load_dwordx4 %2, %6, %8 offen offset:2048 nt lds\n\t"
               "buffer_load_dwordx4 %3, %7, %8 offen offset:3072 nt lds"
               :: "v"(v0), "v"(v1), "v"(v2), "v"(v3), "s"(r0), "s"(r1), "s"(r2), "s"(r3), "s"(soff) : "memory");
}
#define WAIT_K(X, so) wait_dma4<X>(rk, so, vk[0], vk[1], vk[2], vk[3])
#define WAIT_V(X, so) wait_dma4<X>(rv, so, vv[0], vv[1], vv[2], vv[3])

template <int X>
DI void page_asm(__amdgpu_buffer_rsrc_t rk, __amdgpu_buffer_rsrc_t rv, const unsigned (&vk)[4], const unsigned (&vv)[4], unsigned vctl, unsigned vp,
                 int& i, int& pub, unsigned& so, unsigned& mk) {
  unsigned vt;
  asm volatile("s_waitcnt vmcnt(%[X])\n\t"
               "buffer_load_dwordx4 %[k0], %[rk], %[so] offen nt lds\n\t"
               "buffer_load_dwordx4 %[k1], %[rk], %[so] offen offset:1024 nt lds\n\t"
               "buffer_load_dwordx4 %[k2], %[rk], %[so] offen offset:2048 nt lds\n\t"
               "buffer_load_dwordx4 %[k3], %[rk], %[so] offen offset:3072 nt lds\n\t"
               "v_mov_b32 %[vt], %[pub]\n\t"
               "s_add_u32 m0, m0, 0x1000\n\t"
               "ds_write_b32 %[vctl], %[vt]\n\t"
               "s_add_u32 %[pub], %[pub], 1\n\t"
               "s_waitcnt vmcnt(%[X])\n\t"
               "buffer_load_dwordx4 %[v0], %[rv], %[so] offen nt lds\n\t"
               "buffer_load_dwordx4 %[v1], %[rv], %[so] offen offset:1024 nt lds\n\t"
               "buffer_load_dwordx4 %[v2], %[rv], %[so] offen offset:2048 nt lds\n\t"
               "buffer_load_dwordx4 %[v3], %[rv], %[so] offen offset:3072 nt lds\n\t"
               "v_mov_b32 %[vt], %[pub]\n\t"
               "s_add_u32 %[i], %[i], 1\n\t"
               "ds_write_b32 %[vctl], %[vt]\n\t"
               "v_readlane_b32 %[so], %[vp], %[i]\n\t"
               "s_add_u32 %[pub], %[pub], 1\n\t"
               "s_add_u32 %[mk], %[mk], 0x2000\n\t"
               "s_and_b32 %[mk], %[mk], %[msk]\n\t"
               "s_mov_b32 m0, %[mk]"
               : [i] "+s"(i), [pub] "+s"(pub), [so] "+s"(so), [mk] "+s"(mk), [vt] "=&v"(vt)
               : [X] "i"(X), [msk] "i"(NSLOT * 8192 - 1), [k0] "v"(vk[0]), [k1] "v"(vk[1]), [k2] "v"(vk[2]), [k3] "v"(vk[3]), [v0] "v"(vv[0]), [v1] "v"(vv[1]), [v2] "v"(vv[2]), [v3] "v"(vv[3]),
                 [rk] "s"(rk), [rv] "s"(rv), [vctl] "v"(vctl), [vp] "v"(vp) : "memory", "scc");
}

struct Args { const bf16x8* q; const void* k; const void* v; const int* bt; const int* lens; unsigned* out; unsigned char* ws; unsigned kv_bytes; unsigned pad; };

DI float mfac(float m, float M) { return (m == -INFINITY) ? 0.f : ex2((m - M) * C2); }

struct Soft { float m, l; f32x4 o[8]; };
DI void soft_reset(Soft& s) { s.m = -INFINITY; s.l = 0.f;
#pragma unroll
  for (int i = 0; i < 8; ++i) s.o[i] = f32x4{0.f, 0.f, 0.f, 0.f}; }

DI s16x4 soft_step(Soft& s, f32x4 x, bool mask, int tok0, int len) {
  if (mask) {
#pragma unroll
    for (int r = 0; r < 4; ++r) x[r] = (tok0 + r < len) ? x[r] : -INFINITY;
  }
  float pm = fmaxf(fmaxf(x[0], x[1]), fmaxf(x[2], x[3]));
  pm = xrow16_max(pm);
  const float mn = fmaxf(s.m, pm);
  const float ms = (mn == -INFINITY) ? 0.f : mn * C2;
  const float alpha = ex2(s.m * C2 - ms);
  float p[4];
#pragma unroll
  for (int r = 0; r < 4; ++r) p[r] = ex2(fmaf(x[r], C2, -ms));
  s.l = fmaf(s.l, alpha, (p[0] + p[1]) + (p[2] + p[3]));
  s.m = mn;
#pragma unroll
  for (int i = 0; i < 8; ++i) s.o[i] *= alpha;
  u32x2 pk = {cvtpk(p[0], p[1]), cvtpk(p[2], p[3])};
  return __builtin_bit_cast(s16x4, pk);
}


DI int lds_ld(LAS unsigned char* p) { return __hip_atomic_load((LAS int*)p, __ATOMIC_RELAXED, __HIP_MEMORY_SCOPE_WORKGROUP); }
DI void lds_st(LAS unsigned char* p, int v) { __hip_atomic_store((LAS int*)p, v, __ATOMIC_RELAXED, __HIP_MEMORY_SCOPE_WORKGROUP); }
DI int lds_wait_ge(LAS unsigned char* p, int need) {
  int v;
#pragma nounroll
  for (unsigned spins = 0;; ++spins) { v = rfl(lds_ld(p)); if (v >= need || spins > (1u << 20)) break; __builtin_amdgcn_s_sleep(1); }
  asm volatile("" ::: "memory");
  return v;
}
DI void lds_drain() { asm volatile("s_waitcnt lgkmcnt(0)" ::: "memory"); }

__global__ void __launch_bounds__(NTHR, 2) radix_attn(Args) {
  extern __shared__ __attribute__((aligned(16))) unsigned char lds_raw[];
  LAS unsigned char* lds = (LAS unsigned char*)lds_raw;
  LAS unsigned char* ctl = lds + L_MISC;
  const int tid = threadIdx.x, lane = tid & 63, wave = rfl(tid >> 6);
  const int w = blockIdx.x & 15, h = blockIdx.x >> 4;
  const int c = lane & 15, g = lane >> 4;
  int len_raw;

  struct Part { int len, Pe, S, E, spare, exc, lo, hi, xoff, S_w, spare_w, T_w, ns, ss, hp, nsh, np_w, own_w, vA, TL; unsigned xmask; bool all_masked; };
  auto partition = [&](int P) {
    Part p;
    p.all_masked = len_raw <= 0;
    p.len = p.all_masked ? LK : min(len_raw, LK);
    const int np = (p.len + PAGE - 1) >> 4;
    p.Pe = min(P, rdl(row_scan_max(np), 15));
    const int n = max(np - p.Pe, 0);
    const int W = p.Pe + rdl(row_scan_add(n), 15);
    const int T = (W >> 4) + (c < (W & 15) ? 1 : 0);
    const int cap = max(1, ((W >> 4) * SHCAP) >> 6);
    int lg = 0;
    if (p.Pe > cap) lg = 1;
    if (p.Pe > 2 * cap) lg = 2;
    if (p.Pe > 4 * cap) lg = 3;
    if (p.Pe > 8 * cap) lg = 4;
    const int q = p.Pe >> lg, rq = p.Pe & ((1 << lg) - 1);
    const int sh = c < (1 << lg) ? q + (c < rq ? 1 : 0) : 0;
    p.nsh = min(1 << lg, p.Pe);
    p.ns = w < (1 << lg) ? q + (w < rq ? 1 : 0) : 0;
    p.ss = w * q + min(w, rq);
    const int own = min(n, T - sh);
    p.exc = n - own; p.spare = T - sh - own;
    p.S = row_scan_add(p.spare) - p.spare;
    p.E = row_scan_add(p.exc) - p.exc;
    p.S_w = rdl(p.S, w); p.spare_w = rdl(p.spare, w); p.T_w = rdl(T, w);
    p.hp = p.ns + p.spare_w;
    p.lo = max(p.S_w, p.E); p.hi = min(p.S_w + p.spare_w, p.E + p.exc);
    p.xmask = (unsigned)(__ballot(p.hi > p.lo) & 0xFFFFull);
    p.xoff = p.Pe - p.E;
    p.np_w = rdl(np, w); p.own_w = rdl(own, w);
    p.vA = min(NA, p.own_w);
    p.TL = NA + p.hp + p.own_w - p.vA;
    return p;
  };

  if (__builtin_expect(wave == 0, 1)) {
    __builtin_amdgcn_s_setprio(3);
    typedef unsigned u32x16 __attribute__((ext_vector_type(16)));
    u32x16 ka = *(const __attribute__((address_space(4))) u32x16*)__builtin_amdgcn_kernarg_segment_ptr();
    asm volatile("" : "+s"(ka));
    const uintptr_t kptr = ((uintptr_t)ka[3] << 32) | ka[2], vptr = ((uintptr_t)ka[5] << 32) | ka[4], btptr = ((uintptr_t)ka[7] << 32) | ka[6], lensptr = ((uintptr_t)ka[9] << 32) | ka[8];
    const unsigned kv_bytes = ka[14];
    len_raw = *(const GAS int*)(lensptr + 4 * c);
    const i32x4 rw = *(const GAS i32x4*)(btptr + w * (NB * 4) + 16 * lane);
    asm volatile("s_barrier" ::: "memory");
    unsigned vk[4], vv[4];
#pragma unroll
    for (int i = 0; i < 4; ++i) { const int t = 4 * i + g;
      vk[i] = t * TOK_BYTES + ((c ^ t) << 4) - 1024 * i;
      vv[i] = t * TOK_BYTES + ((c ^ ((t & 7) << 1)) << 4) - 1024 * i; }
    asm volatile("" : "+v"(vk[0]), "+v"(vk[1]), "+v"(vk[2]), "+v"(vk[3]), "+v"(vv[0]), "+v"(vv[1]), "+v"(vv[2]), "+v"(vv[3]));
    const unsigned ring = (unsigned)(uintptr_t)(lds + L_RING);
    const __amdgpu_buffer_rsrc_t rk = __builtin_amdgcn_make_buffer_rsrc((void*)kptr, 0, kv_bytes, 0x00020000);
    const __amdgpu_buffer_rsrc_t rv = __builtin_amdgcn_make_buffer_rsrc((void*)vptr, 0, kv_bytes, 0x00020000);
    *(LAS i32x4*)(lds + L_MRG + 16 * lane) = rw;
    unsigned vp0, vp1 = 0, vp2 = 0, vp3 = 0;
    int nq0;
    {
      const int lw = rdl(len_raw, w);
      const int npw = ((lw <= 0 ? LK : min(lw, LK)) + PAGE - 1) >> 4;
      vp0 = (unsigned)*(LAS int*)(lds + L_MRG + 4 * max(npw - 1 - lane, 0)) * PAGE_BYTES + (unsigned)h * (D * 2);
      nq0 = ((lw <= 0 ? LK : min(lw, LK)) - (npw - 1) * PAGE + 3) >> 2;
    }
    unsigned so = (unsigned)rdl((int)vp0, 0), m0k = ring;
    {
      const __amdgpu_buffer_rsrc_t k1 = __builtin_amdgcn_make_buffer_rsrc((void*)kptr, 0, nq0 > 1 ? kv_bytes : 0u, 0x00020000), k2 = __builtin_amdgcn_make_buffer_rsrc((void*)kptr, 0, nq0 > 2 ? kv_bytes : 0u, 0x00020000),
                                   k3 = __builtin_amdgcn_make_buffer_rsrc((void*)kptr, 0, nq0 > 3 ? kv_bytes : 0u, 0x00020000);
      set_m0(m0k, so);
      dma4r(rk, k1, k2, k3, so, vk[0], vk[1], vk[2], vk[3]);
      const __amdgpu_buffer_rsrc_t v1 = __builtin_amdgcn_make_buffer_rsrc((void*)vptr, 0, nq0 > 1 ? kv_bytes : 0u, 0x00020000), v2 = __builtin_amdgcn_make_buffer_rsrc((void*)vptr, 0, nq0 > 2 ? kv_bytes : 0u, 0x00020000),
                                   v3 = __builtin_amdgcn_make_buffer_rsrc((void*)vptr, 0, nq0 > 3 ? kv_bytes : 0u, 0x00020000);
      set_m0(m0k + 4096, so);
      dma4r(rv, v1, v2, v3, so, vv[0], vv[1], vv[2], vv[3]);
      so = (unsigned)rdl((int)vp0, 1); m0k = ring + 8192;
      set_m0(m0k, so);
    }
    static_assert(L_RING == 0 && C_LANDED == 0 && C_REL == 16, "page_asm addresses the ring from LDS offset 0 and the control words by these offsets");
    const unsigned vctl = (unsigned)(uintptr_t)ctl;
    auto publish = [&](int nl) { lds_st(ctl + C_LANDED, nl); };
    const bool wide = ((((unsigned)kptr >> 8) + h) & 3) == 1;
    int i = 1, credit = NSLOT;
    int T_w = NA, TM = NA;
    bool listed = false;
    unsigned mk = m0k;
    for (;;) {
      i = rfl(i); so = (unsigned)rfl((int)so); mk = (unsigned)rfl((int)mk);
      const int iend = rfl(min(min(TM, credit), (i & ~63) + 64));
      if (wide) { int pub = 2 * i - (INFL + CLS01 + 3) / 4;
#pragma nounroll
        while (i < iend) page_asm<INFL + CLS01>(rk, rv, vk, vv, vctl, vp0, i, pub, so, mk);
      } else { int pub = 2 * i - (INFL + 3) / 4;
#pragma nounroll
        while (i < iend) page_asm<INFL>(rk, rv, vk, vv, vctl, vp0, i, pub, so, mk);
      }
      if (!listed) {
        listed = true;
        lds_wait_ge(ctl + C_LIST, NSW);
        T_w = rfl(*(LAS int*)(ctl + C_TL));
        TM = T_w >= NA + 4 ? T_w - 4 : T_w;
        vp0 = *(LAS unsigned*)(lds + L_LIST + 4 * lane); vp1 = *(LAS unsigned*)(lds + L_LIST + 256 + 4 * lane);
        vp2 = *(LAS unsigned*)(lds + L_LIST + 512 + 4 * lane); vp3 = *(LAS unsigned*)(lds + L_LIST + 768 + 4 * lane);
        so = (unsigned)rdl((int)vp0, NA);
        asm volatile("s_nop 4" :: "s"(so));
        if (i < min(TM, credit)) continue;
      }
      if (i >= T_w) break;
      const bool endgroup = i >= TM;
      const u32x4 rel = *(volatile LAS u32x4*)(ctl + C_REL);
      credit = min(min(4 * rfl((int)rel[0]), 4 * rfl((int)rel[1]) + 1), min(4 * rfl((int)rel[2]) + 2, 4 * rfl((int)rel[3]) + 3)) + NSLOT;
      if ((i & 63) == 0) { vp0 = vp1; vp1 = vp2; vp2 = vp3; so = (unsigned)rdl((int)vp0, 0); asm volatile("s_nop 4" :: "s"(so)); }
      if (credit <= i) {
        LAS unsigned char* relp = ctl + C_REL + 4 * (i & 3);
        const int need = (i >> 2) - NSLOT / 4 + 1;
        if (rfl(lds_ld(relp)) < need) { wait_vm<12>(); publish(2 * i - 3);
          if (rfl(lds_ld(relp)) < need) { wait_vm<4>(); publish(2 * i - 1);
            if (rfl(lds_ld(relp)) < need) { wait_vm<0>(); publish(2 * i); lds_wait_ge(relp, need); } } }
        credit = i + 1;
      }
      if (endgroup) {
        if (credit < T_w) { wait_vm<0>(); publish(2 * i);
          for (int e = i; e < T_w; ++e) if (e >= NSLOT) lds_wait_ge(ctl + C_REL + 4 * (e & 3), (e >> 2) - NSLOT / 4 + 1); }
        unsigned se[4];
        int pub = 2 * i - (INFL + 3) / 4;
#pragma unroll
        for (int r = 0; r < 4; ++r) {
          if (r > 0) { if (((i + r) & 63) == 0) { vp0 = vp1; vp1 = vp2; } so = (unsigned)rdl((int)vp0, (i + r) & 63); }
          se[r] = so;
          set_m0(((i + r) & (NSLOT - 1)) * 8192, so);
          WAIT_K(INFL, so);
          publish(pub + r);
        }
#pragma unroll
        for (int r = 0; r < 4; ++r) {
          set_m0(((i + r) & (NSLOT - 1)) * 8192 + 4096, se[r]);
          WAIT_V(INFL, se[r]);
          publish(pub + 4 + r);
        }
        break;
      }
    }
    wait_vm<24>(); publish(2 * T_w - 6);
    wait_vm<20>(); publish(2 * T_w - 5);
    wait_vm<16>(); publish(2 * T_w - 4);
    wait_vm<12>(); publish(2 * T_w - 3);
    wait_vm<8>(); publish(2 * T_w - 2);
    wait_vm<4>(); publish(2 * T_w - 1);
    wait_vm<0>(); publish(2 * T_w);
  } else if (wave <= NSW) {
    const int cw = wave - 1;
    wg_barrier();
    const unsigned ka0 = c * 256 + ((g ^ c) << 4);
    const int tv = 4 * g + (c >> 2);
    const unsigned va0 = 4096 + tv * 256 + ((tv & 7) << 5) + (c & 3) * 8;
    unsigned touch = 0, touch2 = 0;
    if (h < PTEH && cw == 0) {
      typedef unsigned u32x16 __attribute__((ext_vector_type(16)));
      const u32x16 ka = *(const __attribute__((address_space(4))) u32x16*)__builtin_amdgcn_kernarg_segment_ptr();
      const uintptr_t kb = ((uintptr_t)ka[3] << 32) | ka[2], vb = ((uintptr_t)ka[5] << 32) | ka[4], wb = ((uintptr_t)ka[13] << 32) | ka[12];
      const unsigned off = (unsigned)(lane & 31) * PTESTEP;
      if (off < ka[14]) touch = *(const GAS unsigned*)((lane < 32 ? kb : vb) + off);
      if (lane < 2) touch2 = *(const GAS unsigned*)(wb + W_PART + lane * (PART_BYTES / 2));
    }
    lds_wait_ge(ctl + C_READY, 1);
    bf16x8 qf[2][4];
    {
      const u32x2 pq = *(LAS u32x2*)(ctl + C_PTR);
      const GAS bf16x8* qp = (const GAS bf16x8*)(((uintptr_t)(unsigned)rfl((int)pq[1]) << 32) | (unsigned)rfl((int)pq[0]));
#pragma unroll
      for (int cg = 0; cg < 2; ++cg)
#pragma unroll
        for (int s = 0; s < 4; ++s) { const int r = 16 * cg + c; qf[cg][s] = qp[(((r >> 1) * HQ + 2 * h + (r & 1)) * D + 32 * s + 8 * g) / 8]; }
    }
    len_raw = *(LAS int*)(ctl + C_LENS + 4 * c);
    const Part p = partition(rfl(*(LAS int*)(ctl + C_P)));
    const int len = p.len, S_w = p.S_w - p.ns - NA, hp = p.hp, ss = p.ss, T_w = p.TL, lo = p.lo, hi = p.hi, xoff = p.xoff, ns = p.ns, lpA = p.np_w - 1, lpB = p.np_w - p.own_w, vA = p.vA;
    {
      const int i = 64 * cw + lane;
      int idx = (i < NA) ? w * NB + max(lpA - i, 0) : (i < NA + ns) ? ss + (i - NA) : w * NB + lpB + (i - NA - hp);
      for (unsigned mm = p.xmask; mm; mm &= mm - 1) {
        const int b = __builtin_ctz(mm);
        const int lo_b = rdl(lo, b) - S_w, hi_b = rdl(hi, b) - S_w, off_b = rdl(xoff, b) + b * NB + S_w;
        if (i >= lo_b && i < hi_b) idx = i + off_b;
      }
      *(LAS unsigned*)(lds + L_LIST + 4 * i) = (unsigned)*(LAS int*)(lds + L_TAB + 4 * (i < T_w ? idx : 0)) * PAGE_BYTES + (unsigned)h * (D * 2);
      if (tid == 64) *(LAS int*)(ctl + C_TL) = T_w;
      lds_drain();
      if (lane == 0) __hip_atomic_fetch_add((LAS int*)(ctl + C_LIST), 1, __ATOMIC_RELAXED, __HIP_MEMORY_SCOPE_WORKGROUP);
    }
    const int minlen = LK - rdl(row_scan_max(LK - len), 15);
    const int len0 = __shfl(len, c >> 1), len1 = __shfl(len, 8 + (c >> 1));
    {
      const bool z0 = __shfl((int)p.all_masked, c >> 1) != 0, z1 = __shfl((int)p.all_masked, 8 + (c >> 1)) != 0;
#pragma unroll
      for (int s = 0; s < 4; ++s) {
        u32x4 t0 = __builtin_bit_cast(u32x4, qf[0][s]), t1 = __builtin_bit_cast(u32x4, qf[1][s]);
#pragma unroll
        for (int e = 0; e < 4; ++e) { t0[e] = z0 ? 0u : t0[e]; t1[e] = z1 ? 0u : t1[e]; }
        qf[0][s] = __builtin_bit_cast(bf16x8, t0); qf[1][s] = __builtin_bit_cast(bf16x8, t1);
      }
    }
    asm volatile("" :: "v"(touch), "v"(touch2));
    const int TM = T_w >= NA + 4 ? T_w - 4 : T_w + 4;
    int j = 0, seen = 0;
    Soft s0, s1;
    auto page = [&](auto SHc, const bf16x8 (&qa)[4], const bf16x8 (&qb)[4], int la, int lb, int t0, bool mask) {
      constexpr bool SH = decltype(SHc)::value;
      const int i = 4 * j + cw;
      LAS unsigned char* sl = lds + L_RING + (i & (NSLOT - 1)) * 8192;
      const int re = i - TM;
      const int needk = re >= 0 ? 2 * TM + re + 1 : 2 * i + 1, needv = re >= 0 ? 2 * TM + re + 5 : 2 * i + 2;
      if (seen < needk) seen = lds_wait_ge(ctl + C_LANDED, needk);
      bf16x8 kf[4];
#pragma unroll
      for (int s = 0; s < 4; ++s) kf[s] = *(LAS bf16x8*)(sl + (ka0 ^ (s << 6)));
      f32x4 xa = {0.f, 0.f, 0.f, 0.f}, xb = {0.f, 0.f, 0.f, 0.f};
#pragma unroll
      for (int s = 0; s < 4; ++s) { xa = __builtin_amdgcn_mfma_f32_16x16x32_bf16(kf[s], qa[s], xa, 0, 0, 0);
        if (SH) xb = __builtin_amdgcn_mfma_f32_16x16x32_bf16(kf[s], qb[s], xb, 0, 0, 0); }
      const s16x4 pa = soft_step(s0, xa, mask, t0 + 4 * g, la);
      s16x4 pb; if (SH) pb = soft_step(s1, xb, mask, t0 + 4 * g, lb);
      if (seen < needv) seen = lds_wait_ge(ctl + C_LANDED, needv);
      s16x4 vf[8];
#pragma unroll
      for (int blk = 0; blk < 8; ++blk) vf[blk] = __builtin_amdgcn_ds_read_tr16_b64_v4i16((LAS s16x4*)(sl + (va0 ^ (blk << 5))));
      ++j;
      asm volatile("s_waitcnt lgkmcnt(0)" : "+v"(vf[0]), "+v"(vf[1]), "+v"(vf[2]), "+v"(vf[3]), "+v"(vf[4]), "+v"(vf[5]), "+v"(vf[6]), "+v"(vf[7]) :: "memory");
      lds_st(ctl + C_REL + 4 * cw, j);
#pragma unroll
      for (int blk = 0; blk < 8; ++blk) { s0.o[blk] = __builtin_amdgcn_mfma_f32_16x16x16bf16_1k(vf[blk], pa, s0.o[blk], 0, 0, 0);
        if (SH) s1.o[blk] = __builtin_amdgcn_mfma_f32_16x16x16bf16_1k(vf[blk], pb, s1.o[blk], 0, 0, 0); }
    };
    unsigned m = p.xmask;
    for (int kx = 0;; ++kx) {
      const bool early = kx == 0, last = !early && m == 0;
      const int b = (early || last) ? w : __builtin_ctz(m);
      const int i0 = early ? 0 : last ? NA + hp : rdl(lo, b) - S_w;
      const int i1 = early ? NA : last ? T_w : rdl(hi, b) - S_w;
      const int lp0 = last ? lpB : rdl(lo, b) + rdl(xoff, b);
      bf16x8 qa[4];
      if (b < 8) {
#pragma unroll
        for (int s = 0; s < 4; ++s) qa[s] = qf[0][s];
      } else {
#pragma unroll
        for (int s = 0; s < 4; ++s) qa[s] = qf[1][s];
      }
      const int lb = rdl(len, b);
      soft_reset(s0);
      if (last) lds_wait_ge(ctl + C_RFREE, kx);
      for (int i = 4 * j + cw; i < i1; i += 4) {
        if (early && i >= vA) {
          lds_wait_ge(ctl + C_LANDED, 2 * i + 2); ++j; lds_st(ctl + C_REL + 4 * cw, j);
        } else {
          const int t0 = (early ? lpA - i : lp0 + i - i0) * PAGE;
          page(std::false_type{}, qa, qa, lb, lb, t0, t0 + PAGE > lb);
        }
      }
      if (kx > 0 && !last) lds_wait_ge(ctl + C_RFREE, kx);
      const float lt = xrow16_sum(s0.l);
      if ((c >> 1) == (b & 7)) {
        LAS unsigned char* r = lds + L_RECX + (cw * 2 + (c & 1)) * ROW;
#pragma unroll
        for (int blk = 0; blk < 8; ++blk) *(LAS f32x4*)(r + blk * 64 + g * 16) = s0.o[blk];
        if (g == 0) *(LAS f32x2*)(r + 512) = f32x2{s0.m, lt};
      }
      lds_drain();
      if (lane == 0) __hip_atomic_fetch_add((LAS int*)(ctl + C_RCNT), 1, __ATOMIC_RELAXED, __HIP_MEMORY_SCOPE_WORKGROUP);
      if (last) break;
      if (early) {
        if (ns > 0) {
          soft_reset(s0); soft_reset(s1);
          for (int i = cw; i < ns; i += 4) { const int t0 = (ss + i) * PAGE; page(std::true_type{}, qf[0], qf[1], len0, len1, t0, t0 + PAGE > minlen); }
          if (cw < ns) {
            s0.l = xrow16_sum(s0.l); s1.l = xrow16_sum(s1.l);
            if (cw > 0) lds_wait_ge(ctl + C_CSTEP, cw);
#pragma unroll
            for (int cg = 0; cg < 2; ++cg) { Soft& s = cg ? s1 : s0;
              LAS unsigned char* r = lds + L_MRG + (16 * cg + c) * ROW;
              if (cw > 0) {
                const f32x2 ml = *(LAS f32x2*)(r + 512);
                const float M = fmaxf(ml[0], s.m), f0 = mfac(ml[0], M), f1 = mfac(s.m, M);
#pragma unroll
                for (int blk = 0; blk < 8; ++blk) s.o[blk] = *(LAS f32x4*)(r + blk * 64 + g * 16) * f0 + s.o[blk] * f1;
                s.l = ml[1] * f0 + s.l * f1; s.m = M;
              }
#pragma unroll
              for (int blk = 0; blk < 8; ++blk) *(LAS f32x4*)(r + blk * 64 + g * 16) = s.o[blk];
              lds_drain();
              if (g == 0) *(LAS f32x2*)(r + 512) = f32x2{s.m, s.l};
            }
            lds_drain();
            lds_st(ctl + C_CSTEP, cw + 1);
          }
        }
      } else m &= m - 1;
    }
  } else {
    typedef unsigned u32x16 __attribute__((ext_vector_type(16)));
    u32x16 ka = *(const __attribute__((address_space(4))) u32x16*)__builtin_amdgcn_kernarg_segment_ptr();
    if (lane < 12) *(LAS int*)(ctl + 4 * lane) = 0;
    wg_barrier();
    asm volatile("" : "+s"(ka));
    const uintptr_t btptr = ((uintptr_t)ka[7] << 32) | ka[6], lensptr = ((uintptr_t)ka[9] << 32) | ka[8];
    GAS u32x2* outp = (GAS u32x2*)(((uintptr_t)ka[11] << 32) | ka[10]);
    unsigned char* ws = (unsigned char*)(((uintptr_t)ka[13] << 32) | ka[12]);
    len_raw = *(const GAS int*)(lensptr + 4 * c);
    i32x4 rk16[16];
#pragma unroll
    for (int k = 0; k < 16; ++k) rk16[k] = *(const GAS i32x4*)(btptr + k * (NB * 4) + 16 * lane);
    const unsigned long long epoch = __hip_atomic_load((gu64*)(ws + W_EPOCH), RLX_AGENT);
#pragma unroll
    for (int k = 0; k < 16; ++k) *(LAS i32x4*)(lds + L_TAB + (k * NB + 4 * lane) * 4) = rk16[k];
    i32x4 acc = rk16[1] ^ rk16[0];
#pragma unroll
    for (int k = 2; k < 16; ++k) acc |= rk16[k] ^ rk16[0];
    int P;
    {
      const unsigned long long b0 = __ballot(acc[0] != 0), b1 = __ballot(acc[1] != 0), b2 = __ballot(acc[2] != 0), b3 = __ballot(acc[3] != 0);
      const unsigned long long any = b0 | b1 | b2 | b3;
      const int l = any ? (int)__builtin_ctzll(any) : 64;
      const unsigned long long bit = any & (0ull - any);
      P = 4 * l + ((b0 & bit) ? 0 : (b1 & bit) ? 1 : (b2 & bit) ? 2 : (b3 & bit) ? 3 : 0);
    }
    if (lane == 0) { *(LAS int*)(ctl + C_P) = P; *(LAS u32x2*)(ctl + C_PTR) = u32x2{ka[0], ka[1]}; }
    if (lane < 16) *(LAS int*)(ctl + C_LENS + 4 * lane) = len_raw;
    lds_drain();
    lds_st(ctl + C_READY, 1);
    const Part p = partition(P);
    const __amdgpu_buffer_rsrc_t prsrc = __builtin_amdgcn_make_buffer_rsrc(ws + W_PART, 0, PART_BYTES, 0x00020000);
    const unsigned long long tag = epoch + 1ull;
    const int ns = p.ns;
    const unsigned blk = w * 16 + h;
    const int r = lane >> 5, ch = lane & 31;
    float M = -INFINITY, L = 0.f; f32x4 o = {0.f, 0.f, 0.f, 0.f};
    unsigned m = p.xmask;
#pragma nounroll
    for (int kx = 0;; ++kx) {
      const bool early = kx == 0, last = !early && m == 0;
      if (kx == 1 && ns > 0) {
        lds_wait_ge(ctl + C_CSTEP, min(ns, NSW));
#pragma unroll 4
        for (int k = 0; k < 16; ++k) {
          const u32x4 v = *(LAS u32x4*)(lds + L_MRG + (2 * k + r) * ROW + ch * 16);
          __builtin_amdgcn_raw_buffer_store_b128(v, prsrc, k * 1024 + lane * 16, blk * PS_BYTES, 16); }
        if (lane < 32) { const u32x2 v = *(LAS u32x2*)(lds + L_MRG + lane * ROW + 512);
          __builtin_amdgcn_raw_buffer_store_b64(v, prsrc, 32 * 512 + lane * 8, blk * PS_BYTES, 16); }
      }
      if (last) {
        asm volatile("s_waitcnt vmcnt(0)" ::: "memory");
        if (lane == 0) __hip_atomic_store((gu64*)(ws + W_FLAG + blk * FLAG_STRIDE), tag, RLX_AGENT);
        const int E_w = rdl(p.E, w), exc_w = rdl(p.exc, w);
        const bool sh_ov = c < p.nsh;
        const bool ex_ov = min(p.S + p.spare, E_w + exc_w) > max(p.S, E_w);
        unsigned cmask = (unsigned)(__ballot(sh_ov) & 0xFFFFull) | ((unsigned)(__ballot(ex_ov) & 0xFFFFull) << 16);
        if (cmask) lds_wait_ge(ctl + C_LANDED, (p.TL * POLLAT) >> 3);
        {
          const bool need = lane < 16 && (sh_ov || ex_ov);
          gu64* f = (gu64*)(ws + W_FLAG + (size_t)(c * 16 + h) * FLAG_STRIDE);
#pragma nounroll
          for (unsigned spins = 0; spins < (1u << 16); ++spins) {
            const bool ok = !need || __hip_atomic_load(f, RLX_AGENT) == tag;
            if (__all(ok)) break;
            __builtin_amdgcn_s_sleep(32);
          }
          asm volatile("" ::: "memory");
        }
        const unsigned vso = lane < 16 ? (unsigned)(c * 16 + h) * PS_BYTES + (unsigned)(2 * w) * 512 : W_PX + ((unsigned)(c * 16 + h) * 16 + w) * PX_BYTES;
        const unsigned vsm = lane < 16 ? (unsigned)(c * 16 + h) * PS_BYTES + 32 * 512 + (unsigned)(2 * w) * 8 : vso + 1024;
        constexpr int GB = 6;
        while (cmask) {
          u32x4 po[GB]; u32x2 pml[GB]; bool pv[GB];
#pragma unroll
          for (int k = 0; k < GB; ++k) {
            pv[k] = cmask != 0;
            const int id = pv[k] ? __builtin_ctz(cmask) : 0; cmask &= cmask - 1;
            const unsigned so = pv[k] ? (unsigned)rdl((int)vso, id) : PART_BYTES, sm = pv[k] ? (unsigned)rdl((int)vsm, id) : PART_BYTES;
            po[k] = __builtin_amdgcn_raw_buffer_load_b128(prsrc, lane * 16 + so, 0, 16);
            pml[k] = __builtin_amdgcn_raw_buffer_load_b64(prsrc, r * 8 + sm, 0, 16);
          }
#pragma unroll
          for (int k = 0; k < GB; ++k) {
            const float mk = pv[k] ? __uint_as_float(pml[k][0]) : -INFINITY, lk = __uint_as_float(pml[k][1]);
            const float Mn = fmaxf(M, mk), f0 = mfac(M, Mn), f1 = mfac(mk, Mn);
            o = o * f0 + __builtin_bit_cast(f32x4, po[k]) * f1; L = L * f0 + lk * f1; M = Mn;
          }
        }
        if (blk == 0) {
#pragma nounroll
          for (unsigned spins = 0; spins < (1u << 16); ++spins) {
            bool ok = true;
#pragma unroll
            for (int k = 0; k < 4; ++k) ok &= __hip_atomic_load((gu64*)(ws + W_FLAG + (size_t)(k * 64 + lane) * FLAG_STRIDE), RLX_AGENT) == tag;
            if (__all(ok)) break;
            __builtin_amdgcn_s_sleep(8);
          }
          if (lane == 0) __hip_atomic_store((gu64*)(ws + W_EPOCH), tag, RLX_AGENT);
        }
      }
      lds_wait_ge(ctl + C_RCNT, NSW * (kx + 1));
      f32x2 ml[4]; f32x4 ok[4];
#pragma unroll
      for (int k = 0; k < 4; ++k) { ml[k] = *(LAS f32x2*)(lds + L_RECX + (k * 2 + r) * ROW + 512); ok[k] = *(LAS f32x4*)(lds + L_RECX + (k * 2 + r) * ROW + ch * 16); }
      const bool mine = early || last;
      float Mx = mine ? M : -INFINITY;
#pragma unroll
      for (int k = 0; k < 4; ++k) Mx = fmaxf(Mx, ml[k][0]);
      const float fr = mine ? mfac(M, Mx) : 0.f;
      float Lx = L * fr; f32x4 ox = o * fr;
#pragma unroll
      for (int k = 0; k < 4; ++k) { const float f = mfac(ml[k][0], Mx); Lx = fmaf(f, ml[k][1], Lx); ox += ok[k] * f; }
      asm volatile("s_waitcnt lgkmcnt(0)" : "+v"(ox) :: "memory");
      if (last) {
        const float inv = __builtin_amdgcn_rcpf(Lx);
        outp[((w * HQ + 2 * h + r) * D + 4 * ch) >> 2] = u32x2{cvtpk(ox[0] * inv, ox[1] * inv), cvtpk(ox[2] * inv, ox[3] * inv)};
        break;
      }
      lds_st(ctl + C_RFREE, kx + 1);
      if (early) { M = Mx; L = Lx; o = ox; }
      else {
        const unsigned so = W_PX + (blk * 16 + __builtin_ctz(m)) * PX_BYTES;
        __builtin_amdgcn_raw_buffer_store_b128(__builtin_bit_cast(u32x4, ox), prsrc, lane * 16, so, 16);
        const f32x4 mlv = {__builtin_bit_cast(float, rdl(__builtin_bit_cast(int, Mx), 0)), __builtin_bit_cast(float, rdl(__builtin_bit_cast(int, Lx), 0)),
                           __builtin_bit_cast(float, rdl(__builtin_bit_cast(int, Mx), 32)), __builtin_bit_cast(float, rdl(__builtin_bit_cast(int, Lx), 32))};
        if (lane < 8) __builtin_amdgcn_raw_buffer_store_b128(__builtin_bit_cast(u32x4, mlv), prsrc, 1024 + lane * 16, so, 16);
        m &= m - 1;
      }
    }
  }
}

extern "C" void kernel_launch(void* const* d_in, const int* in_sizes, int n_in, void* d_out, int out_size, void* d_ws, size_t ws_size, hipStream_t stream) {
  Args a;
  a.q = (const bf16x8*)d_in[0]; a.k = d_in[1]; a.v = d_in[2]; a.bt = (const int*)d_in[3]; a.lens = (const int*)d_in[4];
  a.out = (unsigned*)d_out; a.ws = (unsigned char*)d_ws; a.kv_bytes = (unsigned)in_sizes[1] * 2u; a.pad = 0;
  (void)hipFuncSetAttribute((const void*)radix_attn, hipFuncAttributeMaxDynamicSharedMemorySize, LDS_BYTES);
  hipLaunchKernelGGL(radix_attn, dim3(256), dim3(NTHR), LDS_BYTES, stream, a);
}
```

```cpp
#include <hip/hip_runtime.h>
#include <hip/hip_bf16.h>
#include <stdint.h>
#include <type_traits>

constexpr int NSLOT = 16;
constexpr int INFL = 20;
constexpr int CLS01 = 4;
constexpr int SHCAP = 21;
constexpr int POLLAT = 9;
constexpr int NA = 12;
constexpr unsigned PTESTEP = 8u << 20;
constexpr int PTEH = 2;

constexpr int B = 16, HQ = 32, HKV = 16, D = 128, PAGE = 16, NB = 256, LK = NB * PAGE;
constexpr int NSW = 4, NTHR = (NSW + 2) * 64;
constexpr float C2 = 0.088388347648318440f * 1.4426950408889634f;
constexpr unsigned PAGE_BYTES = PAGE * HKV * D * 2;
constexpr unsigned TOK_BYTES = HKV * D * 2;

constexpr int ROW = 528;
constexpr int L_RING = 0;
constexpr int L_MRG = NSLOT * 8192;
constexpr int L_TAB = L_RING + (NSLOT - 2) * 8192;
constexpr int L_RECX = L_MRG + 32 * ROW;
constexpr int L_LIST = L_RECX + 4 * 2 * ROW;
constexpr int L_MISC = L_LIST + 1024;
constexpr int C_LANDED = 0, C_CSTEP = 4, C_RCNT = 8, C_RFREE = 12, C_REL = 16, C_P = 32, C_LIST = 36, C_TL = 44, C_READY = 40, C_PTR = 48, C_LENS = 80;
constexpr int LDS_BYTES = L_MISC + 160;
static_assert(NSLOT % 4 == 0 && (NSLOT & (NSLOT - 1)) == 0, "ring geometry");
static_assert(LDS_BYTES <= 160 * 1024, "LDS");
static_assert(NA % 4 == 0 && NA <= NSLOT - 2, "the table image lives in the last two ring slots until the list is built");
static_assert(INFL + CLS01 + 4 <= 28, "closing waits");

constexpr size_t W_EPOCH = 0, W_FLAG = 256, FLAG_STRIDE = 128;
constexpr size_t W_PART = 65536;
constexpr unsigned PS_BYTES = 32 * 512 + 256, PX_BYTES = 2 * 512 + 128;
constexpr unsigned W_PX = 256 * PS_BYTES;
constexpr unsigned PART_BYTES = W_PX + 256 * 16 * PX_BYTES;

typedef __bf16 bf16x8 __attribute__((ext_vector_type(8)));
typedef __bf16 bf16x2 __attribute__((ext_vector_type(2)));
typedef short s16x4 __attribute__((ext_vector_type(4)));
typedef float f32x4 __attribute__((ext_vector_type(4)));
typedef float f32x2 __attribute__((ext_vector_type(2)));
typedef unsigned u32x4 __attribute__((ext_vector_type(4)));
typedef unsigned u32x2 __attribute__((ext_vector_type(2)));
typedef int i32x4 __attribute__((ext_vector_type(4)));
#define LAS __attribute__((address_space(3)))
#define GAS __attribute__((address_space(1)))
#define DI __device__ __forceinline__
typedef GAS unsigned long long gu64;
#define RLX_AGENT __ATOMIC_RELAXED, __HIP_MEMORY_SCOPE_AGENT

DI int rfl(int x) { return __builtin_amdgcn_readfirstlane(x); }
DI int rdl(int x, int l) { return __builtin_amdgcn_readlane(x, l); }
template <int CTRL> DI int dpp_i(int oldv, int x) { return __builtin_amdgcn_update_dpp(oldv, x, CTRL, 0xf, 0xf, false); }
DI int row_scan_add(int x) {
  x += dpp_i<0x111>(0, x); x += dpp_i<0x112>(0, x); x += dpp_i<0x114>(0, x); x += dpp_i<0x118>(0, x); return x;
}
DI int row_scan_max(int x) {
  x = max(x, dpp_i<0x111>(0, x)); x = max(x, dpp_i<0x112>(0, x)); x = max(x, dpp_i<0x114>(0, x)); x = max(x, dpp_i<0x118>(0, x)); return x;
}
DI float xrow16_max(float x) {
  auto s = __builtin_amdgcn_permlane16_swap(__float_as_uint(x), __float_as_uint(x), false, false);
  x = fmaxf(__uint_as_float(s[0]), __uint_as_float(s[1]));
  auto t = __builtin_amdgcn_permlane32_swap(__float_as_uint(x), __float_as_uint(x), false, false);
  return fmaxf(__uint_as_float(t[0]), __uint_as_float(t[1]));
}
DI float xrow16_sum(float x) {
  auto s = __builtin_amdgcn_permlane16_swap(__float_as_uint(x), __float_as_uint(x), false, false);
  x = __uint_as_float(s[0]) + __uint_as_float(s[1]);
  auto t = __builtin_amdgcn_permlane32_swap(__float_as_uint(x), __float_as_uint(x), false, false);
  return __uint_as_float(t[0]) + __uint_as_float(t[1]);
}
DI unsigned cvtpk(float lo, float hi) { f32x2 v = {lo, hi}; bf16x2 b = __builtin_convertvector(v, bf16x2); return __builtin_bit_cast(unsigned, b); }
DI float ex2(float x) { return __builtin_amdgcn_exp2f(x); }
DI void wg_barrier() { asm volatile("s_waitcnt lgkmcnt(0)\n\ts_barrier" ::: "memory"); }
template <int N> DI void wait_vm() { asm volatile("s_waitcnt vmcnt(%0)" :: "i"(N) : "memory"); }

DI void set_m0(unsigned m0v, unsigned so) { asm volatile("s_mov_b32 m0, %0\n\ts_nop 3" :: "s"(m0v), "s"(so) : "memory"); }
template <int X> DI void wait_dma4(__amdgpu_buffer_rsrc_t rsrc, unsigned soff, unsigned v0, unsigned v1, unsigned v2, unsigned v3) {
  asm volatile("s_waitcnt vmcnt(%6)\n\t"
               "buffer_load_dwordx4 %0, %4, %5 offen nt lds\n\t"
               "buffer_load_dwordx4 %1, %4, %5 offen offset:1024 nt lds\n\t"
               "buffer_load_dwordx4 %2, %4, %5 offen offset:2048 nt lds\n\t"
               "buffer_load_dwordx4 %3, %4, %5 offen offset:3072 nt lds"
               :: "v"(v0), "v"(v1), "v"(v2), "v"(v3), "s"(rsrc), "s"(soff), "i"(X) : "memory");
}
DI void dma4r(__amdgpu_buffer_rsrc_t r0, __amdgpu_buffer_rsrc_t r1, __amdgpu_buffer_rsrc_t r2, __amdgpu_buffer_rsrc_t r3, unsigned soff, unsigned v0, unsigned v1, unsigned v2, unsigned v3) {
  asm volatile("s_nop 0\n\t"
               "buffer_load_dwordx4 %0, %4, %8 offen nt lds\n\t"
               "buffer_load_dwordx4 %1, %5, %8 offen offset:1024 nt lds\n\t"
               "buffer_load_dwordx4 %2, %6, %8 offen offset:2048 nt lds\n\t"
               "buffer_load_dwordx4 %3, %7, %8 offen offset:3072 nt lds"
               :: "v"(v0), "v"(v1), "v"(v2), "v"(v3), "s"(r0), "s"(r1), "s"(r2), "s"(r3), "s"(soff) : "memory");
}
#define WAIT_K(X, so) wait_dma4<X>(rk, so, vk[0], vk[1], vk[2], vk[3])
#define WAIT_V(X, so) wait_dma4<X>(rv, so, vv[0], vv[1], vv[2], vv[3])

template <int X>
DI void page_asm(__amdgpu_buffer_rsrc_t rk, __amdgpu_buffer_rsrc_t rv, const unsigned (&vk)[4], const unsigned (&vv)[4], unsigned vctl, unsigned vp,
                 int& i, int& pub, unsigned& so, unsigned& mk) {
  unsigned vt;
  asm volatile("s_waitcnt vmcnt(%[X])\n\t"
               "buffer_load_dwordx4 %[k0], %[rk], %[so] offen nt lds\n\t"
               "buffer_load_dwordx4 %[k1], %[rk], %[so] offen offset:1024 nt lds\n\t"
               "buffer_load_dwordx4 %[k2], %[rk], %[so] offen offset:2048 nt lds\n\t"
               "buffer_load_dwordx4 %[k3], %[rk], %[so] offen offset:3072 nt lds\n\t"
               "v_mov_b32 %[vt], %[pub]\n\t"
               "s_add_u32 m0, m0, 0x1000\n\t"
               "ds_write_b32 %[vctl], %[vt]\n\t"
               "s_add_u32 %[pub], %[pub], 1\n\t"
               "s_waitcnt vmcnt(%[X])\n\t"
               "buffer_load_dwordx4 %[v0], %[rv], %[so] offen nt lds\n\t"
               "buffer_load_dwordx4 %[v1], %[rv], %[so] offen offset:1024 nt lds\n\t"
               "buffer_load_dwordx4 %[v2], %[rv], %[so] offen offset:2048 nt lds\n\t"
               "buffer_load_dwordx4 %[v3], %[rv], %[so] offen offset:3072 nt lds\n\t"
               "v_mov_b32 %[vt], %[pub]\n\t"
               "s_add_u32 %[i], %[i], 1\n\t"
               "ds_write_b32 %[vctl], %[vt]\n\t"
               "v_readlane_b32 %[so], %[vp], %[i]\n\t"
               "s_add_u32 %[pub], %[pub], 1\n\t"
               "s_add_u32 %[mk], %[mk], 0x2000\n\t"
               "s_and_b32 %[mk], %[mk], %[msk]\n\t"
               "s_mov_b32 m0, %[mk]"
               : [i] "+s"(i), [pub] "+s"(pub), [so] "+s"(so), [mk] "+s"(mk), [vt] "=&v"(vt)
               : [X] "i"(X), [msk] "i"(NSLOT * 8192 - 1), [k0] "v"(vk[0]), [k1] "v"(vk[1]), [k2] "v"(vk[2]), [k3] "v"(vk[3]), [v0] "v"(vv[0]), [v1] "v"(vv[1]), [v2] "v"(vv[2]), [v3] "v"(vv[3]),
                 [rk] "s"(rk), [rv] "s"(rv), [vctl] "v"(vctl), [vp] "v"(vp) : "memory", "scc");
}

struct Args { const bf16x8* q; const void* k; const void* v; const int* bt; const int* lens; unsigned* out; unsigned char* ws; unsigned kv_bytes; unsigned pad; };

DI float mfac(float m, float M) { return (m == -INFINITY) ? 0.f : ex2((m - M) * C2); }

struct Soft { float m, l; f32x4 o[8]; };
DI void soft_reset(Soft& s) { s.m = -INFINITY; s.l = 0.f;
#pragma unroll
  for (int i = 0; i < 8; ++i) s.o[i] = f32x4{0.f, 0.f, 0.f, 0.f}; }

DI s16x4 soft_step(Soft& s, f32x4 x, bool mask, int tok0, int len) {
  if (mask) {
#pragma unroll
    for (int r = 0; r < 4; ++r) x[r] = (tok0 + r < len) ? x[r] : -INFINITY;
  }
  float pm = fmaxf(fmaxf(x[0], x[1]), fmaxf(x[2], x[3]));
  pm = xrow16_max(pm);
  const float mn = fmaxf(s.m, pm);
  const float ms = (mn == -INFINITY) ? 0.f : mn * C2;
  const float alpha = ex2(s.m * C2 - ms);
  float p[4];
#pragma unroll
  for (int r = 0; r < 4; ++r) p[r] = ex2(fmaf(x[r], C2, -ms));
  s.l = fmaf(s.l, alpha, (p[0] + p[1]) + (p[2] + p[3]));
  s.m = mn;
#pragma unroll
  for (int i = 0; i < 8; ++i) s.o[i] *= alpha;
  u32x2 pk = {cvtpk(p[0], p[1]), cvtpk(p[2], p[3])};
  return __builtin_bit_cast(s16x4, pk);
}


DI int lds_ld(LAS unsigned char* p) { return __hip_atomic_load((LAS int*)p, __ATOMIC_RELAXED, __HIP_MEMORY_SCOPE_WORKGROUP); }
DI void lds_st(LAS unsigned char* p, int v) { __hip_atomic_store((LAS int*)p, v, __ATOMIC_RELAXED, __HIP_MEMORY_SCOPE_WORKGROUP); }
DI int lds_wait_ge(LAS unsigned char* p, int need) {
  int v;
#pragma nounroll
  for (unsigned spins = 0;; ++spins) { v = rfl(lds_ld(p)); if (v >= need || spins > (1u << 20)) break; __builtin_amdgcn_s_sleep(1); }
  asm volatile("" ::: "memory");
  return v;
}
DI void lds_drain() { asm volatile("s_waitcnt lgkmcnt(0)" ::: "memory"); }

__global__ void __launch_bounds__(NTHR, 2) radix_attn(Args) {
  extern __shared__ __attribute__((aligned(16))) unsigned char lds_raw[];
  LAS unsigned char* lds = (LAS unsigned char*)lds_raw;
  LAS unsigned char* ctl = lds + L_MISC;
  const int tid = threadIdx.x, lane = tid & 63, wave = rfl(tid >> 6);
  const int w = blockIdx.x & 15, h = blockIdx.x >> 4;
  const int c = lane & 15, g = lane >> 4;
  int len_raw;

  struct Part { int len, Pe, S, E, spare, exc, lo, hi, xoff, S_w, spare_w, T_w, ns, ss, hp, nsh, np_w, own_w, vA, TL; unsigned xmask; bool all_masked; };
  auto partition = [&](int P) {
    Part p;
    p.all_masked = len_raw <= 0;
    p.len = p.all_masked ? LK : min(len_raw, LK);
    const int np = (p.len + PAGE - 1) >> 4;
    p.Pe = min(P, rdl(row_scan_max(np), 15));
    const int n = max(np - p.Pe, 0);
    const int W = p.Pe + rdl(row_scan_add(n), 15);
    const int T = (W >> 4) + (c < (W & 15) ? 1 : 0);
    const int cap = max(1, ((W >> 4) * SHCAP) >> 6);
    int lg = 0;
    if (p.Pe > cap) lg = 1;
    if (p.Pe > 2 * cap) lg = 2;
    if (p.Pe > 4 * cap) lg = 3;
    if (p.Pe > 8 * cap) lg = 4;
    const int q = p.Pe >> lg, rq = p.Pe & ((1 << lg) - 1);
    const int sh = c < (1 << lg) ? q + (c < rq ? 1 : 0) : 0;
    p.nsh = min(1 << lg, p.Pe);
    p.ns = w < (1 << lg) ? q + (w < rq ? 1 : 0) : 0;
    p.ss = w * q + min(w, rq);
    const int own = min(n, T - sh);
    p.exc = n - own; p.spare = T - sh - own;
    p.S = row_scan_add(p.spare) - p.spare;
    p.E = row_scan_add(p.exc) - p.exc;
    p.S_w = rdl(p.S, w); p.spare_w = rdl(p.spare, w); p.T_w = rdl(T, w);
    p.hp = p.ns + p.spare_w;
    p.lo = max(p.S_w, p.E); p.hi = min(p.S_w + p.spare_w, p.E + p.exc);
    p.xmask = (unsigned)(__ballot(p.hi > p.lo) & 0xFFFFull);
    p.xoff = p.Pe - p.E;
    p.np_w = rdl(np, w); p.own_w = rdl(own, w);
    p.vA = min(NA, p.own_w);
    p.TL = NA + p.hp + p.own_w - p.vA;
    return p;
  };

  if (__builtin_expect(wave == 0, 1)) {
    __builtin_amdgcn_s_setprio(3);
    typedef unsigned u32x16 __attribute__((ext_vector_type(16)));
    u32x16 ka = *(const __attribute__((address_space(4))) u32x16*)__builtin_amdgcn_kernarg_segment_ptr();
    asm volatile("" : "+s"(ka));
    const uintptr_t kptr = ((uintptr_t)ka[3] << 32) | ka[2], vptr = ((uintptr_t)ka[5] << 32) | ka[4], btptr = ((uintptr_t)ka[7] << 32) | ka[6], lensptr = ((uintptr_t)ka[9] << 32) | ka[8];
    const unsigned kv_bytes = ka[14];
    len_raw = *(const GAS int*)(lensptr + 4 * c);
    const i32x4 rw = *(const GAS i32x4*)(btptr + w * (NB * 4) + 16 * lane);
    asm volatile("s_barrier" ::: "memory");
    unsigned vk[4], vv[4];
#pragma unroll
    for (int i = 0; i < 4; ++i) { const int t = 4 * i + g;
      vk[i] = t * TOK_BYTES + ((c ^ t) << 4) - 1024 * i;
      vv[i] = t * TOK_BYTES + ((c ^ ((t & 7) << 1)) << 4) - 1024 * i; }
    asm volatile("" : "+v"(vk[0]), "+v"(vk[1]), "+v"(vk[2]), "+v"(vk[3]), "+v"(vv[0]), "+v"(vv[1]), "+v"(vv[2]), "+v"(vv[3]));
    const unsigned ring = (unsigned)(uintptr_t)(lds + L_RING);
    const __amdgpu_buffer_rsrc_t rk = __builtin_amdgcn_make_buffer_rsrc((void*)kptr, 0, kv_bytes, 0x00020000);
    const __amdgpu_buffer_rsrc_t rv = __builtin_amdgcn_make_buffer_rsrc((void*)vptr, 0, kv_bytes, 0x00020000);
    *(LAS i32x4*)(lds + L_MRG + 16 * lane) = rw;
    unsigned vp0, vp1 = 0, vp2 = 0, vp3 = 0;
    int nq0;
    {
      const int lw = rdl(len_raw, w);
      const int npw = ((lw <= 0 ? LK : min(lw, LK)) + PAGE - 1) >> 4;
      vp0 = (unsigned)*(LAS int*)(lds + L_MRG + 4 * max(npw - 1 - lane, 0)) * PAGE_BYTES + (unsigned)h * (D * 2);
      nq0 = ((lw <= 0 ? LK : min(lw, LK)) - (npw - 1) * PAGE + 3) >> 2;
    }
    unsigned so = (unsigned)rdl((int)vp0, 0), m0k = ring;
    {
      const __amdgpu_buffer_rsrc_t k1 = __builtin_amdgcn_make_buffer_rsrc((void*)kptr, 0, nq0 > 1 ? kv_bytes : 0u, 0x00020000), k2 = __builtin_amdgcn_make_buffer_rsrc((void*)kptr, 0, nq0 > 2 ? kv_bytes : 0u, 0x00020000),
                                   k3 = __builtin_amdgcn_make_buffer_rsrc((void*)kptr, 0, nq0 > 3 ? kv_bytes : 0u, 0x00020000);
      set_m0(m0k, so);
      dma4r(rk, k1, k2, k3, so, vk[0], vk[1], vk[2], vk[3]);
      const __amdgpu_buffer_rsrc_t v1 = __builtin_amdgcn_make_buffer_rsrc((void*)vptr, 0, nq0 > 1 ? kv_bytes : 0u, 0x00020000), v2 = __builtin_amdgcn_make_buffer_rsrc((void*)vptr, 0, nq0 > 2 ? kv_bytes : 0u, 0x00020000),
                                   v3 = __builtin_amdgcn_make_buffer_rsrc((void*)vptr, 0, nq0 > 3 ? kv_bytes : 0u, 0x00020000);
      set_m0(m0k + 4096, so);
      dma4r(rv, v1, v2, v3, so, vv[0], vv[1], vv[2], vv[3]);
      so = (unsigned)rdl((int)vp0, 1); m0k = ring + 8192;
      set_m0(m0k, so);
    }
    static_assert(L_RING == 0 && C_LANDED == 0 && C_REL == 16, "page_asm addresses the ring from LDS offset 0 and the control words by these offsets");
    const unsigned vctl = (unsigned)(uintptr_t)ctl;
    auto publish = [&](int nl) { lds_st(ctl + C_LANDED, nl); };
    const bool wide = ((((unsigned)kptr >> 8) + h) & 3) == 1;
    int i = 1, credit = NSLOT;
    int T_w = NA, TM = NA;
    bool listed = false;
    unsigned mk = m0k;
    for (;;) {
      i = rfl(i); so = (unsigned)rfl((int)so); mk = (unsigned)rfl((int)mk);
      const int iend = rfl(min(min(TM, credit), (i & ~63) + 64));
      if (wide) { int pub = 2 * i - (INFL + CLS01 + 3) / 4;
#pragma nounroll
        while (i < iend) page_asm<INFL + CLS01>(rk, rv, vk, vv, vctl, vp0, i, pub, so, mk);
      } else { int pub = 2 * i - (INFL + 3) / 4;
#pragma nounroll
        while (i < iend) page_asm<INFL>(rk, rv, vk, vv, vctl, vp0, i, pub, so, mk);
      }
      if (!listed) {
        listed = true;
        lds_wait_ge(ctl + C_LIST, 1);
        T_w = rfl(*(LAS int*)(ctl + C_TL));
        TM = T_w >= NA + 4 ? T_w - 4 : T_w;
        vp0 = *(LAS unsigned*)(lds + L_LIST + 4 * lane); vp1 = *(LAS unsigned*)(lds + L_LIST + 256 + 4 * lane);
        vp2 = *(LAS unsigned*)(lds + L_LIST + 512 + 4 * lane); vp3 = *(LAS unsigned*)(lds + L_LIST + 768 + 4 * lane);
        so = (unsigned)rdl((int)vp0, NA);
        asm volatile("s_nop 4" :: "s"(so));
        if (i < min(TM, credit)) continue;
      }
      if (i >= T_w) break;
      const bool endgroup = i >= TM;
      const u32x4 rel = *(volatile LAS u32x4*)(ctl + C_REL);
      credit = min(min(4 * rfl((int)rel[0]), 4 * rfl((int)rel[1]) + 1), min(4 * rfl((int)rel[2]) + 2, 4 * rfl((int)rel[3]) + 3)) + NSLOT;
      if ((i & 63) == 0) { vp0 = vp1; vp1 = vp2; vp2 = vp3; so = (unsigned)rdl((int)vp0, 0); asm volatile("s_nop 4" :: "s"(so)); }
      if (credit <= i) {
        LAS unsigned char* relp = ctl + C_REL + 4 * (i & 3);
        const int need = (i >> 2) - NSLOT / 4 + 1;
        if (rfl(lds_ld(relp)) < need) { wait_vm<12>(); publish(2 * i - 3);
          if (rfl(lds_ld(relp)) < need) { wait_vm<4>(); publish(2 * i - 1);
            if (rfl(lds_ld(relp)) < need) { wait_vm<0>(); publish(2 * i); lds_wait_ge(relp, need); } } }
        credit = i + 1;
      }
      if (endgroup) {
        if (credit < T_w) { wait_vm<0>(); publish(2 * i);
          for (int e = i; e < T_w; ++e) if (e >= NSLOT) lds_wait_ge(ctl + C_REL + 4 * (e & 3), (e >> 2) - NSLOT / 4 + 1); }
        unsigned se[4];
        int pub = 2 * i - (INFL + 3) / 4;
#pragma unroll
        for (int r = 0; r < 4; ++r) {
          if (r > 0) { if (((i + r) & 63) == 0) { vp0 = vp1; vp1 = vp2; } so = (unsigned)rdl((int)vp0, (i + r) & 63); }
          se[r] = so;
          set_m0(((i + r) & (NSLOT - 1)) * 8192, so);
          WAIT_K(INFL, so);
          publish(pub + r);
        }
#pragma unroll
        for (int r = 0; r < 4; ++r) {
          set_m0(((i + r) & (NSLOT - 1)) * 8192 + 4096, se[r]);
          WAIT_V(INFL, se[r]);
          publish(pub + 4 + r);
        }
        break;
      }
    }
    wait_vm<24>(); publish(2 * T_w - 6);
    wait_vm<20>(); publish(2 * T_w - 5);
    wait_vm<16>(); publish(2 * T_w - 4);
    wait_vm<12>(); publish(2 * T_w - 3);
    wait_vm<8>(); publish(2 * T_w - 2);
    wait_vm<4>(); publish(2 * T_w - 1);
    wait_vm<0>(); publish(2 * T_w);
  } else if (wave <= NSW) {
    const int cw = wave - 1;
    wg_barrier();
    const unsigned ka0 = c * 256 + ((g ^ c) << 4);
    const int tv = 4 * g + (c >> 2);
    const unsigned va0 = 4096 + tv * 256 + ((tv & 7) << 5) + (c & 3) * 8;
    unsigned touch = 0, touch2 = 0;
    if (h < PTEH && cw == 0) {
      typedef unsigned u32x16 __attribute__((ext_vector_type(16)));
      const u32x16 ka = *(const __attribute__((address_space(4))) u32x16*)__builtin_amdgcn_kernarg_segment_ptr();
      const uintptr_t kb = ((uintptr_t)ka[3] << 32) | ka[2], vb = ((uintptr_t)ka[5] << 32) | ka[4], wb = ((uintptr_t)ka[13] << 32) | ka[12];
      const unsigned off = (unsigned)(lane & 31) * PTESTEP;
      if (off < ka[14]) touch = *(const GAS unsigned*)((lane < 32 ? kb : vb) + off);
      if (lane < 2) touch2 = *(const GAS unsigned*)(wb + W_PART + lane * (PART_BYTES / 2));
    }
    lds_wait_ge(ctl + C_READY, 1);
    bf16x8 qf[2][4];
    {
      const u32x2 pq = *(LAS u32x2*)(ctl + C_PTR);
      const GAS bf16x8* qp = (const GAS bf16x8*)(((uintptr_t)(unsigned)rfl((int)pq[1]) << 32) | (unsigned)rfl((int)pq[0]));
#pragma unroll
      for (int cg = 0; cg < 2; ++cg)
#pragma unroll
        for (int s = 0; s < 4; ++s) { const int r = 16 * cg + c; qf[cg][s] = qp[(((r >> 1) * HQ + 2 * h + (r & 1)) * D + 32 * s + 8 * g) / 8]; }
    }
    len_raw = *(LAS int*)(ctl + C_LENS + 4 * c);
    const Part p = partition(rfl(*(LAS int*)(ctl + C_P)));
    const int len = p.len, S_w = p.S_w - p.ns - NA, hp = p.hp, ss = p.ss, T_w = p.TL, lo = p.lo, hi = p.hi, xoff = p.xoff, ns = p.ns, lpA = p.np_w - 1, lpB = p.np_w - p.own_w, vA = p.vA;
    const int minlen = LK - rdl(row_scan_max(LK - len), 15);
    const int len0 = __shfl(len, c >> 1), len1 = __shfl(len, 8 + (c >> 1));
    {
      const bool z0 = __shfl((int)p.all_masked, c >> 1) != 0, z1 = __shfl((int)p.all_masked, 8 + (c >> 1)) != 0;
#pragma unroll
      for (int s = 0; s < 4; ++s) {
        u32x4 t0 = __builtin_bit_cast(u32x4, qf[0][s]), t1 = __builtin_bit_cast(u32x4, qf[1][s]);
#pragma unroll
        for (int e = 0; e < 4; ++e) { t0[e] = z0 ? 0u : t0[e]; t1[e] = z1 ? 0u : t1[e]; }
        qf[0][s] = __builtin_bit_cast(bf16x8, t0); qf[1][s] = __builtin_bit_cast(bf16x8, t1);
      }
    }
    asm volatile("" :: "v"(touch), "v"(touch2));
    const int TM = T_w >= NA + 4 ? T_w - 4 : T_w + 4;
    int j = 0, seen = 0;
    Soft s0, s1;
    auto page = [&](auto SHc, const bf16x8 (&qa)[4], const bf16x8 (&qb)[4], int la, int lb, int t0, bool mask) {
      constexpr bool SH = decltype(SHc)::value;
      const int i = 4 * j + cw;
      LAS unsigned char* sl = lds + L_RING + (i & (NSLOT - 1)) * 8192;
      const int re = i - TM;
      const int needk = re >= 0 ? 2 * TM + re + 1 : 2 * i + 1, needv = re >= 0 ? 2 * TM + re + 5 : 2 * i + 2;
      if (seen < needk) seen = lds_wait_ge(ctl + C_LANDED, needk);
      bf16x8 kf[4];
#pragma unroll
      for (int s = 0; s < 4; ++s) kf[s] = *(LAS bf16x8*)(sl + (ka0 ^ (s << 6)));
      f32x4 xa = {0.f, 0.f, 0.f, 0.f}, xb = {0.f, 0.f, 0.f, 0.f};
#pragma unroll
      for (int s = 0; s < 4; ++s) { xa = __builtin_amdgcn_mfma_f32_16x16x32_bf16(kf[s], qa[s], xa, 0, 0, 0);
        if (SH) xb = __builtin_amdgcn_mfma_f32_16x16x32_bf16(kf[s], qb[s], xb, 0, 0, 0); }
      const s16x4 pa = soft_step(s0, xa, mask, t0 + 4 * g, la);
      s16x4 pb; if (SH) pb = soft_step(s1, xb, mask, t0 + 4 * g, lb);
      if (seen < needv) seen = lds_wait_ge(ctl + C_LANDED, needv);
      s16x4 vf[8];
#pragma unroll
      for (int blk = 0; blk < 8; ++blk) vf[blk] = __builtin_amdgcn_ds_read_tr16_b64_v4i16((LAS s16x4*)(sl + (va0 ^ (blk << 5))));
      ++j;
      asm volatile("s_waitcnt lgkmcnt(0)" : "+v"(vf[0]), "+v"(vf[1]), "+v"(vf[2]), "+v"(vf[3]), "+v"(vf[4]), "+v"(vf[5]), "+v"(vf[6]), "+v"(vf[7]) :: "memory");
      lds_st(ctl + C_REL + 4 * cw, j);
#pragma unroll
      for (int blk = 0; blk < 8; ++blk) { s0.o[blk] = __builtin_amdgcn_mfma_f32_16x16x16bf16_1k(vf[blk], pa, s0.o[blk], 0, 0, 0);
        if (SH) s1.o[blk] = __builtin_amdgcn_mfma_f32_16x16x16bf16_1k(vf[blk], pb, s1.o[blk], 0, 0, 0); }
    };
    unsigned m = p.xmask;
    for (int kx = 0;; ++kx) {
      const bool early = kx == 0, last = !early && m == 0;
      const int b = (early || last) ? w : __builtin_ctz(m);
      const int i0 = early ? 0 : last ? NA + hp : rdl(lo, b) - S_w;
      const int i1 = early ? NA : last ? T_w : rdl(hi, b) - S_w;
      const int lp0 = last ? lpB : rdl(lo, b) + rdl(xoff, b);
      bf16x8 qa[4];
      if (b < 8) {
#pragma unroll
        for (int s = 0; s < 4; ++s) qa[s] = qf[0][s];
      } else {
#pragma unroll
        for (int s = 0; s < 4; ++s) qa[s] = qf[1][s];
      }
      const int lb = rdl(len, b);
      soft_reset(s0);
      if (last) lds_wait_ge(ctl + C_RFREE, kx);
      for (int i = 4 * j + cw; i < i1; i += 4) {
        if (early && i >= vA) {
          lds_wait_ge(ctl + C_LANDED, 2 * i + 2); ++j; lds_st(ctl + C_REL + 4 * cw, j);
        } else {
          const int t0 = (early ? lpA - i : lp0 + i - i0) * PAGE;
          page(std::false_type{}, qa, qa, lb, lb, t0, t0 + PAGE > lb);
        }
      }
      if (kx > 0 && !last) lds_wait_ge(ctl + C_RFREE, kx);
      const float lt = xrow16_sum(s0.l);
      if ((c >> 1) == (b & 7)) {
        LAS unsigned char* r = lds + L_RECX + (cw * 2 + (c & 1)) * ROW;
#pragma unroll
        for (int blk = 0; blk < 8; ++blk) *(LAS f32x4*)(r + blk * 64 + g * 16) = s0.o[blk];
        if (g == 0) *(LAS f32x2*)(r + 512) = f32x2{s0.m, lt};
      }
      lds_drain();
      if (lane == 0) __hip_atomic_fetch_add((LAS int*)(ctl + C_RCNT), 1, __ATOMIC_RELAXED, __HIP_MEMORY_SCOPE_WORKGROUP);
      if (last) break;
      if (early) {
        if (ns > 0) {
          soft_reset(s0); soft_reset(s1);
          for (int i = cw; i < ns; i += 4) { const int t0 = (ss + i) * PAGE; page(std::true_type{}, qf[0], qf[1], len0, len1, t0, t0 + PAGE > minlen); }
          if (cw < ns) {
            s0.l = xrow16_sum(s0.l); s1.l = xrow16_sum(s1.l);
            if (cw > 0) lds_wait_ge(ctl + C_CSTEP, cw);
#pragma unroll
            for (int cg = 0; cg < 2; ++cg) { Soft& s = cg ? s1 : s0;
              LAS unsigned char* r = lds + L_MRG + (16 * cg + c) * ROW;
              if (cw > 0) {
                const f32x2 ml = *(LAS f32x2*)(r + 512);
                const float M = fmaxf(ml[0], s.m), f0 = mfac(ml[0], M), f1 = mfac(s.m, M);
#pragma unroll
                for (int blk = 0; blk < 8; ++blk) s.o[blk] = *(LAS f32x4*)(r + blk * 64 + g * 16) * f0 + s.o[blk] * f1;
                s.l = ml[1] * f0 + s.l * f1; s.m = M;
              }
#pragma unroll
              for (int blk = 0; blk < 8; ++blk) *(LAS f32x4*)(r + blk * 64 + g * 16) = s.o[blk];
              lds_drain();
              if (g == 0) *(LAS f32x2*)(r + 512) = f32x2{s.m, s.l};
            }
            lds_drain();
            lds_st(ctl + C_CSTEP, cw + 1);
          }
        }
      } else m &= m - 1;
    }
  } else {
    typedef unsigned u32x16 __attribute__((ext_vector_type(16)));
    u32x16 ka = *(const __attribute__((address_space(4))) u32x16*)__builtin_amdgcn_kernarg_segment_ptr();
    if (lane < 12) *(LAS int*)(ctl + 4 * lane) = 0;
    wg_barrier();
    asm volatile("" : "+s"(ka));
    const uintptr_t btptr = ((uintptr_t)ka[7] << 32) | ka[6], lensptr = ((uintptr_t)ka[9] << 32) | ka[8];
    GAS u32x2* outp = (GAS u32x2*)(((uintptr_t)ka[11] << 32) | ka[10]);
    unsigned char* ws = (unsigned char*)(((uintptr_t)ka[13] << 32) | ka[12]);
    len_raw = *(const GAS int*)(lensptr + 4 * c);
    i32x4 rk16[16];
#pragma unroll
    for (int k = 0; k < 16; ++k) rk16[k] = *(const GAS i32x4*)(btptr + k * (NB * 4) + 16 * lane);
    const unsigned long long epoch = __hip_atomic_load((gu64*)(ws + W_EPOCH), RLX_AGENT);
#pragma unroll
    for (int k = 0; k < 16; ++k) *(LAS i32x4*)(lds + L_TAB + (k * NB + 4 * lane) * 4) = rk16[k];
    i32x4 acc = rk16[1] ^ rk16[0];
#pragma unroll
    for (int k = 2; k < 16; ++k) acc |= rk16[k] ^ rk16[0];
    int P;
    {
      const unsigned long long b0 = __ballot(acc[0] != 0), b1 = __ballot(acc[1] != 0), b2 = __ballot(acc[2] != 0), b3 = __ballot(acc[3] != 0);
      const unsigned long long any = b0 | b1 | b2 | b3;
      const int l = any ? (int)__builtin_ctzll(any) : 64;
      const unsigned long long bit = any & (0ull - any);
      P = 4 * l + ((b0 & bit) ? 0 : (b1 & bit) ? 1 : (b2 & bit) ? 2 : (b3 & bit) ? 3 : 0);
    }
    if (lane == 0) { *(LAS int*)(ctl + C_P) = P; *(LAS u32x2*)(ctl + C_PTR) = u32x2{ka[0], ka[1]}; }
    if (lane < 16) *(LAS int*)(ctl + C_LENS + 4 * lane) = len_raw;
    lds_drain();
    lds_st(ctl + C_READY, 1);
    const Part p = partition(P);
    {
      const int S_w = p.S_w - p.ns - NA, lpA = p.np_w - 1, lpB = p.np_w - p.own_w;
#pragma unroll
      for (int r = 0; r < 4; ++r) {
        const int i = 64 * r + lane;
        int idx = (i < NA) ? w * NB + max(lpA - i, 0) : (i < NA + p.ns) ? p.ss + (i - NA) : w * NB + lpB + (i - NA - p.hp);
        for (unsigned mm = p.xmask; mm; mm &= mm - 1) {
          const int b = __builtin_ctz(mm);
          const int lo_b = rdl(p.lo, b) - S_w, hi_b = rdl(p.hi, b) - S_w, off_b = rdl(p.xoff, b) + b * NB + S_w;
          if (i >= lo_b && i < hi_b) idx = i + off_b;
        }
        *(LAS unsigned*)(lds + L_LIST + 4 * i) = (unsigned)*(LAS int*)(lds + L_TAB + 4 * (i < p.TL ? idx : 0)) * PAGE_BYTES + (unsigned)h * (D * 2);
      }
      if (lane == 0) *(LAS int*)(ctl + C_TL) = p.TL;
      lds_drain();
      lds_st(ctl + C_LIST, 1);
    }
    unsigned tl[4] = {0u, 0u, 0u, 0u};
    {
      const uintptr_t kb = ((uintptr_t)ka[3] << 32) | ka[2], vb = ((uintptr_t)ka[5] << 32) | ka[4];
#pragma unroll
      for (int r = 0; r < 2; ++r) {
        const int i = 64 * r + lane;
        const unsigned src = *(LAS unsigned*)(lds + L_LIST + 4 * i), prv = *(LAS unsigned*)(lds + L_LIST + 4 * max(i - 1, 0));
        if (i >= NA && i < p.TL && ((src ^ prv) >> 21) != 0 && src < ka[14]) { tl[2 * r] = *(const GAS unsigned*)(kb + src); tl[2 * r + 1] = *(const GAS unsigned*)(vb + src); }
      }
    }
    const __amdgpu_buffer_rsrc_t prsrc = __builtin_amdgcn_make_buffer_rsrc(ws + W_PART, 0, PART_BYTES, 0x00020000);
    const unsigned long long tag = epoch + 1ull;
    const int ns = p.ns;
    const unsigned blk = w * 16 + h;
    const int r = lane >> 5, ch = lane & 31;
    float M = -INFINITY, L = 0.f; f32x4 o = {0.f, 0.f, 0.f, 0.f};
    unsigned m = p.xmask;
#pragma nounroll
    for (int kx = 0;; ++kx) {
      const bool early = kx == 0, last = !early && m == 0;
      if (kx == 1 && ns > 0) {
        lds_wait_ge(ctl + C_CSTEP, min(ns, NSW));
#pragma unroll 4
        for (int k = 0; k < 16; ++k) {
          const u32x4 v = *(LAS u32x4*)(lds + L_MRG + (2 * k + r) * ROW + ch * 16);
          __builtin_amdgcn_raw_buffer_store_b128(v, prsrc, k * 1024 + lane * 16, blk * PS_BYTES, 16); }
        if (lane < 32) { const u32x2 v = *(LAS u32x2*)(lds + L_MRG + lane * ROW + 512);
          __builtin_amdgcn_raw_buffer_store_b64(v, prsrc, 32 * 512 + lane * 8, blk * PS_BYTES, 16); }
      }
      if (last) {
        asm volatile("s_waitcnt vmcnt(0)" :: "v"(tl[0]), "v"(tl[1]), "v"(tl[2]), "v"(tl[3]) : "memory");
        if (lane == 0) __hip_atomic_store((gu64*)(ws + W_FLAG + blk * FLAG_STRIDE), tag, RLX_AGENT);
        const int E_w = rdl(p.E, w), exc_w = rdl(p.exc, w);
        const bool sh_ov = c < p.nsh;
        const bool ex_ov = min(p.S + p.spare, E_w + exc_w) > max(p.S, E_w);
        unsigned cmask = (unsigned)(__ballot(sh_ov) & 0xFFFFull) | ((unsigned)(__ballot(ex_ov) & 0xFFFFull) << 16);
        if (cmask) lds_wait_ge(ctl + C_LANDED, (p.TL * POLLAT) >> 3);
        {
          const bool need = lane < 16 && (sh_ov || ex_ov);
          gu64* f = (gu64*)(ws + W_FLAG + (size_t)(c * 16 + h) * FLAG_STRIDE);
#pragma nounroll
          for (unsigned spins = 0; spins < (1u << 16); ++spins) {
            const bool ok = !need || __hip_atomic_load(f, RLX_AGENT) == tag;
            if (__all(ok)) break;
            __builtin_amdgcn_s_sleep(32);
          }
          asm volatile("" ::: "memory");
        }
        const unsigned vso = lane < 16 ? (unsigned)(c * 16 + h) * PS_BYTES + (unsigned)(2 * w) * 512 : W_PX + ((unsigned)(c * 16 + h) * 16 + w) * PX_BYTES;
        const unsigned vsm = lane < 16 ? (unsigned)(c * 16 + h) * PS_BYTES + 32 * 512 + (unsigned)(2 * w) * 8 : vso + 1024;
        constexpr int GB = 6;
        while (cmask) {
          u32x4 po[GB]; u32x2 pml[GB]; bool pv[GB];
#pragma unroll
          for (int k = 0; k < GB; ++k) {
            pv[k] = cmask != 0;
            const int id = pv[k] ? __builtin_ctz(cmask) : 0; cmask &= cmask - 1;
            const unsigned so = pv[k] ? (unsigned)rdl((int)vso, id) : PART_BYTES, sm = pv[k] ? (unsigned)rdl((int)vsm, id) : PART_BYTES;
            po[k] = __builtin_amdgcn_raw_buffer_load_b128(prsrc, lane * 16 + so, 0, 16);
            pml[k] = __builtin_amdgcn_raw_buffer_load_b64(prsrc, r * 8 + sm, 0, 16);
          }
#pragma unroll
          for (int k = 0; k < GB; ++k) {
            const float mk = pv[k] ? __uint_as_float(pml[k][0]) : -INFINITY, lk = __uint_as_float(pml[k][1]);
            const float Mn = fmaxf(M, mk), f0 = mfac(M, Mn), f1 = mfac(mk, Mn);
            o = o * f0 + __builtin_bit_cast(f32x4, po[k]) * f1; L = L * f0 + lk * f1; M = Mn;
          }
        }
        if (blk == 0) {
#pragma nounroll
          for (unsigned spins = 0; spins < (1u << 16); ++spins) {
            bool ok = true;
#pragma unroll
            for (int k = 0; k < 4; ++k) ok &= __hip_atomic_load((gu64*)(ws + W_FLAG + (size_t)(k * 64 + lane) * FLAG_STRIDE), RLX_AGENT) == tag;
            if (__all(ok)) break;
            __builtin_amdgcn_s_sleep(8);
          }
          if (lane == 0) __hip_atomic_store((gu64*)(ws + W_EPOCH), tag, RLX_AGENT);
        }
      }
      lds_wait_ge(ctl + C_RCNT, NSW * (kx + 1));
      f32x2 ml[4]; f32x4 ok[4];
#pragma unroll
      for (int k = 0; k < 4; ++k) { ml[k] = *(LAS f32x2*)(lds + L_RECX + (k * 2 + r) * ROW + 512); ok[k] = *(LAS f32x4*)(lds + L_RECX + (k * 2 + r) * ROW + ch * 16); }
      const bool mine = early || last;
      float Mx = mine ? M : -INFINITY;
#pragma unroll
      for (int k = 0; k < 4; ++k) Mx = fmaxf(Mx, ml[k][0]);
      const float fr = mine ? mfac(M, Mx) : 0.f;
      float Lx = L * fr; f32x4 ox = o * fr;
#pragma unroll
      for (int k = 0; k < 4; ++k) { const float f = mfac(ml[k][0], Mx); Lx = fmaf(f, ml[k][1], Lx); ox += ok[k] * f; }
      asm volatile("s_waitcnt lgkmcnt(0)" : "+v"(ox) :: "memory");
      if (last) {
        const float inv = __builtin_amdgcn_rcpf(Lx);
        outp[((w * HQ + 2 * h + r) * D + 4 * ch) >> 2] = u32x2{cvtpk(ox[0] * inv, ox[1] * inv), cvtpk(ox[2] * inv, ox[3] * inv)};
        break;
      }
      lds_st(ctl + C_RFREE, kx + 1);
      if (early) { M = Mx; L = Lx; o = ox; }
      else {
        const unsigned so = W_PX + (blk * 16 + __builtin_ctz(m)) * PX_BYTES;
        __builtin_amdgcn_raw_buffer_store_b128(__builtin_bit_cast(u32x4, ox), prsrc, lane * 16, so, 16);
        const f32x4 mlv = {__builtin_bit_cast(float, rdl(__builtin_bit_cast(int, Mx), 0)), __builtin_bit_cast(float, rdl(__builtin_bit_cast(int, Lx), 0)),
                           __builtin_bit_cast(float, rdl(__builtin_bit_cast(int, Mx), 32)), __builtin_bit_cast(float, rdl(__builtin_bit_cast(int, Lx), 32))};
        if (lane < 8) __builtin_amdgcn_raw_buffer_store_b128(__builtin_bit_cast(u32x4, mlv), prsrc, 1024 + lane * 16, so, 16);
        m &= m - 1;
      }
    }
  }
}

extern "C" void kernel_launch(void* const* d_in, const int* in_sizes, int n_in, void* d_out, int out_size, void* d_ws, size_t ws_size, hipStream_t stream) {
  Args a;
  a.q = (const bf16x8*)d_in[0]; a.k = d_in[1]; a.v = d_in[2]; a.bt = (const int*)d_in[3]; a.lens = (const int*)d_in[4];
  a.out = (unsigned*)d_out; a.ws = (unsigned char*)d_ws; a.kv_bytes = (unsigned)in_sizes[1] * 2u; a.pad = 0;
  (void)hipFuncSetAttribute((const void*)radix_attn, hipFuncAttributeMaxDynamicSharedMemorySize, LDS_BYTES);
  hipLaunchKernelGGL(radix_attn, dim3(256), dim3(NTHR), LDS_BYTES, stream, a);
}
```
